```python
import torch

def _softmax(x, axis=-1):
    return torch.softmax(x, dim=axis)

def _gelu(x):
    return torch.nn.functional.gelu(x, approximate='tanh')

def _silu(x):
    return torch.nn.functional.silu(x)

def _sigmoid(x):
    return torch.sigmoid(x)

def _layernorm(x, axis=-1, eps=1e-05):
    m = x.mean(dim=axis, keepdim=True)
    v = x.var(dim=axis, keepdim=True, unbiased=False)
    return (x - m) / torch.sqrt(v + eps)

def _scatter_add(tgt, idx, src, axis=0):
    return tgt.clone().index_add_(axis, idx, src)

def _segment_sum(src, seg, n):
    out = torch.zeros((n,) + src.shape[1:], dtype=src.dtype)
    return out.index_add_(0, seg, src)

def _pad(x, pw):
    flat = tuple((v for pair in reversed(pw) for v in pair))
    return torch.nn.functional.pad(x, flat)
kernel_name = 'adaLN_cond__sliding_local_dilated__dense_activation_silu__prompt'

def setup_inputs():

    _w = lambda a: (lambda t: t.to(torch.bfloat16) if t.is_floating_point() else t)(torch.as_tensor(a))

    def _A_setup():
        B, S, D = (1, 2048, 640)
        return {'x': _w((lambda *s: torch.randn(*s, dtype=torch.bfloat16) * 0.05)(B, S, D)), 'wqkv': _w((lambda *s: torch.randn(*s, dtype=torch.bfloat16) * 0.05)(D, 3 * D)), 'wo': _w((lambda *s: torch.randn(*s, dtype=torch.bfloat16) * 0.05)(D, D))}
    _A_in = _A_setup()

    def _B_setup():
        B, S, D, F = (1, 2048, 640, 1536)
        return {'x': _w((lambda *s: torch.randn(*s, dtype=torch.bfloat16) * 0.05)(B, S, D)), 'w1': _w((lambda *s: torch.randn(*s, dtype=torch.bfloat16) * 3.2)(D, F) * D ** (-0.5)), 'w2': _w((lambda *s: torch.randn(*s, dtype=torch.bfloat16) * 0.4)(F, D) * F ** (-0.5))}
    _B_in = _B_setup()
    return {'A_x': _A_in['x'], 'A_wqkv': _A_in['wqkv'], 'A_wo': _A_in['wo'], 'B_x': _B_in['x'], 'B_w1': _B_in['w1'], 'B_w2': _B_in['w2'], 'c': _w((lambda *s: torch.randn(*s, dtype=torch.bfloat16) * 0.1)(1, 640) * 8.0), 'w_mod': _w((lambda *s: torch.randn(*s, dtype=torch.bfloat16) * 0.1)(640, 6 * 640) * 640 ** (-0.5) * 8.0)}

def reference(A_x, A_wqkv, A_wo, B_x, B_w1, B_w2, c, w_mod):

    def _A_ref(x, wqkv, wo):
        B, S, D = x.shape
        H = 32
        Dh = D // H
        W, dil = (8, 2)
        qkv = (lambda x, *d: x.permute(*d))((x @ wqkv).reshape(B, S, 3, H, Dh), 2, 0, 3, 1, 4)
        q, k, v = (qkv[0], qkv[1], qkv[2])
        scale = Dh ** (-0.5)
        pos = torch.arange(S)
        off = torch.arange(W) * dil
        idx = pos[:, None] - off[None, :]
        mask = idx >= 0
        idx_c = torch.where(mask, idx, 0).reshape(-1)
        kg = k[:, :, idx_c, :].reshape(B, H, S, W, Dh)
        vg = v[:, :, idx_c, :].reshape(B, H, S, W, Dh)
        logits = torch.einsum('bhsd,bhswd->bhsw', q, kg) * scale
        logits = torch.where(mask[None, None, :, :], logits, -1000000000.0)
        attn = _softmax(logits, axis=-1)
        o = torch.einsum('bhsw,bhswd->bhsd', attn, vg)
        return (lambda x, *d: x.permute(*d))(o, 0, 2, 1, 3).reshape(B, S, D) @ wo

    def _B_ref(x, w1, w2):
        h = torch.einsum('bsd,df->bsf', x, w1)
        h = _silu(h)
        out = torch.einsum('bsf,fd->bsd', h, w2)
        return out
    x = A_x
    mod = (_silu(c) @ w_mod).reshape(c.shape[0], 6, 1, -1)
    sa, ca, ga, sb, cb, gb = (mod[:, 0].clone(), mod[:, 1].clone(), mod[:, 2].clone(), mod[:, 3].clone(), mod[:, 4].clone(), mod[:, 5].clone())
    h = _layernorm(x, axis=-1) * (1 + ca) + sa
    x = x + ga * _A_ref(h, A_wqkv, A_wo)
    h = _layernorm(x, axis=-1) * (1 + cb) + sb
    x = x + gb * _B_ref(h, B_w1, B_w2)
    return x
```

```cpp
#include <hip/hip_runtime.h>
#include <hip/hip_bf16.h>
typedef __bf16 bfx8 __attribute__((ext_vector_type(8)));
typedef __bf16 bfx4 __attribute__((ext_vector_type(4)));
typedef short s16x4 __attribute__((ext_vector_type(4)));
typedef float f32x4 __attribute__((ext_vector_type(4)));
typedef unsigned u32x4 __attribute__((ext_vector_type(4)));
typedef unsigned short u16;
constexpr int S = 2048, D = 640, F = 1536;
__device__ __forceinline__ float bf2f(u16 v) { return __uint_as_float((unsigned)v << 16); }
__device__ __forceinline__ u16 f2bf(float f) { return __builtin_bit_cast(u16, (__bf16)f); }
__device__ __forceinline__ float rb(float f) { return (float)(__bf16)f; }

__global__ __launch_bounds__(256) void k_mod(const u16* c, const u16* wm, float* mod) {
  __shared__ float red[16][17];
  const int t = threadIdx.x, col = blockIdx.x * 16 + (t & 15), kg = t >> 4;
  float a = 0;
  for (int k = kg; k < D; k += 16) {
    float cv = bf2f(c[k]);
    float s = rb(cv / (1.f + __expf(-cv)));
    a += s * bf2f(wm[k * 3840 + col]);
  }
  red[kg][t & 15] = a;
  __syncthreads();
  if (t < 16) { float s = 0; for (int i = 0; i < 16; ++i) s += red[i][t]; mod[col] = rb(s); }
}

__global__ __launch_bounds__(256) void k_ln(const u16* x, const float* sh, const float* sc, u16* h) {
  const int lane = threadIdx.x & 63, row = blockIdx.x * 4 + (threadIdx.x >> 6);
  float v[10], s = 0;
  for (int i = 0; i < 10; ++i) { v[i] = bf2f(x[row * D + i * 64 + lane]); s += v[i]; }
  for (int o = 32; o; o >>= 1) s += __shfl_xor(s, o);
  float m = s / D, q = 0;
  for (int i = 0; i < 10; ++i) { float d = v[i] - m; q += d * d; }
  for (int o = 32; o; o >>= 1) q += __shfl_xor(q, o);
  float r = rsqrtf(q / D + 1e-5f);
  for (int i = 0; i < 10; ++i) { int cc = i * 64 + lane; h[row * D + cc] = f2bf((v[i] - m) * r * (1.f + sc[cc]) + sh[cc]); }
}

__global__ __launch_bounds__(256) void k_attn(const u16* qkv, u16* o) {
  const int id = blockIdx.x * 256 + threadIdx.x, s = id >> 5, hh = id & 31;
  float q[20], l[8], acc[20];
  for (int d = 0; d < 20; ++d) { q[d] = bf2f(qkv[s * 1920 + hh * 20 + d]); acc[d] = 0; }
  float mx = -3e38f;
#pragma unroll
  for (int w = 0; w < 8; ++w) {
    int p = s - 2 * w; float a = 0;
    if (p >= 0) { for (int d = 0; d < 20; ++d) a += q[d] * bf2f(qkv[p * 1920 + 640 + hh * 20 + d]); a *= 0.22360679775f; } else a = -1e9f;
    l[w] = a; mx = fmaxf(mx, a);
  }
  float sum = 0;
#pragma unroll
  for (int w = 0; w < 8; ++w) { l[w] = __expf(l[w] - mx); sum += l[w]; }
#pragma unroll
  for (int w = 0; w < 8; ++w) {
    int p = max(s - 2 * w, 0); float a = l[w] / sum;
    for (int d = 0; d < 20; ++d) acc[d] += a * bf2f(qkv[p * 1920 + 1280 + hh * 20 + d]);
  }
  for (int d = 0; d < 20; ++d) o[s * D + hh * 20 + d] = f2bf(acc[d]);
}

template <int MODE>
__global__ __launch_bounds__(256) void k_gemm(const u16* A, const u16* B, u16* C, const u16* R, const float* g, int N, int K) {
  __shared__ __attribute__((aligned(16))) u16 As[64][72];
  __shared__ __attribute__((aligned(16))) u16 Bs[64][72];
  const int t = threadIdx.x, lane = t & 63, w = t >> 6, wm = w >> 1, wn = w & 1;
  const int m0 = blockIdx.y * 64, n0 = blockIdx.x * 64;
  f32x4 acc[2][2] = {};
  const int lr = t >> 3, lc = (t & 7) * 8;
  u32x4 ra0 = *(const u32x4*)(A + (m0 + lr) * K + lc), ra1 = *(const u32x4*)(A + (m0 + lr + 32) * K + lc);
  u32x4 rb0 = *(const u32x4*)(B + lr * N + n0 + lc), rb1 = *(const u32x4*)(B + (lr + 32) * N + n0 + lc);
  for (int k0 = 0; k0 < K; k0 += 64) {
    __syncthreads();
    *(u32x4*)&As[lr][lc] = ra0; *(u32x4*)&As[lr + 32][lc] = ra1;
    *(u32x4*)&Bs[lr][lc] = rb0; *(u32x4*)&Bs[lr + 32][lc] = rb1;
    __syncthreads();
    if (k0 + 64 < K) {
      ra0 = *(const u32x4*)(A + (m0 + lr) * K + k0 + 64 + lc); ra1 = *(const u32x4*)(A + (m0 + lr + 32) * K + k0 + 64 + lc);
      rb0 = *(const u32x4*)(B + (k0 + 64 + lr) * N + n0 + lc); rb1 = *(const u32x4*)(B + (k0 + 64 + lr + 32) * N + n0 + lc);
    }
#pragma unroll
    for (int kk = 0; kk < 2; ++kk) {
      bfx8 a[2], b[2];
#pragma unroll
      for (int i = 0; i < 2; ++i) a[i] = *(const bfx8*)&As[wm * 32 + i * 16 + (lane & 15)][kk * 32 + (lane >> 4) * 8];
#pragma unroll
      for (int i = 0; i < 2; ++i) {
        const u16* p = &Bs[kk * 32 + (lane >> 4) * 8 + ((lane & 15) >> 2)][wn * 32 + i * 16 + (lane & 3) * 4];
        s16x4 lo = __builtin_amdgcn_ds_read_tr16_b64_v4i16((__attribute__((address_space(3))) s16x4*)p);
        s16x4 hi = __builtin_amdgcn_ds_read_tr16_b64_v4i16((__attribute__((address_space(3))) s16x4*)(p + 4 * 72));
        typedef short s16x8 __attribute__((ext_vector_type(8)));
        s16x8 c8 = __builtin_shufflevector(lo, hi, 0, 1, 2, 3, 4, 5, 6, 7);
        b[i] = __builtin_bit_cast(bfx8, c8);
      }
#pragma unroll
      for (int i = 0; i < 2; ++i)
#pragma unroll
        for (int j = 0; j < 2; ++j) acc[i][j] = __builtin_amdgcn_mfma_f32_16x16x32_bf16(b[j], a[i], acc[i][j], 0, 0, 0);
    }
  }
#pragma unroll
  for (int i = 0; i < 2; ++i)
#pragma unroll
    for (int j = 0; j < 2; ++j) {
      const int row = m0 + wm * 32 + i * 16 + (lane & 15), col = n0 + wn * 32 + j * 16 + (lane >> 4) * 4;
      u16 o[4];
#pragma unroll
      for (int r = 0; r < 4; ++r) {
        float v = acc[i][j][r];
        if (MODE == 1) v = bf2f(R[row * N + col + r]) + g[col + r] * v;
        if (MODE == 2) v = v / (1.f + __expf(-v));
        o[r] = f2bf(v);
      }
      *(uint2*)(C + row * N + col) = make_uint2(o[0] | (o[1] << 16), o[2] | (o[3] << 16));
    }
}

extern "C" void kernel_launch(void* const* d_in, const int* in_sizes, int n_in, void* d_out, int out_size, void* d_ws, size_t ws_size, hipStream_t stream) {
  const u16 *x = (const u16*)d_in[0], *wqkv = (const u16*)d_in[1], *wo = (const u16*)d_in[2], *w1 = (const u16*)d_in[4], *w2 = (const u16*)d_in[5], *c = (const u16*)d_in[6], *wm = (const u16*)d_in[7];
  char* ws = (char*)d_ws;
  float* mod = (float*)ws;
  u16 *h = (u16*)(ws + (1 << 20)), *qkv = (u16*)(ws + (4 << 20)), *o = (u16*)(ws + (16 << 20)), *x1 = (u16*)(ws + (20 << 20)), *hh = (u16*)(ws + (24 << 20));
  k_mod<<<240, 256, 0, stream>>>(c, wm, mod);
  k_ln<<<512, 256, 0, stream>>>(x, mod, mod + 640, h);
  k_gemm<0><<<dim3(30, 32), 256, 0, stream>>>(h, wqkv, qkv, nullptr, nullptr, 1920, 640);
  k_attn<<<256, 256, 0, stream>>>(qkv, o);
  k_gemm<1><<<dim3(10, 32), 256, 0, stream>>>(o, wo, x1, x, mod + 1280, 640, 640);
  k_ln<<<512, 256, 0, stream>>>(x1, mod + 1920, mod + 2560, h);
  k_gemm<2><<<dim3(24, 32), 256, 0, stream>>>(h, w1, hh, nullptr, nullptr, 1536, 640);
  k_gemm<1><<<dim3(10, 32), 256, 0, stream>>>(hh, w2, (u16*)d_out, x1, mod + 3200, 640, 1536);
}
```

```cpp
#include <hip/hip_runtime.h>
struct Srd { const void* p; unsigned n; unsigned cfg; };
struct Args { Srd x, wm, c, ws, w1, out, wqkv, wo, w2; };
__global__ __launch_bounds__(256, 1) void fused_block(Args args) {
  __shared__ __attribute__((aligned(16))) char lds[163840];
  asm volatile(
    "s_getpc_b64 s[32:33]\n"
    ".Lbase_%=:\n"
    "s_load_dwordx16 s[4:19], s[0:1], 0\n"
    "s_load_dwordx8 s[20:27], s[0:1], 64\n"
    "s_load_dwordx8 s[80:87], s[0:1], 96\n"
    "s_load_dwordx4 s[28:31], s[0:1], 128\n"
    "s_mov_b32 s34, .Lend_%=-.Lbase_%=\n"
    "s_mov_b32 s35, 0x20000\n"
    "v_lshrrev_b32 v33, 6, v0\n"
    "s_lshr_b32 s92, s2, 3\n"
    "s_mul_i32 s92, s92, 0x700\n"
    "v_lshl_add_u32 v24, v0, 7, s92\n"
    "v_cmp_gt_u32 vcc, 14, v0\n"
    "s_nop 1\n"
    "v_cndmask_b32 v32, 0x40000000, v24, vcc\n"
    "buffer_load_dword v254, v32, s[32:35], 0 offen\n"
    "v_readfirstlane_b32 s36, v33\n"
    "v_and_b32 v1, 63, v0\n"
    "s_and_b32 s41, s2, 7\n"
    "s_bfe_u32 s37, s2, 0x30003\n"
    "s_lshr_b32 s60, s2, 6\n"
    "s_sub_u32 s92, 3, s60\n"
    "s_lshl_b32 s92, s92, 2\n"
    "s_add_u32 s92, s92, s36\n"
    "s_lshl_b32 s92, s92, 3\n"
    "s_add_u32 s39, s92, s41\n"
    "s_lshl_b32 s92, s41, 2\n"
    "s_add_u32 s38, s92, s60\n"
    "s_lshr_b32 s92, s38, 1\n"
    "s_lshl_b32 s92, s92, 7\n"
    "s_and_b32 s93, s38, 1\n"
    "s_add_u32 s92, s92, s93\n"
    "s_mul_i32 s44, s92, 0x500\n"
    "v_mov_b32 v33, 0\n"
    "v_lshlrev_b32 v2, 4, v1\n"
    "v_lshlrev_b32 v34, 2, v1\n"
    "s_mul_i32 s92, s36, 0xb400\n"
    "s_add_u32 s92, s44, s92\n"
    "s_add_u32 s90, s92, 0xffffb000\n"
    "s_cmp_lt_u32 s60, 2\n"
    "s_cbranch_scc1 .Lgp_%=\n"
    "s_mul_i32 s92, s39, 0xcd\n"
    "s_lshr_b32 s61, s92, 12\n"
    "s_mul_i32 s92, s61, 20\n"
    "s_sub_u32 s63, s39, s92\n"
    "s_mul_i32 s92, s63, 52\n"
    "s_lshr_b32 s68, s92, 8\n"
    "s_mul_i32 s92, s68, 5\n"
    "s_sub_u32 s69, s63, s92\n"
    "s_mul_i32 s92, s68, 0xa0\n"
    "s_mul_i32 s93, s69, 40\n"
    "s_cmp_eq_u32 s69, 4\n"
    "s_cselect_b32 s93, 16, s93\n"
    "s_add_u32 s64, s92, s93\n"
    "s_cmp_eq_u32 s69, 4\n"
    "s_cselect_b32 s65, 40, 20\n"
    "s_cselect_b32 s66, 20, 4\n"
    "s_cselect_b32 s67, 0x50, 8\n"
    "s_lshl_b32 s92, s63, 5\n"
    "s_cmp_eq_u32 s61, 3\n"
    "s_cselect_b32 s64, s64, s92\n"
    "s_cselect_b32 s65, s65, 16\n"
    "s_cselect_b32 s66, s66, 8\n"
    "s_cselect_b32 s67, s67, 4\n"
    "s_mov_b32 s92, 0xf00\n"
    "s_cmp_lt_u32 s61, 3\n"
    "s_cselect_b32 s68, s92, 0x500\n"
    "s_mul_i32 s92, s61, 0x500\n"
    "s_cmp_lt_u32 s61, 3\n"
    "s_cselect_b32 s92, s92, 0\n"
    "s_mul_i32 s93, s37, 0xa0\n"
    "s_add_u32 s69, s92, s93\n"
    "v_lshrrev_b32 v24, 5, v1\n"
    "v_bfe_u32 v25, v1, 4, 1\n"
    "v_bfe_u32 v26, v1, 3, 1\n"
    "v_bfe_u32 v27, v1, 1, 2\n"
    "v_add_u32 v27, s64, v27\n"
    "v_mad_u32_u24 v26, v26, s67, v27\n"
    "v_mad_u32_u24 v25, v25, s66, v26\n"
    "v_mad_u32_u24 v24, v24, s65, v25\n"
    "v_and_b32 v25, 1, v1\n"
    "v_lshlrev_b32 v25, 4, v25\n"
    "v_mad_u32_u24 v32, v24, s68, v25\n"
    "s_cmp_eq_u32 s61, 3\n"
    "s_waitcnt lgkmcnt(0)\n"
    "s_cselect_b64 s[80:81], s[84:85], s[80:81]\n"
    "s_cselect_b32 s82, s86, s82\n"
    "buffer_load_dwordx4 a[0:3], v32, s[80:83], s69 offen\n"
    "buffer_load_dwordx4 a[4:7], v32, s[80:83], s69 offen offset:32\n"
    "buffer_load_dwordx4 a[8:11], v32, s[80:83], s69 offen offset:64\n"
    "buffer_load_dwordx4 a[12:15], v32, s[80:83], s69 offen offset:96\n"
    "buffer_load_dwordx4 a[16:19], v32, s[80:83], s69 offen offset:128\n"
    "buffer_load_dword v253, v33, s[16:19], 0 offen sc1\n"
    "s_mul_i32 s92, s36, 0x3c00\n"
    "s_add_u32 s78, s92, 0x17100\n"
    "v_and_b32 v24, 1, v1\n"
    "s_max_i32 s91, s90, 0\n"
    "buffer_load_dwordx4 v[46:49], v2, s[4:7], s91 offen\n"
    "v_lshrrev_b32 v25, 1, v1\n"
    "v_mul_u32_u24 v25, 0xa0, v25\n"
    "v_add_u32 v25, s78, v25\n"
    "v_lshl_add_u32 v16, v24, 4, v25\n"
    "v_lshrrev_b32 v24, 4, v1\n"
    "v_bfe_u32 v25, v1, 2, 2\n"
    "v_lshl_add_u32 v24, v24, 3, v25\n"
    "v_mul_u32_u24 v24, 0xa0, v24\n"
    "v_add_u32 v35, s78, v24\n"
    "v_and_b32 v24, 3, v1\n"
    "s_cmp_lt_u32 s61, 3\n"
    "s_cselect_b32 s92, 8, 16\n"
    "buffer_load_dword v45, v34, s[4:7], s91 offen offset:1024\n"
    "v_mad_u32_u24 v36, v24, s92, v35\n"
    "s_cmp_lt_u32 s61, 3\n"
    "s_cselect_b32 s92, 40, 8\n"
    "v_add_u32 v37, s92, v36\n"
    "s_cmp_lt_u32 s61, 3\n"
    "s_cselect_b32 s79, 0x50, 64\n"
    "v_add_u32 v38, s79, v36\n"
    "v_add_u32 v39, s79, v37\n"
    "v_and_b32 v24, 1, v1\n"
    "s_cmp_lt_u32 s61, 3\n"
    "s_cselect_b32 s92, 40, 16\n"
    "buffer_load_dwordx4 v[56:59], v2, s[4:7], s91 offen offset:2560\n"
    "v_bfe_u32 v25, v1, 1, 1\n"
    "s_cmp_lt_u32 s61, 3\n"
    "s_cselect_b32 s93, 0x50, 8\n"
    "s_cmp_lt_u32 s61, 3\n"
    "s_cselect_b32 s94, 32, 0x80\n"
    "v_add_u32 v26, s94, v35\n"
    "v_mad_u32_u24 v25, v25, s93, v26\n"
    "v_mad_u32_u24 v35, v24, s92, v25\n"
    "s_mul_i32 s92, s37, 0x50\n"
    "buffer_load_dword v55, v34, s[4:7], s91 offen offset:3584\n"
    "s_add_u32 s92, s92, s63\n"
    "s_mul_i32 s92, s92, 0xf00\n"
    "s_lshl_b32 s93, s61, 10\n"
    "s_add_u32 s92, s92, s93\n"
    "s_add_u32 s92, s92, 0x100000\n"
    "s_mul_i32 s93, s37, 20\n"
    "s_add_u32 s93, s93, s63\n"
    "s_mul_i32 s93, s93, 0x1400\n"
    "s_add_u32 s93, s93, 0x360000\n"
    "s_cmp_lt_u32 s61, 3\n"
    "s_cselect_b32 s80, s92, s93\n"
    "s_add_u32 s90, s90, 5120\n"
    "s_max_i32 s91, s90, 0\n"
    "buffer_load_dwordx4 v[66:69], v2, s[4:7], s91 offen\n"
    "s_mov_b32 s92, 0x12c00\n"
    "s_cmp_lt_u32 s61, 3\n"
    "s_cselect_b32 s79, s92, 0x400\n"
    "s_add_u32 s81, s80, s79\n"
    "s_add_u32 s82, s81, s79\n"
    "s_add_u32 s83, s82, s79\n"
    "v_bfe_u32 v24, v1, 2, 2\n"
    "s_cmp_lt_u32 s61, 3\n"
    "s_cselect_b32 s92, 0x12c00, 64\n"
    "v_lshrrev_b32 v25, 4, v1\n"
    "s_cmp_lt_u32 s61, 3\n"
    "s_cselect_b32 s93, 64, 0x100\n"
    "buffer_load_dword v65, v34, s[4:7], s91 offen offset:1024\n"
    "v_and_b32 v26, 3, v1\n"
    "s_mul_i32 s94, s61, 0x300\n"
    "s_sub_u32 s94, 0xc00, s94\n"
    "s_cmp_lt_u32 s61, 3\n"
    "s_cselect_b32 s94, s94, 0x1000\n"
    "v_lshl_add_u32 v26, v26, 4, s94\n"
    "v_mad_u32_u24 v25, v25, s93, v26\n"
    "v_mad_u32_u24 v23, v24, s92, v25\n"
    "s_waitcnt vmcnt(11)\n"
    "ds_write_b128 v16, a[0:3]\n"
    "s_waitcnt vmcnt(10)\n"
    "ds_write_b128 v16, a[4:7] offset:32\n"
    "s_waitcnt vmcnt(9)\n"
    "ds_write_b128 v16, a[8:11] offset:64\n"
    "s_waitcnt vmcnt(8)\n"
    "ds_write_b128 v16, a[12:15] offset:96\n"
    "s_waitcnt vmcnt(7)\n"
    "ds_write_b128 v16, a[16:19] offset:128\n"
    "ds_read_b64_tr_b16 a[216:217], v36\n"
    "ds_read_b64_tr_b16 a[218:219], v36 offset:640\n"
    "ds_read_b64_tr_b16 a[220:221], v37\n"
    "ds_read_b64_tr_b16 a[222:223], v37 offset:640\n"
    "ds_read_b64_tr_b16 a[224:225], v38\n"
    "ds_read_b64_tr_b16 a[226:227], v38 offset:640\n"
    "ds_read_b64_tr_b16 a[228:229], v39\n"
    "ds_read_b64_tr_b16 a[230:231], v39 offset:640\n"
    "ds_read_b64_tr_b16 a[232:233], v35\n"
    "ds_read_b64_tr_b16 a[234:235], v35 offset:640\n"
    "s_waitcnt lgkmcnt(8)\n"
    "buffer_store_dwordx4 a[216:219], v2, s[16:19], s80 offen sc1\n"
    "s_waitcnt lgkmcnt(6)\n"
    "buffer_store_dwordx4 a[220:223], v2, s[16:19], s81 offen sc1\n"
    "s_waitcnt lgkmcnt(4)\n"
    "buffer_store_dwordx4 a[224:227], v2, s[16:19], s82 offen sc1\n"
    "s_waitcnt lgkmcnt(2)\n"
    "buffer_store_dwordx4 a[228:231], v2, s[16:19], s83 offen sc1\n"
    "s_waitcnt lgkmcnt(0)\n"
    "buffer_store_dwordx4 a[232:235], v23, s[16:19], s80 offen sc1\n"
    "s_add_u32 s62, s39, 64\n"
    "buffer_load_dwordx4 v[76:79], v2, s[4:7], s91 offen offset:2560\n"
    "s_mul_i32 s92, s62, 0xcd\n"
    "s_lshr_b32 s61, s92, 12\n"
    "s_min_u32 s61, s61, 4\n"
    "s_mul_i32 s92, s61, 20\n"
    "s_sub_u32 s63, s62, s92\n"
    "s_mul_i32 s92, s63, 52\n"
    "s_lshr_b32 s68, s92, 8\n"
    "s_mul_i32 s92, s68, 5\n"
    "s_sub_u32 s69, s63, s92\n"
    "buffer_load_dword v75, v34, s[4:7], s91 offen offset:3584\n"
    "s_mul_i32 s92, s68, 0xa0\n"
    "s_mul_i32 s93, s69, 40\n"
    "s_cmp_eq_u32 s69, 4\n"
    "s_cselect_b32 s93, 16, s93\n"
    "s_add_u32 s64, s92, s93\n"
    "s_cmp_eq_u32 s69, 4\n"
    "s_cselect_b32 s65, 40, 20\n"
    "s_cselect_b32 s66, 20, 4\n"
    "s_cselect_b32 s67, 0x50, 8\n"
    "s_add_u32 s90, s90, 5120\n"
    "s_max_i32 s91, s90, 0\n"
    "buffer_load_dwordx4 v[86:89], v2, s[4:7], s91 offen\n"
    "s_lshl_b32 s92, s63, 5\n"
    "s_cmp_eq_u32 s61, 3\n"
    "s_cselect_b32 s64, s64, s92\n"
    "s_cselect_b32 s65, s65, 16\n"
    "s_cselect_b32 s66, s66, 8\n"
    "s_cselect_b32 s67, s67, 4\n"
    "s_mov_b32 s68, 0x500\n"
    "buffer_load_dword v85, v34, s[4:7], s91 offen offset:1024\n"
    "s_mul_i32 s69, s37, 0xa0\n"
    "v_lshrrev_b32 v24, 5, v1\n"
    "v_bfe_u32 v25, v1, 4, 1\n"
    "v_bfe_u32 v26, v1, 3, 1\n"
    "v_bfe_u32 v27, v1, 1, 2\n"
    "v_add_u32 v27, s64, v27\n"
    "v_mad_u32_u24 v26, v26, s67, v27\n"
    "v_mad_u32_u24 v25, v25, s66, v26\n"
    "buffer_load_dwordx4 v[96:99], v2, s[4:7], s91 offen offset:2560\n"
    "v_mad_u32_u24 v24, v24, s65, v25\n"
    "v_and_b32 v25, 1, v1\n"
    "v_lshlrev_b32 v25, 4, v25\n"
    "v_mad_u32_u24 v32, v24, s68, v25\n"
    "s_cmp_eq_u32 s61, 3\n"
    "s_cselect_b64 s[28:29], s[84:85], s[28:29]\n"
    "s_cselect_b32 s30, s86, s30\n"
    "s_add_u32 s92, s39, -8\n"
    "buffer_load_dword v95, v34, s[4:7], s91 offen offset:3584\n"
    "s_cmp_lt_u32 s39, 8\n"
    "s_cselect_b32 s73, 0, s92\n"
    "s_mul_i32 s92, s73, 43\n"
    "s_lshr_b32 s74, s92, 8\n"
    "s_mul_i32 s92, s74, 6\n"
    "s_sub_u32 s75, s73, s92\n"
    "s_mul_i32 s92, s74, 52\n"
    "s_lshr_b32 s76, s92, 8\n"
    "s_mul_i32 s92, s76, 5\n"
    "s_add_u32 s90, s90, 5120\n"
    "s_max_i32 s91, s90, 0\n"
    "buffer_load_dwordx4 v[106:109], v2, s[4:7], s91 offen\n"
    "s_sub_u32 s77, s74, s92\n"
    "s_mul_i32 s92, s76, 0xa0\n"
    "s_lshr_b32 s93, s77, 1\n"
    "s_mul_i32 s93, s93, 0x50\n"
    "s_and_b32 s94, s77, 1\n"
    "s_lshl_b32 s94, s94, 5\n"
    "s_add_u32 s93, s93, s94\n"
    "s_cmp_eq_u32 s77, 4\n"
    "s_cselect_b32 s93, 64, s93\n"
    "buffer_load_dword v105, v34, s[4:7], s91 offen offset:1024\n"
    "s_add_u32 s92, s92, s93\n"
    "s_mul_i32 s92, s92, 0xc00\n"
    "s_mul_i32 s93, s37, 0x180\n"
    "s_add_u32 s92, s92, s93\n"
    "s_lshl_b32 s93, s75, 6\n"
    "s_add_u32 s76, s92, s93\n"
    "s_mov_b32 s92, 0x3c000\n"
    "buffer_load_dwordx4 v[116:119], v2, s[4:7], s91 offen offset:2560\n"
    "s_cmp_eq_u32 s77, 4\n"
    "s_cselect_b32 s92, s92, 0xc000\n"
    "s_add_u32 s77, s92, s76\n"
    "v_and_b32 v24, 3, v1\n"
    "v_lshrrev_b32 v25, 2, v1\n"
    "v_mul_u32_u24 v25, 0xc00, v25\n"
    "v_lshl_add_u32 v11, v24, 4, v25\n"
    "s_mov_b32 s42, 0x40000\n"
    "buffer_load_dword v115, v34, s[4:7], s91 offen offset:3584\n"
    "s_mov_b32 s43, 0x3f803f80\n"
    "s_lshl_b32 s92, s37, 9\n"
    "s_lshl_b32 s93, s39, 2\n"
    "s_add_u32 s92, s92, s93\n"
    "s_add_u32 s92, s92, 0x1000\n"
    "v_lshl_add_u32 v24, v1, 12, s92\n"
    "s_lshl_b32 s92, s38, 7\n"
    "s_lshl_b32 s93, s37, 4\n"
    "s_add_u32 s90, s90, 5120\n"
    "buffer_load_dwordx4 v[126:129], v2, s[4:7], s90 offen\n"
    "s_add_u32 s92, s92, s93\n"
    "s_lshl_b32 s93, s36, 2\n"
    "s_add_u32 s92, s92, s93\n"
    "s_add_u32 s92, s92, 0xa0000\n"
    "v_mov_b32 v25, s92\n"
    "v_cmp_eq_u32 vcc, 8, v1\n"
    "s_nop 1\n"
    "v_cndmask_b32 v25, 0x40000000, v25, vcc\n"
    "v_cmp_gt_u32 vcc, 8, v1\n"
    "buffer_load_dword v125, v34, s[4:7], s90 offen offset:1024\n"
    "s_nop 0\n"
    "v_cndmask_b32 v9, v25, v24, vcc\n"
    "s_lshl_b32 s92, s38, 2\n"
    "s_lshr_b32 s93, s37, 1\n"
    "s_add_u32 s92, s92, s93\n"
    "s_mul_i32 s92, s92, 0x5000\n"
    "s_add_u32 s49, s92, 0x800000\n"
    "v_lshrrev_b32 v24, 5, v1\n"
    "v_mul_u32_u24 v24, 0x1400, v24\n"
    "v_and_b32 v25, 31, v1\n"
    "buffer_load_dwordx4 v[136:139], v2, s[4:7], s90 offen offset:2560\n"
    "s_and_b32 s92, s37, 1\n"
    "s_lshl_b32 s92, s92, 11\n"
    "s_add_u32 s92, s49, s92\n"
    "s_lshl_b32 s93, s36, 9\n"
    "s_add_u32 s92, s92, s93\n"
    "v_lshl_add_u32 v25, v25, 4, s92\n"
    "v_add_u32 v7, v24, v25\n"
    "v_lshrrev_b32 v24, 4, v1\n"
    "buffer_load_dword v135, v34, s[4:7], s90 offen offset:3584\n"
    "v_mul_u32_u24 v24, 0x1400, v24\n"
    "v_and_b32 v25, 15, v1\n"
    "s_and_b32 s92, s37, 1\n"
    "s_lshl_b32 s92, s92, 9\n"
    "s_add_u32 s92, s49, s92\n"
    "s_lshl_b32 s93, s36, 7\n"
    "s_add_u32 s92, s92, s93\n"
    "s_add_u32 s92, s92, 0x1000\n"
    "s_add_u32 s90, s90, 5120\n"
    "buffer_load_dwordx4 v[146:149], v2, s[4:7], s90 offen\n"
    "v_lshl_add_u32 v25, v25, 3, s92\n"
    "v_add_u32 v8, v24, v25\n"
    "s_mov_b32 s50, 0x2800\n"
    "v_accvgpr_write_b32 a244, -1\n"
    "v_accvgpr_write_b32 a245, -1\n"
    "v_accvgpr_write_b32 a246, -1\n"
    "v_accvgpr_write_b32 a247, -1\n"
    "v_and_b32 v24, 3, v1\n"
    "buffer_load_dword v145, v34, s[4:7], s90 offen offset:1024\n"
    "v_lshrrev_b32 v25, 2, v1\n"
    "v_mul_u32_u24 v25, 0x50, v25\n"
    "v_add_u32 v25, s78, v25\n"
    "v_lshl_add_u32 v22, v24, 4, v25\n"
    "v_and_b32 v24, 1, v1\n"
    "v_bfe_u32 v25, v1, 1, 1\n"
    "s_cmp_lt_u32 s75, 4\n"
    "s_cselect_b32 s92, 32, 8\n"
    "buffer_load_dwordx4 v[156:159], v2, s[4:7], s90 offen offset:2560\n"
    "v_lshrrev_b32 v26, 4, v1\n"
    "v_bfe_u32 v27, v1, 2, 2\n"
    "v_lshl_add_u32 v26, v26, 3, v27\n"
    "v_mul_u32_u24 v26, 0x50, v26\n"
    "v_add_u32 v26, s78, v26\n"
    "v_mad_u32_u24 v25, v25, s92, v26\n"
    "v_lshl_add_u32 v21, v24, 4, v25\n"
    "s_cmp_lt_u32 s75, 4\n"
    "s_cselect_b32 s92, 8, 32\n"
    "buffer_load_dword v155, v34, s[4:7], s90 offen offset:3584\n"
    "v_add_u32 v20, s92, v21\n"
    "s_mul_i32 s92, s37, 0x50\n"
    "s_add_u32 s92, s92, s74\n"
    "s_mul_i32 s92, s92, 0xc00\n"
    "s_mul_i32 s93, s75, 0xf000\n"
    "s_add_u32 s93, s93, 0x610000\n"
    "s_mul_i32 s94, s75, 0x1e000\n"
    "s_add_u32 s94, s94, 0x598800\n"
    "s_add_u32 s90, s90, 5120\n"
    "buffer_load_dwordx4 v[166:169], v2, s[4:7], s90 offen\n"
    "s_cmp_lt_u32 s75, 4\n"
    "s_cselect_b32 s93, s93, s94\n"
    "s_add_u32 s92, s92, s93\n"
    "s_cmp_lt_u32 s39, 8\n"
    "s_cselect_b32 s12, 0x40000000, s92\n"
    "s_mov_b32 s92, 0x400\n"
    "s_cmp_lt_u32 s75, 4\n"
    "s_cselect_b32 s92, s92, 0xf000\n"
    "buffer_load_dword v165, v34, s[4:7], s90 offen offset:1024\n"
    "s_add_u32 s13, s12, s92\n"
    "s_mul_i32 s92, s41, 0x3c00\n"
    "s_add_u32 s45, s92, 0x20000\n"
    "s_add_u32 s46, s45, 0x1000\n"
    "v_lshlrev_b32 v6, 4, v0\n"
    "v_cmp_gt_u32 vcc, 60, v1\n"
    "s_nop 1\n"
    "v_cndmask_b32 v24, 59, v1, vcc\n"
    "s_lshl_b32 s92, s41, 12\n"
    "buffer_load_dwordx4 v[176:179], v2, s[4:7], s90 offen offset:2560\n"
    "s_lshl_b32 s93, s37, 9\n"
    "s_add_u32 s92, s92, s93\n"
    "s_add_u32 s92, s92, 0x1000\n"
    "v_lshl_add_u32 v10, v24, 2, s92\n"
    "s_lshl_b32 s92, s38, 3\n"
    "s_add_u32 s92, s92, s37\n"
    "s_lshl_b32 s92, s92, 2\n"
    "s_add_u32 s87, s92, s36\n"
    "buffer_load_dword v175, v34, s[4:7], s90 offen offset:3584\n"
    "s_mul_i32 s92, s87, 0xa00\n"
    "s_add_u32 s32, s92, 0x800000\n"
    "s_add_u32 s33, s32, 0x280000\n"
    "s_waitcnt vmcnt(33)\n"
    "v_readfirstlane_b32 s40, v253\n"
    "s_nop 1\n"
    "s_mul_i32 s40, s40, 0x9E3779B1\n"
    "s_add_u32 s40, s40, 0x7F4A7C15\n"
    "v_mov_b32 v3, s40\n"
    "s_waitcnt vmcnt(22)\n"
    "buffer_store_dword v3, v9, s[16:19], 0 offen sc1\n"
    "v_lshrrev_b32 v24, 4, v1\n"
    "s_add_u32 s90, s90, 5120\n"
    "buffer_load_dwordx4 v[186:189], v2, s[4:7], s90 offen\n"
    "v_bfe_u32 v25, v1, 2, 2\n"
    "v_lshl_add_u32 v24, v24, 3, v25\n"
    "v_mul_u32_u24 v24, 0xa0, v24\n"
    "v_add_u32 v35, s78, v24\n"
    "buffer_load_dword v185, v34, s[4:7], s90 offen offset:1024\n"
    "v_and_b32 v24, 3, v1\n"
    "v_lshl_add_u32 v36, v24, 4, v35\n"
    "v_add_u32 v37, 8, v36\n"
    "s_mov_b32 s79, 64\n"
    "buffer_load_dwordx4 v[196:199], v2, s[4:7], s90 offen offset:2560\n"
    "v_add_u32 v38, s79, v36\n"
    "v_add_u32 v39, s79, v37\n"
    "v_and_b32 v24, 1, v1\n"
    "v_bfe_u32 v25, v1, 1, 1\n"
    "buffer_load_dword v195, v34, s[4:7], s90 offen offset:3584\n"
    "v_add_u32 v26, 0x80, v35\n"
    "v_lshl_add_u32 v25, v25, 3, v26\n"
    "v_lshl_add_u32 v35, v24, 4, v25\n"
    "s_mul_i32 s92, s37, 20\n"
    "s_add_u32 s90, s90, 5120\n"
    "buffer_load_dwordx4 v[206:209], v2, s[4:7], s90 offen\n"
    "s_mul_i32 s93, s37, 48\n"
    "s_cmp_eq_u32 s61, 3\n"
    "s_cselect_b32 s92, s92, s93\n"
    "s_add_u32 s92, s92, s63\n"
    "buffer_load_dword v205, v34, s[4:7], s90 offen offset:1024\n"
    "s_mul_i32 s92, s92, 0x1400\n"
    "s_mov_b32 s93, 0x360000\n"
    "s_cmp_eq_u32 s61, 3\n"
    "s_cselect_b32 s93, s93, 0x430000\n"
    "buffer_load_dwordx4 v[216:219], v2, s[4:7], s90 offen offset:2560\n"
    "s_add_u32 s8, s92, s93\n"
    "s_mov_b32 s79, 0x400\n"
    "s_add_u32 s9, s8, s79\n"
    "s_add_u32 s10, s9, s79\n"
    "buffer_load_dword v215, v34, s[4:7], s90 offen offset:3584\n"
    "s_add_u32 s11, s10, s79\n"
    "v_add_u32 v23, 0x1000, v2\n"
    "buffer_load_dwordx4 a[0:3], v32, s[28:31], s69 offen\n"
    "buffer_load_dwordx4 a[4:7], v32, s[28:31], s69 offen offset:32\n"
    "buffer_load_dwordx4 a[8:11], v32, s[28:31], s69 offen offset:64\n"
    "buffer_load_dwordx4 a[12:15], v32, s[28:31], s69 offen offset:96\n"
    "buffer_load_dwordx4 a[16:19], v32, s[28:31], s69 offen offset:128\n"
    "buffer_load_dwordx4 a[40:43], v11, s[20:23], s76 offen\n"
    "buffer_load_dwordx4 a[44:47], v11, s[20:23], s77 offen\n"
    "v_dot2_f32_bf16 v40, v45, s43, 0\n"
    "v_dot2_f32_bf16 v41, v45, v45, 0\n"
    "v_dot2_f32_bf16 v50, v55, s43, 0\n"
    "v_dot2_f32_bf16 v51, v55, v55, 0\n"
    "v_dot2_f32_bf16 v40, v46, s43, v40\n"
    "v_dot2_f32_bf16 v41, v46, v46, v41\n"
    "v_dot2_f32_bf16 v50, v56, s43, v50\n"
    "v_dot2_f32_bf16 v51, v56, v56, v51\n"
    "v_dot2_f32_bf16 v40, v47, s43, v40\n"
    "v_dot2_f32_bf16 v41, v47, v47, v41\n"
    "v_dot2_f32_bf16 v50, v57, s43, v50\n"
    "v_dot2_f32_bf16 v51, v57, v57, v51\n"
    "v_dot2_f32_bf16 v40, v48, s43, v40\n"
    "v_dot2_f32_bf16 v41, v48, v48, v41\n"
    "v_dot2_f32_bf16 v50, v58, s43, v50\n"
    "v_dot2_f32_bf16 v51, v58, v58, v51\n"
    "v_dot2_f32_bf16 v40, v49, s43, v40\n"
    "v_dot2_f32_bf16 v41, v49, v49, v41\n"
    "v_dot2_f32_bf16 v50, v59, s43, v50\n"
    "v_dot2_f32_bf16 v51, v59, v59, v51\n"
    "v_lshlrev_b32 v42, 16, v46\n"
    "v_and_b32 v43, 0xffff0000, v46\n"
    "v_lshlrev_b32 v52, 16, v56\n"
    "v_and_b32 v53, 0xffff0000, v56\n"
    "v_permlane32_swap_b32 v40, v41\n"
    "v_permlane32_swap_b32 v50, v51\n"
    "v_add_f32 v40, v40, v41\n"
    "v_add_f32 v50, v50, v51\n"
    "v_lshlrev_b32 v44, 16, v47\n"
    "v_lshlrev_b32 v54, 16, v57\n"
    "v_permlane16_swap_b32 v40, v50\n"
    "v_add_f32 v24, v40, v50\n"
    "v_lshlrev_b32 v40, 16, v45\n"
    "v_and_b32 v41, 0xffff0000, v45\n"
    "v_and_b32 v45, 0xffff0000, v47\n"
    "v_lshlrev_b32 v46, 16, v48\n"
    "v_and_b32 v47, 0xffff0000, v48\n"
    "v_lshlrev_b32 v48, 16, v49\n"
    "v_and_b32 v49, 0xffff0000, v49\n"
    "v_lshlrev_b32 v50, 16, v55\n"
    "v_and_b32 v51, 0xffff0000, v55\n"
    "v_and_b32 v55, 0xffff0000, v57\n"
    "v_lshlrev_b32 v56, 16, v58\n"
    "v_and_b32 v57, 0xffff0000, v58\n"
    "v_lshlrev_b32 v58, 16, v59\n"
    "v_and_b32 v59, 0xffff0000, v59\n"
    "v_dot2_f32_bf16 v60, v65, s43, 0\n"
    "v_dot2_f32_bf16 v61, v65, v65, 0\n"
    "s_waitcnt vmcnt(36)\n"
    "v_dot2_f32_bf16 v70, v75, s43, 0\n"
    "v_dot2_f32_bf16 v71, v75, v75, 0\n"
    "v_dot2_f32_bf16 v60, v66, s43, v60\n"
    "v_dot2_f32_bf16 v61, v66, v66, v61\n"
    "v_dot2_f32_bf16 v70, v76, s43, v70\n"
    "v_dot2_f32_bf16 v71, v76, v76, v71\n"
    "v_dot2_f32_bf16 v60, v67, s43, v60\n"
    "v_dot2_f32_bf16 v61, v67, v67, v61\n"
    "v_dot2_f32_bf16 v70, v77, s43, v70\n"
    "v_dot2_f32_bf16 v71, v77, v77, v71\n"
    "v_dot2_f32_bf16 v60, v68, s43, v60\n"
    "v_dot2_f32_bf16 v61, v68, v68, v61\n"
    "v_dot2_f32_bf16 v70, v78, s43, v70\n"
    "v_dot2_f32_bf16 v71, v78, v78, v71\n"
    "v_dot2_f32_bf16 v60, v69, s43, v60\n"
    "v_dot2_f32_bf16 v61, v69, v69, v61\n"
    "v_dot2_f32_bf16 v70, v79, s43, v70\n"
    "v_dot2_f32_bf16 v71, v79, v79, v71\n"
    "v_lshlrev_b32 v62, 16, v66\n"
    "v_and_b32 v63, 0xffff0000, v66\n"
    "v_lshlrev_b32 v72, 16, v76\n"
    "v_and_b32 v73, 0xffff0000, v76\n"
    "v_permlane32_swap_b32 v60, v61\n"
    "v_permlane32_swap_b32 v70, v71\n"
    "v_add_f32 v60, v60, v61\n"
    "v_add_f32 v70, v70, v71\n"
    "v_lshlrev_b32 v64, 16, v67\n"
    "v_lshlrev_b32 v74, 16, v77\n"
    "v_permlane16_swap_b32 v60, v70\n"
    "v_add_f32 v25, v60, v70\n"
    "v_lshlrev_b32 v60, 16, v65\n"
    "v_and_b32 v61, 0xffff0000, v65\n"
    "v_and_b32 v65, 0xffff0000, v67\n"
    "v_lshlrev_b32 v66, 16, v68\n"
    "v_and_b32 v67, 0xffff0000, v68\n"
    "v_lshlrev_b32 v68, 16, v69\n"
    "v_and_b32 v69, 0xffff0000, v69\n"
    "v_lshlrev_b32 v70, 16, v75\n"
    "v_and_b32 v71, 0xffff0000, v75\n"
    "v_and_b32 v75, 0xffff0000, v77\n"
    "v_lshlrev_b32 v76, 16, v78\n"
    "v_and_b32 v77, 0xffff0000, v78\n"
    "v_lshlrev_b32 v78, 16, v79\n"
    "v_and_b32 v79, 0xffff0000, v79\n"
    "s_waitcnt vmcnt(34)\n"
    "v_dot2_f32_bf16 v80, v85, s43, 0\n"
    "v_dot2_f32_bf16 v81, v85, v85, 0\n"
    "s_waitcnt vmcnt(32)\n"
    "v_dot2_f32_bf16 v90, v95, s43, 0\n"
    "v_dot2_f32_bf16 v91, v95, v95, 0\n"
    "v_dot2_f32_bf16 v80, v86, s43, v80\n"
    "v_dot2_f32_bf16 v81, v86, v86, v81\n"
    "v_dot2_f32_bf16 v90, v96, s43, v90\n"
    "v_dot2_f32_bf16 v91, v96, v96, v91\n"
    "v_dot2_f32_bf16 v80, v87, s43, v80\n"
    "v_dot2_f32_bf16 v81, v87, v87, v81\n"
    "v_dot2_f32_bf16 v90, v97, s43, v90\n"
    "v_dot2_f32_bf16 v91, v97, v97, v91\n"
    "v_dot2_f32_bf16 v80, v88, s43, v80\n"
    "v_dot2_f32_bf16 v81, v88, v88, v81\n"
    "v_dot2_f32_bf16 v90, v98, s43, v90\n"
    "v_dot2_f32_bf16 v91, v98, v98, v91\n"
    "v_dot2_f32_bf16 v80, v89, s43, v80\n"
    "v_dot2_f32_bf16 v81, v89, v89, v81\n"
    "v_dot2_f32_bf16 v90, v99, s43, v90\n"
    "v_dot2_f32_bf16 v91, v99, v99, v91\n"
    "v_lshlrev_b32 v82, 16, v86\n"
    "v_and_b32 v83, 0xffff0000, v86\n"
    "v_lshlrev_b32 v92, 16, v96\n"
    "v_and_b32 v93, 0xffff0000, v96\n"
    "v_permlane32_swap_b32 v80, v81\n"
    "v_permlane32_swap_b32 v90, v91\n"
    "v_add_f32 v80, v80, v81\n"
    "v_add_f32 v90, v90, v91\n"
    "v_lshlrev_b32 v84, 16, v87\n"
    "v_lshlrev_b32 v94, 16, v97\n"
    "v_permlane16_swap_b32 v80, v90\n"
    "v_add_f32 v26, v80, v90\n"
    "v_lshlrev_b32 v80, 16, v85\n"
    "v_and_b32 v81, 0xffff0000, v85\n"
    "v_and_b32 v85, 0xffff0000, v87\n"
    "v_lshlrev_b32 v86, 16, v88\n"
    "v_and_b32 v87, 0xffff0000, v88\n"
    "v_lshlrev_b32 v88, 16, v89\n"
    "v_and_b32 v89, 0xffff0000, v89\n"
    "v_lshlrev_b32 v90, 16, v95\n"
    "v_and_b32 v91, 0xffff0000, v95\n"
    "v_and_b32 v95, 0xffff0000, v97\n"
    "v_lshlrev_b32 v96, 16, v98\n"
    "v_and_b32 v97, 0xffff0000, v98\n"
    "v_lshlrev_b32 v98, 16, v99\n"
    "v_and_b32 v99, 0xffff0000, v99\n"
    "s_waitcnt vmcnt(30)\n"
    "v_dot2_f32_bf16 v100, v105, s43, 0\n"
    "v_dot2_f32_bf16 v101, v105, v105, 0\n"
    "s_waitcnt vmcnt(28)\n"
    "v_dot2_f32_bf16 v110, v115, s43, 0\n"
    "v_dot2_f32_bf16 v111, v115, v115, 0\n"
    "v_dot2_f32_bf16 v100, v106, s43, v100\n"
    "v_dot2_f32_bf16 v101, v106, v106, v101\n"
    "v_dot2_f32_bf16 v110, v116, s43, v110\n"
    "v_dot2_f32_bf16 v111, v116, v116, v111\n"
    "v_dot2_f32_bf16 v100, v107, s43, v100\n"
    "v_dot2_f32_bf16 v101, v107, v107, v101\n"
    "v_dot2_f32_bf16 v110, v117, s43, v110\n"
    "v_dot2_f32_bf16 v111, v117, v117, v111\n"
    "v_dot2_f32_bf16 v100, v108, s43, v100\n"
    "v_dot2_f32_bf16 v101, v108, v108, v101\n"
    "v_dot2_f32_bf16 v110, v118, s43, v110\n"
    "v_dot2_f32_bf16 v111, v118, v118, v111\n"
    "v_dot2_f32_bf16 v100, v109, s43, v100\n"
    "v_dot2_f32_bf16 v101, v109, v109, v101\n"
    "v_dot2_f32_bf16 v110, v119, s43, v110\n"
    "v_dot2_f32_bf16 v111, v119, v119, v111\n"
    "v_lshlrev_b32 v102, 16, v106\n"
    "v_and_b32 v103, 0xffff0000, v106\n"
    "v_lshlrev_b32 v112, 16, v116\n"
    "v_and_b32 v113, 0xffff0000, v116\n"
    "v_permlane32_swap_b32 v100, v101\n"
    "v_permlane32_swap_b32 v110, v111\n"
    "v_add_f32 v100, v100, v101\n"
    "v_add_f32 v110, v110, v111\n"
    "v_lshlrev_b32 v104, 16, v107\n"
    "v_lshlrev_b32 v114, 16, v117\n"
    "v_permlane16_swap_b32 v100, v110\n"
    "v_add_f32 v27, v100, v110\n"
    "v_lshlrev_b32 v100, 16, v105\n"
    "v_and_b32 v101, 0xffff0000, v105\n"
    "v_and_b32 v105, 0xffff0000, v107\n"
    "v_lshlrev_b32 v106, 16, v108\n"
    "v_and_b32 v107, 0xffff0000, v108\n"
    "v_lshlrev_b32 v108, 16, v109\n"
    "v_and_b32 v109, 0xffff0000, v109\n"
    "v_lshlrev_b32 v110, 16, v115\n"
    "v_and_b32 v111, 0xffff0000, v115\n"
    "v_and_b32 v115, 0xffff0000, v117\n"
    "v_lshlrev_b32 v116, 16, v118\n"
    "v_and_b32 v117, 0xffff0000, v118\n"
    "v_lshlrev_b32 v118, 16, v119\n"
    "v_and_b32 v119, 0xffff0000, v119\n"
    "s_waitcnt vmcnt(26)\n"
    "v_dot2_f32_bf16 v120, v125, s43, 0\n"
    "v_dot2_f32_bf16 v121, v125, v125, 0\n"
    "s_waitcnt vmcnt(24)\n"
    "v_dot2_f32_bf16 v130, v135, s43, 0\n"
    "v_dot2_f32_bf16 v131, v135, v135, 0\n"
    "v_dot2_f32_bf16 v120, v126, s43, v120\n"
    "v_dot2_f32_bf16 v121, v126, v126, v121\n"
    "v_dot2_f32_bf16 v130, v136, s43, v130\n"
    "v_dot2_f32_bf16 v131, v136, v136, v131\n"
    "v_dot2_f32_bf16 v120, v127, s43, v120\n"
    "v_dot2_f32_bf16 v121, v127, v127, v121\n"
    "v_dot2_f32_bf16 v130, v137, s43, v130\n"
    "v_dot2_f32_bf16 v131, v137, v137, v131\n"
    "v_dot2_f32_bf16 v120, v128, s43, v120\n"
    "v_dot2_f32_bf16 v121, v128, v128, v121\n"
    "v_dot2_f32_bf16 v130, v138, s43, v130\n"
    "v_dot2_f32_bf16 v131, v138, v138, v131\n"
    "v_dot2_f32_bf16 v120, v129, s43, v120\n"
    "v_dot2_f32_bf16 v121, v129, v129, v121\n"
    "v_dot2_f32_bf16 v130, v139, s43, v130\n"
    "v_dot2_f32_bf16 v131, v139, v139, v131\n"
    "v_lshlrev_b32 v122, 16, v126\n"
    "v_and_b32 v123, 0xffff0000, v126\n"
    "v_lshlrev_b32 v132, 16, v136\n"
    "v_and_b32 v133, 0xffff0000, v136\n"
    "v_permlane32_swap_b32 v120, v121\n"
    "v_permlane32_swap_b32 v130, v131\n"
    "v_add_f32 v120, v120, v121\n"
    "v_add_f32 v130, v130, v131\n"
    "v_lshlrev_b32 v124, 16, v127\n"
    "v_lshlrev_b32 v134, 16, v137\n"
    "v_permlane16_swap_b32 v120, v130\n"
    "v_add_f32 v28, v120, v130\n"
    "v_lshlrev_b32 v120, 16, v125\n"
    "v_and_b32 v121, 0xffff0000, v125\n"
    "v_and_b32 v125, 0xffff0000, v127\n"
    "v_lshlrev_b32 v126, 16, v128\n"
    "v_and_b32 v127, 0xffff0000, v128\n"
    "v_lshlrev_b32 v128, 16, v129\n"
    "v_and_b32 v129, 0xffff0000, v129\n"
    "v_lshlrev_b32 v130, 16, v135\n"
    "v_and_b32 v131, 0xffff0000, v135\n"
    "v_and_b32 v135, 0xffff0000, v137\n"
    "v_lshlrev_b32 v136, 16, v138\n"
    "v_and_b32 v137, 0xffff0000, v138\n"
    "v_lshlrev_b32 v138, 16, v139\n"
    "v_and_b32 v139, 0xffff0000, v139\n"
    "s_waitcnt vmcnt(22)\n"
    "v_dot2_f32_bf16 v140, v145, s43, 0\n"
    "v_dot2_f32_bf16 v141, v145, v145, 0\n"
    "s_waitcnt vmcnt(20)\n"
    "v_dot2_f32_bf16 v150, v155, s43, 0\n"
    "v_dot2_f32_bf16 v151, v155, v155, 0\n"
    "v_dot2_f32_bf16 v140, v146, s43, v140\n"
    "v_dot2_f32_bf16 v141, v146, v146, v141\n"
    "v_dot2_f32_bf16 v150, v156, s43, v150\n"
    "v_dot2_f32_bf16 v151, v156, v156, v151\n"
    "v_dot2_f32_bf16 v140, v147, s43, v140\n"
    "v_dot2_f32_bf16 v141, v147, v147, v141\n"
    "v_dot2_f32_bf16 v150, v157, s43, v150\n"
    "v_dot2_f32_bf16 v151, v157, v157, v151\n"
    "v_dot2_f32_bf16 v140, v148, s43, v140\n"
    "v_dot2_f32_bf16 v141, v148, v148, v141\n"
    "v_dot2_f32_bf16 v150, v158, s43, v150\n"
    "v_dot2_f32_bf16 v151, v158, v158, v151\n"
    "v_dot2_f32_bf16 v140, v149, s43, v140\n"
    "v_dot2_f32_bf16 v141, v149, v149, v141\n"
    "v_dot2_f32_bf16 v150, v159, s43, v150\n"
    "v_dot2_f32_bf16 v151, v159, v159, v151\n"
    "v_lshlrev_b32 v142, 16, v146\n"
    "v_and_b32 v143, 0xffff0000, v146\n"
    "v_lshlrev_b32 v152, 16, v156\n"
    "v_and_b32 v153, 0xffff0000, v156\n"
    "v_permlane32_swap_b32 v140, v141\n"
    "v_permlane32_swap_b32 v150, v151\n"
    "v_add_f32 v140, v140, v141\n"
    "v_add_f32 v150, v150, v151\n"
    "v_lshlrev_b32 v144, 16, v147\n"
    "v_lshlrev_b32 v154, 16, v157\n"
    "v_permlane16_swap_b32 v140, v150\n"
    "v_add_f32 v29, v140, v150\n"
    "v_lshlrev_b32 v140, 16, v145\n"
    "v_and_b32 v141, 0xffff0000, v145\n"
    "v_and_b32 v145, 0xffff0000, v147\n"
    "v_lshlrev_b32 v146, 16, v148\n"
    "v_and_b32 v147, 0xffff0000, v148\n"
    "v_lshlrev_b32 v148, 16, v149\n"
    "v_and_b32 v149, 0xffff0000, v149\n"
    "v_lshlrev_b32 v150, 16, v155\n"
    "v_and_b32 v151, 0xffff0000, v155\n"
    "v_and_b32 v155, 0xffff0000, v157\n"
    "v_lshlrev_b32 v156, 16, v158\n"
    "v_and_b32 v157, 0xffff0000, v158\n"
    "v_lshlrev_b32 v158, 16, v159\n"
    "v_and_b32 v159, 0xffff0000, v159\n"
    "s_waitcnt vmcnt(6)\n"
    "ds_write_b128 v16, a[0:3]\n"
    "s_waitcnt vmcnt(5)\n"
    "ds_write_b128 v16, a[4:7] offset:32\n"
    "s_waitcnt vmcnt(4)\n"
    "ds_write_b128 v16, a[8:11] offset:64\n"
    "s_waitcnt vmcnt(3)\n"
    "ds_write_b128 v16, a[12:15] offset:96\n"
    "s_waitcnt vmcnt(2)\n"
    "ds_write_b128 v16, a[16:19] offset:128\n"
    "ds_read_b64_tr_b16 a[216:217], v36\n"
    "ds_read_b64_tr_b16 a[218:219], v36 offset:640\n"
    "ds_read_b64_tr_b16 a[220:221], v37\n"
    "ds_read_b64_tr_b16 a[222:223], v37 offset:640\n"
    "ds_read_b64_tr_b16 a[224:225], v38\n"
    "ds_read_b64_tr_b16 a[226:227], v38 offset:640\n"
    "ds_read_b64_tr_b16 a[228:229], v39\n"
    "ds_read_b64_tr_b16 a[230:231], v39 offset:640\n"
    "ds_read_b64_tr_b16 a[232:233], v35\n"
    "ds_read_b64_tr_b16 a[234:235], v35 offset:640\n"
    "s_waitcnt vmcnt(1)\n"
    "ds_write_b128 v22, a[40:43] offset:5120\n"
    "s_waitcnt vmcnt(0)\n"
    "ds_write_b128 v22, a[44:47] offset:6400\n"
    "ds_read_b64_tr_b16 a[236:237], v21 offset:5120\n"
    "ds_read_b64_tr_b16 a[238:239], v21 offset:5440\n"
    "ds_read_b64_tr_b16 a[240:241], v20 offset:5120\n"
    "ds_read_b64_tr_b16 a[242:243], v20 offset:5440\n"
    "buffer_load_dword v11, v10, s[16:19], 0 offen sc1\n"
    "v_dot2_f32_bf16 v160, v165, s43, 0\n"
    "v_dot2_f32_bf16 v161, v165, v165, 0\n"
    "v_dot2_f32_bf16 v170, v175, s43, 0\n"
    "v_dot2_f32_bf16 v171, v175, v175, 0\n"
    "v_dot2_f32_bf16 v160, v166, s43, v160\n"
    "v_dot2_f32_bf16 v161, v166, v166, v161\n"
    "v_dot2_f32_bf16 v170, v176, s43, v170\n"
    "v_dot2_f32_bf16 v171, v176, v176, v171\n"
    "v_dot2_f32_bf16 v160, v167, s43, v160\n"
    "v_dot2_f32_bf16 v161, v167, v167, v161\n"
    "v_dot2_f32_bf16 v170, v177, s43, v170\n"
    "v_dot2_f32_bf16 v171, v177, v177, v171\n"
    "v_dot2_f32_bf16 v160, v168, s43, v160\n"
    "v_dot2_f32_bf16 v161, v168, v168, v161\n"
    "v_dot2_f32_bf16 v170, v178, s43, v170\n"
    "v_dot2_f32_bf16 v171, v178, v178, v171\n"
    "v_dot2_f32_bf16 v160, v169, s43, v160\n"
    "v_dot2_f32_bf16 v161, v169, v169, v161\n"
    "v_dot2_f32_bf16 v170, v179, s43, v170\n"
    "v_dot2_f32_bf16 v171, v179, v179, v171\n"
    "v_lshlrev_b32 v162, 16, v166\n"
    "v_and_b32 v163, 0xffff0000, v166\n"
    "v_lshlrev_b32 v172, 16, v176\n"
    "v_and_b32 v173, 0xffff0000, v176\n"
    "v_permlane32_swap_b32 v160, v161\n"
    "v_permlane32_swap_b32 v170, v171\n"
    "v_add_f32 v160, v160, v161\n"
    "v_add_f32 v170, v170, v171\n"
    "v_lshlrev_b32 v164, 16, v167\n"
    "v_lshlrev_b32 v174, 16, v177\n"
    "v_permlane16_swap_b32 v160, v170\n"
    "v_add_f32 v30, v160, v170\n"
    "v_lshlrev_b32 v160, 16, v165\n"
    "v_and_b32 v161, 0xffff0000, v165\n"
    "v_and_b32 v165, 0xffff0000, v167\n"
    "v_lshlrev_b32 v166, 16, v168\n"
    "v_and_b32 v167, 0xffff0000, v168\n"
    "v_lshlrev_b32 v168, 16, v169\n"
    "v_and_b32 v169, 0xffff0000, v169\n"
    "v_lshlrev_b32 v170, 16, v175\n"
    "v_and_b32 v171, 0xffff0000, v175\n"
    "v_and_b32 v175, 0xffff0000, v177\n"
    "v_lshlrev_b32 v176, 16, v178\n"
    "v_and_b32 v177, 0xffff0000, v178\n"
    "v_lshlrev_b32 v178, 16, v179\n"
    "v_and_b32 v179, 0xffff0000, v179\n"
    "s_waitcnt vmcnt(0)\n"
    "s_waitcnt lgkmcnt(0)\n"
    ".Ljoin_%=:\n"
    "s_lshl_b32 s92, s87, 7\n"
    "s_add_u32 s92, s92, 0xb0000\n"
    "v_add_u32 v35, s92, v2\n"
    "v_cmp_gt_u32 vcc, 8, v1\n"
    "s_nop 1\n"
    "v_cndmask_b32 v17, 0x40000000, v35, vcc\n"
    "v_lshrrev_b32 v35, 5, v1\n"
    "v_mul_u32_u24 v35, 0x27fe00, v35\n"
    "v_add_u32 v18, v35, v2\n"
    "v_cmp_ne_u32 vcc, s40, v11\n"
    "s_nop 1\n"
    "s_cbranch_vccz .Ld1_%=\n"
    ".Lr1_%=:\n"
    "s_cmp_eq_u32 s42, 0\n"
    "s_cbranch_scc1 .Ld1_%=\n"
    "s_sub_u32 s42, s42, 1\n"
    "buffer_load_dword v11, v10, s[16:19], 0 offen sc1\n"
    "s_waitcnt vmcnt(0)\n"
    "v_cmp_ne_u32 vcc, s40, v11\n"
    "s_nop 1\n"
    "s_cbranch_vccnz .Lr1_%=\n"
    ".Ld1_%=:\n"
    "s_lshl_b32 s92, s37, 2\n"
    "s_add_u32 s92, s92, s36\n"
    "s_mul_i32 s92, s92, 0x12c00\n"
    "s_add_u32 s51, s92, 0x100000\n"
    "v_bfe_u32 v35, v1, 2, 2\n"
    "v_add_u32 v35, 1, v35\n"
    "v_lshrrev_b32 v35, 1, v35\n"
    "v_lshrrev_b32 v36, 4, v1\n"
    "v_and_b32 v37, 3, v1\n"
    "v_lshlrev_b32 v37, 4, v37\n"
    "v_lshl_add_u32 v36, v36, 6, v37\n"
    "v_lshl_add_u32 v35, v35, 8, v36\n"
    "v_bfe_u32 v36, v1, 2, 2\n"
    "v_mov_b32 v37, 0x40000000\n"
    "v_cmp_eq_u32 vcc, 2, v36\n"
    "s_nop 1\n"
    "v_cndmask_b32 v16, v35, v37, vcc\n"
    "v_dot2_f32_bf16 v180, v185, s43, 0\n"
    "v_lshrrev_b32 v34, 1, v6\n"
    "v_min_u32 v34, 0x3f8, v34\n"
    "buffer_load_dwordx4 v[12:15], v6, s[16:19], s45 offen sc1\n"
    "buffer_load_dwordx2 v[4:5], v34, s[16:19], s46 offen sc1\n"
    "v_dot2_f32_bf16 v181, v185, v185, 0\n"
    "v_dot2_f32_bf16 v190, v195, s43, 0\n"
    "v_dot2_f32_bf16 v191, v195, v195, 0\n"
    "v_dot2_f32_bf16 v180, v186, s43, v180\n"
    "v_dot2_f32_bf16 v181, v186, v186, v181\n"
    "v_dot2_f32_bf16 v190, v196, s43, v190\n"
    "v_dot2_f32_bf16 v191, v196, v196, v191\n"
    "v_dot2_f32_bf16 v180, v187, s43, v180\n"
    "v_dot2_f32_bf16 v181, v187, v187, v181\n"
    "v_dot2_f32_bf16 v190, v197, s43, v190\n"
    "v_dot2_f32_bf16 v191, v197, v197, v191\n"
    "v_dot2_f32_bf16 v180, v188, s43, v180\n"
    "v_dot2_f32_bf16 v181, v188, v188, v181\n"
    "v_dot2_f32_bf16 v190, v198, s43, v190\n"
    "v_dot2_f32_bf16 v191, v198, v198, v191\n"
    "v_dot2_f32_bf16 v180, v189, s43, v180\n"
    "buffer_load_dwordx4 a[0:3], v2, s[16:19], s51 offen sc1\n"
    "v_dot2_f32_bf16 v181, v189, v189, v181\n"
    "v_dot2_f32_bf16 v190, v199, s43, v190\n"
    "v_dot2_f32_bf16 v191, v199, v199, v191\n"
    "v_lshlrev_b32 v182, 16, v186\n"
    "v_and_b32 v183, 0xffff0000, v186\n"
    "v_lshlrev_b32 v192, 16, v196\n"
    "v_and_b32 v193, 0xffff0000, v196\n"
    "v_permlane32_swap_b32 v180, v181\n"
    "v_permlane32_swap_b32 v190, v191\n"
    "v_add_f32 v180, v180, v181\n"
    "v_add_f32 v190, v190, v191\n"
    "v_lshlrev_b32 v184, 16, v187\n"
    "v_lshlrev_b32 v194, 16, v197\n"
    "v_permlane16_swap_b32 v180, v190\n"
    "v_add_f32 v31, v180, v190\n"
    "v_lshlrev_b32 v180, 16, v185\n"
    "buffer_store_dwordx4 a[244:247], v2, s[16:19], s32 offen sc1\n"
    "v_and_b32 v181, 0xffff0000, v185\n"
    "v_and_b32 v185, 0xffff0000, v187\n"
    "v_lshlrev_b32 v186, 16, v188\n"
    "v_and_b32 v187, 0xffff0000, v188\n"
    "v_lshlrev_b32 v188, 16, v189\n"
    "v_and_b32 v189, 0xffff0000, v189\n"
    "v_lshlrev_b32 v190, 16, v195\n"
    "v_and_b32 v191, 0xffff0000, v195\n"
    "v_and_b32 v195, 0xffff0000, v197\n"
    "v_lshlrev_b32 v196, 16, v198\n"
    "v_and_b32 v197, 0xffff0000, v198\n"
    "v_lshlrev_b32 v198, 16, v199\n"
    "v_and_b32 v199, 0xffff0000, v199\n"
    "v_dot2_f32_bf16 v200, v205, s43, 0\n"
    "v_dot2_f32_bf16 v201, v205, v205, 0\n"
    "v_dot2_f32_bf16 v210, v215, s43, 0\n"
    "buffer_load_dwordx4 a[4:7], v2, s[16:19], s51 offen offset:1024 sc1\n"
    "v_dot2_f32_bf16 v211, v215, v215, 0\n"
    "v_dot2_f32_bf16 v200, v206, s43, v200\n"
    "v_dot2_f32_bf16 v201, v206, v206, v201\n"
    "v_dot2_f32_bf16 v210, v216, s43, v210\n"
    "v_dot2_f32_bf16 v211, v216, v216, v211\n"
    "v_dot2_f32_bf16 v200, v207, s43, v200\n"
    "v_dot2_f32_bf16 v201, v207, v207, v201\n"
    "v_dot2_f32_bf16 v210, v217, s43, v210\n"
    "v_dot2_f32_bf16 v211, v217, v217, v211\n"
    "v_dot2_f32_bf16 v200, v208, s43, v200\n"
    "v_dot2_f32_bf16 v201, v208, v208, v201\n"
    "v_dot2_f32_bf16 v210, v218, s43, v210\n"
    "v_dot2_f32_bf16 v211, v218, v218, v211\n"
    "v_dot2_f32_bf16 v200, v209, s43, v200\n"
    "v_dot2_f32_bf16 v201, v209, v209, v201\n"
    "v_dot2_f32_bf16 v210, v219, s43, v210\n"
    "buffer_load_dwordx4 a[8:11], v2, s[16:19], s51 offen offset:2048 sc1\n"
    "v_dot2_f32_bf16 v211, v219, v219, v211\n"
    "v_lshlrev_b32 v202, 16, v206\n"
    "v_and_b32 v203, 0xffff0000, v206\n"
    "v_lshlrev_b32 v212, 16, v216\n"
    "v_and_b32 v213, 0xffff0000, v216\n"
    "v_permlane32_swap_b32 v200, v201\n"
    "v_permlane32_swap_b32 v210, v211\n"
    "v_add_f32 v200, v200, v201\n"
    "v_add_f32 v210, v210, v211\n"
    "v_lshlrev_b32 v204, 16, v207\n"
    "v_lshlrev_b32 v214, 16, v217\n"
    "v_permlane16_swap_b32 v200, v210\n"
    "v_add_f32 v32, v200, v210\n"
    "v_lshlrev_b32 v200, 16, v205\n"
    "v_and_b32 v201, 0xffff0000, v205\n"
    "v_and_b32 v205, 0xffff0000, v207\n"
    "buffer_load_dwordx4 a[12:15], v16, s[16:19], s51 offen offset:3072 sc1\n"
    "v_lshlrev_b32 v206, 16, v208\n"
    "v_and_b32 v207, 0xffff0000, v208\n"
    "v_lshlrev_b32 v208, 16, v209\n"
    "v_and_b32 v209, 0xffff0000, v209\n"
    "v_lshlrev_b32 v210, 16, v215\n"
    "v_and_b32 v211, 0xffff0000, v215\n"
    "v_and_b32 v215, 0xffff0000, v217\n"
    "v_lshlrev_b32 v216, 16, v218\n"
    "v_and_b32 v217, 0xffff0000, v218\n"
    "v_lshlrev_b32 v218, 16, v219\n"
    "v_and_b32 v219, 0xffff0000, v219\n"
    "v_add_f32_dpp v24, v24, v24 row_ror:8 row_mask:0xf bank_mask:0xf\n"
    "v_add_f32_dpp v25, v25, v25 row_ror:8 row_mask:0xf bank_mask:0xf\n"
    "v_add_f32_dpp v26, v26, v26 row_ror:8 row_mask:0xf bank_mask:0xf\n"
    "v_add_f32_dpp v27, v27, v27 row_ror:8 row_mask:0xf bank_mask:0xf\n"
    "v_add_f32_dpp v28, v28, v28 row_ror:8 row_mask:0xf bank_mask:0xf\n"
    "s_add_u32 s51, s51, 3840\n"
    "buffer_load_dwordx4 a[16:19], v2, s[16:19], s51 offen sc1\n"
    "v_add_f32_dpp v29, v29, v29 row_ror:8 row_mask:0xf bank_mask:0xf\n"
    "v_add_f32_dpp v30, v30, v30 row_ror:8 row_mask:0xf bank_mask:0xf\n"
    "v_add_f32_dpp v31, v31, v31 row_ror:8 row_mask:0xf bank_mask:0xf\n"
    "v_add_f32_dpp v32, v32, v32 row_ror:8 row_mask:0xf bank_mask:0xf\n"
    "v_add_f32_dpp v24, v24, v24 row_ror:4 row_mask:0xf bank_mask:0xf\n"
    "v_add_f32_dpp v25, v25, v25 row_ror:4 row_mask:0xf bank_mask:0xf\n"
    "v_add_f32_dpp v26, v26, v26 row_ror:4 row_mask:0xf bank_mask:0xf\n"
    "v_add_f32_dpp v27, v27, v27 row_ror:4 row_mask:0xf bank_mask:0xf\n"
    "v_add_f32_dpp v28, v28, v28 row_ror:4 row_mask:0xf bank_mask:0xf\n"
    "v_add_f32_dpp v29, v29, v29 row_ror:4 row_mask:0xf bank_mask:0xf\n"
    "v_add_f32_dpp v30, v30, v30 row_ror:4 row_mask:0xf bank_mask:0xf\n"
    "v_add_f32_dpp v31, v31, v31 row_ror:4 row_mask:0xf bank_mask:0xf\n"
    "v_add_f32_dpp v32, v32, v32 row_ror:4 row_mask:0xf bank_mask:0xf\n"
    "v_add_f32_dpp v24, v24, v24 row_ror:2 row_mask:0xf bank_mask:0xf\n"
    "v_add_f32_dpp v25, v25, v25 row_ror:2 row_mask:0xf bank_mask:0xf\n"
    "v_add_f32_dpp v26, v26, v26 row_ror:2 row_mask:0xf bank_mask:0xf\n"
    "s_cmp_lt_u32 s2, 0x80\n"
    "s_cbranch_scc1 .Lsk0_%=\n"
    "buffer_store_dwordx4 a[216:219], v2, s[16:19], s8 offen sc1\n"
    ".Lsk0_%=:\n"
    "v_add_f32_dpp v27, v27, v27 row_ror:2 row_mask:0xf bank_mask:0xf\n"
    "v_add_f32_dpp v28, v28, v28 row_ror:2 row_mask:0xf bank_mask:0xf\n"
    "v_add_f32_dpp v29, v29, v29 row_ror:2 row_mask:0xf bank_mask:0xf\n"
    "v_add_f32_dpp v30, v30, v30 row_ror:2 row_mask:0xf bank_mask:0xf\n"
    "v_add_f32_dpp v31, v31, v31 row_ror:2 row_mask:0xf bank_mask:0xf\n"
    "v_add_f32_dpp v32, v32, v32 row_ror:2 row_mask:0xf bank_mask:0xf\n"
    "v_add_f32_dpp v24, v24, v24 row_ror:1 row_mask:0xf bank_mask:0xf\n"
    "v_add_f32_dpp v25, v25, v25 row_ror:1 row_mask:0xf bank_mask:0xf\n"
    "v_add_f32_dpp v26, v26, v26 row_ror:1 row_mask:0xf bank_mask:0xf\n"
    "v_add_f32_dpp v27, v27, v27 row_ror:1 row_mask:0xf bank_mask:0xf\n"
    "v_add_f32_dpp v28, v28, v28 row_ror:1 row_mask:0xf bank_mask:0xf\n"
    "v_add_f32_dpp v29, v29, v29 row_ror:1 row_mask:0xf bank_mask:0xf\n"
    "v_add_f32_dpp v30, v30, v30 row_ror:1 row_mask:0xf bank_mask:0xf\n"
    "v_add_f32_dpp v31, v31, v31 row_ror:1 row_mask:0xf bank_mask:0xf\n"
    "v_add_f32_dpp v32, v32, v32 row_ror:1 row_mask:0xf bank_mask:0xf\n"
    "v_permlane32_swap_b32 v24, v25\n"
    "buffer_load_dwordx4 a[20:23], v2, s[16:19], s51 offen offset:1024 sc1\n"
    "v_permlane32_swap_b32 v26, v27\n"
    "v_permlane32_swap_b32 v28, v29\n"
    "v_permlane32_swap_b32 v30, v31\n"
    "v_permlane32_swap_b32 v32, v33\n"
    "v_mul_f32 v24, 0x3acccccd, v24\n"
    "v_mul_f32 v26, 0x3acccccd, v26\n"
    "v_mul_f32 v28, 0x3acccccd, v28\n"
    "v_mul_f32 v30, 0x3acccccd, v30\n"
    "v_mul_f32 v32, 0x3acccccd, v32\n"
    "v_mul_f32 v25, 0x3acccccd, v25\n"
    "v_mul_f32 v27, 0x3acccccd, v27\n"
    "v_mul_f32 v29, 0x3acccccd, v29\n"
    "v_mul_f32 v31, 0x3acccccd, v31\n"
    "v_mul_f32 v33, 0x3acccccd, v33\n"
    "v_fma_f32 v25, -v24, v24, v25\n"
    "v_fma_f32 v27, -v26, v26, v27\n"
    "buffer_load_dwordx4 a[24:27], v2, s[16:19], s51 offen offset:2048 sc1\n"
    "v_fma_f32 v29, -v28, v28, v29\n"
    "v_fma_f32 v31, -v30, v30, v31\n"
    "v_fma_f32 v33, -v32, v32, v33\n"
    "v_max_f32 v25, 0, v25\n"
    "v_max_f32 v27, 0, v27\n"
    "v_max_f32 v29, 0, v29\n"
    "v_max_f32 v31, 0, v31\n"
    "v_max_f32 v33, 0, v33\n"
    "v_add_f32 v25, 0x3727c5ac, v25\n"
    "v_add_f32 v27, 0x3727c5ac, v27\n"
    "v_add_f32 v29, 0x3727c5ac, v29\n"
    "v_add_f32 v31, 0x3727c5ac, v31\n"
    "v_add_f32 v33, 0x3727c5ac, v33\n"
    "v_rsq_f32 v25, v25\n"
    "v_rsq_f32 v27, v27\n"
    "buffer_load_dwordx4 a[28:31], v16, s[16:19], s51 offen offset:3072 sc1\n"
    "v_rsq_f32 v29, v29\n"
    "v_rsq_f32 v31, v31\n"
    "v_rsq_f32 v33, v33\n"
    "v_mul_f32 v24, -v24, v25\n"
    "v_mul_f32 v26, -v26, v27\n"
    "v_mul_f32 v28, -v28, v29\n"
    "v_mul_f32 v30, -v30, v31\n"
    "v_mul_f32 v32, -v32, v33\n"
    "v_readlane_b32 s56, v25, 0\n"
    "v_readlane_b32 s57, v24, 0\n"
    "v_readlane_b32 s58, v25, 16\n"
    "v_readlane_b32 s59, v24, 16\n"
    "v_readlane_b32 s60, v25, 32\n"
    "v_readlane_b32 s61, v24, 32\n"
    "v_readlane_b32 s62, v25, 48\n"
    "v_readlane_b32 s63, v24, 48\n"
    "buffer_store_dwordx4 a[244:247], v2, s[16:19], s32 offen offset:1024 sc1\n"
    "v_readlane_b32 s64, v27, 0\n"
    "v_readlane_b32 s65, v26, 0\n"
    "v_readlane_b32 s66, v27, 16\n"
    "v_readlane_b32 s67, v26, 16\n"
    "v_readlane_b32 s68, v27, 32\n"
    "v_readlane_b32 s69, v26, 32\n"
    "v_readlane_b32 s70, v27, 48\n"
    "v_readlane_b32 s71, v26, 48\n"
    "v_readlane_b32 s72, v29, 0\n"
    "v_readlane_b32 s73, v28, 0\n"
    "v_readlane_b32 s74, v29, 16\n"
    "v_readlane_b32 s75, v28, 16\n"
    "v_readlane_b32 s76, v29, 32\n"
    "v_readlane_b32 s77, v28, 32\n"
    "v_readlane_b32 s78, v29, 48\n"
    "v_readlane_b32 s79, v28, 48\n"
    "s_add_u32 s51, s51, 3840\n"
    "buffer_load_dwordx4 a[32:35], v2, s[16:19], s51 offen sc1\n"
    "v_readlane_b32 s80, v31, 0\n"
    "v_readlane_b32 s81, v30, 0\n"
    "v_readlane_b32 s82, v31, 16\n"
    "v_readlane_b32 s83, v30, 16\n"
    "v_readlane_b32 s84, v31, 32\n"
    "v_readlane_b32 s85, v30, 32\n"
    "v_readlane_b32 s86, v31, 48\n"
    "v_readlane_b32 s87, v30, 48\n"
    "v_readlane_b32 s88, v33, 0\n"
    "v_readlane_b32 s89, v32, 0\n"
    "v_readlane_b32 s90, v33, 16\n"
    "v_readlane_b32 s91, v32, 16\n"
    "v_pk_fma_f32 v[40:41], v[40:41], s[56:57], s[56:57] op_sel:[0,0,1] op_sel_hi:[1,0,1]\n"
    "v_pk_fma_f32 v[42:43], v[42:43], s[56:57], s[56:57] op_sel:[0,0,1] op_sel_hi:[1,0,1]\n"
    "v_pk_fma_f32 v[44:45], v[44:45], s[56:57], s[56:57] op_sel:[0,0,1] op_sel_hi:[1,0,1]\n"
    "v_pk_fma_f32 v[46:47], v[46:47], s[56:57], s[56:57] op_sel:[0,0,1] op_sel_hi:[1,0,1]\n"
    "buffer_load_dwordx4 a[36:39], v2, s[16:19], s51 offen offset:1024 sc1\n"
    "v_pk_fma_f32 v[48:49], v[48:49], s[56:57], s[56:57] op_sel:[0,0,1] op_sel_hi:[1,0,1]\n"
    "v_pk_fma_f32 v[50:51], v[50:51], s[58:59], s[58:59] op_sel:[0,0,1] op_sel_hi:[1,0,1]\n"
    "v_pk_fma_f32 v[52:53], v[52:53], s[58:59], s[58:59] op_sel:[0,0,1] op_sel_hi:[1,0,1]\n"
    "v_pk_fma_f32 v[54:55], v[54:55], s[58:59], s[58:59] op_sel:[0,0,1] op_sel_hi:[1,0,1]\n"
    "v_pk_fma_f32 v[56:57], v[56:57], s[58:59], s[58:59] op_sel:[0,0,1] op_sel_hi:[1,0,1]\n"
    "v_pk_fma_f32 v[58:59], v[58:59], s[58:59], s[58:59] op_sel:[0,0,1] op_sel_hi:[1,0,1]\n"
    "v_pk_fma_f32 v[60:61], v[60:61], s[60:61], s[60:61] op_sel:[0,0,1] op_sel_hi:[1,0,1]\n"
    "v_pk_fma_f32 v[62:63], v[62:63], s[60:61], s[60:61] op_sel:[0,0,1] op_sel_hi:[1,0,1]\n"
    "v_pk_fma_f32 v[64:65], v[64:65], s[60:61], s[60:61] op_sel:[0,0,1] op_sel_hi:[1,0,1]\n"
    "v_pk_fma_f32 v[66:67], v[66:67], s[60:61], s[60:61] op_sel:[0,0,1] op_sel_hi:[1,0,1]\n"
    "v_pk_fma_f32 v[68:69], v[68:69], s[60:61], s[60:61] op_sel:[0,0,1] op_sel_hi:[1,0,1]\n"
    "v_pk_fma_f32 v[70:71], v[70:71], s[62:63], s[62:63] op_sel:[0,0,1] op_sel_hi:[1,0,1]\n"
    "v_pk_fma_f32 v[72:73], v[72:73], s[62:63], s[62:63] op_sel:[0,0,1] op_sel_hi:[1,0,1]\n"
    "v_pk_fma_f32 v[74:75], v[74:75], s[62:63], s[62:63] op_sel:[0,0,1] op_sel_hi:[1,0,1]\n"
    "v_pk_fma_f32 v[76:77], v[76:77], s[62:63], s[62:63] op_sel:[0,0,1] op_sel_hi:[1,0,1]\n"
    "v_pk_fma_f32 v[78:79], v[78:79], s[62:63], s[62:63] op_sel:[0,0,1] op_sel_hi:[1,0,1]\n"
    "buffer_load_dwordx4 a[40:43], v2, s[16:19], s51 offen offset:2048 sc1\n"
    "v_pk_fma_f32 v[80:81], v[80:81], s[64:65], s[64:65] op_sel:[0,0,1] op_sel_hi:[1,0,1]\n"
    "v_pk_fma_f32 v[82:83], v[82:83], s[64:65], s[64:65] op_sel:[0,0,1] op_sel_hi:[1,0,1]\n"
    "v_pk_fma_f32 v[84:85], v[84:85], s[64:65], s[64:65] op_sel:[0,0,1] op_sel_hi:[1,0,1]\n"
    "v_pk_fma_f32 v[86:87], v[86:87], s[64:65], s[64:65] op_sel:[0,0,1] op_sel_hi:[1,0,1]\n"
    "v_pk_fma_f32 v[88:89], v[88:89], s[64:65], s[64:65] op_sel:[0,0,1] op_sel_hi:[1,0,1]\n"
    "v_pk_fma_f32 v[90:91], v[90:91], s[66:67], s[66:67] op_sel:[0,0,1] op_sel_hi:[1,0,1]\n"
    "v_pk_fma_f32 v[92:93], v[92:93], s[66:67], s[66:67] op_sel:[0,0,1] op_sel_hi:[1,0,1]\n"
    "v_pk_fma_f32 v[94:95], v[94:95], s[66:67], s[66:67] op_sel:[0,0,1] op_sel_hi:[1,0,1]\n"
    "v_pk_fma_f32 v[96:97], v[96:97], s[66:67], s[66:67] op_sel:[0,0,1] op_sel_hi:[1,0,1]\n"
    "v_pk_fma_f32 v[98:99], v[98:99], s[66:67], s[66:67] op_sel:[0,0,1] op_sel_hi:[1,0,1]\n"
    "v_pk_fma_f32 v[100:101], v[100:101], s[68:69], s[68:69] op_sel:[0,0,1] op_sel_hi:[1,0,1]\n"
    "v_pk_fma_f32 v[102:103], v[102:103], s[68:69], s[68:69] op_sel:[0,0,1] op_sel_hi:[1,0,1]\n"
    "v_pk_fma_f32 v[104:105], v[104:105], s[68:69], s[68:69] op_sel:[0,0,1] op_sel_hi:[1,0,1]\n"
    "v_pk_fma_f32 v[106:107], v[106:107], s[68:69], s[68:69] op_sel:[0,0,1] op_sel_hi:[1,0,1]\n"
    "v_pk_fma_f32 v[108:109], v[108:109], s[68:69], s[68:69] op_sel:[0,0,1] op_sel_hi:[1,0,1]\n"
    "v_pk_fma_f32 v[110:111], v[110:111], s[70:71], s[70:71] op_sel:[0,0,1] op_sel_hi:[1,0,1]\n"
    "buffer_load_dwordx4 a[44:47], v16, s[16:19], s51 offen offset:3072 sc1\n"
    "v_pk_fma_f32 v[112:113], v[112:113], s[70:71], s[70:71] op_sel:[0,0,1] op_sel_hi:[1,0,1]\n"
    "v_pk_fma_f32 v[114:115], v[114:115], s[70:71], s[70:71] op_sel:[0,0,1] op_sel_hi:[1,0,1]\n"
    "v_pk_fma_f32 v[116:117], v[116:117], s[70:71], s[70:71] op_sel:[0,0,1] op_sel_hi:[1,0,1]\n"
    "v_pk_fma_f32 v[118:119], v[118:119], s[70:71], s[70:71] op_sel:[0,0,1] op_sel_hi:[1,0,1]\n"
    "v_pk_fma_f32 v[120:121], v[120:121], s[72:73], s[72:73] op_sel:[0,0,1] op_sel_hi:[1,0,1]\n"
    "v_pk_fma_f32 v[122:123], v[122:123], s[72:73], s[72:73] op_sel:[0,0,1] op_sel_hi:[1,0,1]\n"
    "v_pk_fma_f32 v[124:125], v[124:125], s[72:73], s[72:73] op_sel:[0,0,1] op_sel_hi:[1,0,1]\n"
    "v_pk_fma_f32 v[126:127], v[126:127], s[72:73], s[72:73] op_sel:[0,0,1] op_sel_hi:[1,0,1]\n"
    "v_pk_fma_f32 v[128:129], v[128:129], s[72:73], s[72:73] op_sel:[0,0,1] op_sel_hi:[1,0,1]\n"
    "v_pk_fma_f32 v[130:131], v[130:131], s[74:75], s[74:75] op_sel:[0,0,1] op_sel_hi:[1,0,1]\n"
    "v_pk_fma_f32 v[132:133], v[132:133], s[74:75], s[74:75] op_sel:[0,0,1] op_sel_hi:[1,0,1]\n"
    "v_pk_fma_f32 v[134:135], v[134:135], s[74:75], s[74:75] op_sel:[0,0,1] op_sel_hi:[1,0,1]\n"
    "v_pk_fma_f32 v[136:137], v[136:137], s[74:75], s[74:75] op_sel:[0,0,1] op_sel_hi:[1,0,1]\n"
    "v_pk_fma_f32 v[138:139], v[138:139], s[74:75], s[74:75] op_sel:[0,0,1] op_sel_hi:[1,0,1]\n"
    "v_pk_fma_f32 v[140:141], v[140:141], s[76:77], s[76:77] op_sel:[0,0,1] op_sel_hi:[1,0,1]\n"
    "v_pk_fma_f32 v[142:143], v[142:143], s[76:77], s[76:77] op_sel:[0,0,1] op_sel_hi:[1,0,1]\n"
    "s_cmp_lt_u32 s2, 0x80\n"
    "s_cbranch_scc1 .Lsk1_%=\n"
    "buffer_store_dwordx4 a[220:223], v2, s[16:19], s9 offen sc1\n"
    ".Lsk1_%=:\n"
    "v_pk_fma_f32 v[144:145], v[144:145], s[76:77], s[76:77] op_sel:[0,0,1] op_sel_hi:[1,0,1]\n"
    "v_pk_fma_f32 v[146:147], v[146:147], s[76:77], s[76:77] op_sel:[0,0,1] op_sel_hi:[1,0,1]\n"
    "v_pk_fma_f32 v[148:149], v[148:149], s[76:77], s[76:77] op_sel:[0,0,1] op_sel_hi:[1,0,1]\n"
    "v_pk_fma_f32 v[150:151], v[150:151], s[78:79], s[78:79] op_sel:[0,0,1] op_sel_hi:[1,0,1]\n"
    "v_pk_fma_f32 v[152:153], v[152:153], s[78:79], s[78:79] op_sel:[0,0,1] op_sel_hi:[1,0,1]\n"
    "v_pk_fma_f32 v[154:155], v[154:155], s[78:79], s[78:79] op_sel:[0,0,1] op_sel_hi:[1,0,1]\n"
    "v_pk_fma_f32 v[156:157], v[156:157], s[78:79], s[78:79] op_sel:[0,0,1] op_sel_hi:[1,0,1]\n"
    "v_pk_fma_f32 v[158:159], v[158:159], s[78:79], s[78:79] op_sel:[0,0,1] op_sel_hi:[1,0,1]\n"
    "v_pk_fma_f32 v[160:161], v[160:161], s[80:81], s[80:81] op_sel:[0,0,1] op_sel_hi:[1,0,1]\n"
    "v_pk_fma_f32 v[162:163], v[162:163], s[80:81], s[80:81] op_sel:[0,0,1] op_sel_hi:[1,0,1]\n"
    "v_pk_fma_f32 v[164:165], v[164:165], s[80:81], s[80:81] op_sel:[0,0,1] op_sel_hi:[1,0,1]\n"
    "v_pk_fma_f32 v[166:167], v[166:167], s[80:81], s[80:81] op_sel:[0,0,1] op_sel_hi:[1,0,1]\n"
    "v_pk_fma_f32 v[168:169], v[168:169], s[80:81], s[80:81] op_sel:[0,0,1] op_sel_hi:[1,0,1]\n"
    "v_pk_fma_f32 v[170:171], v[170:171], s[82:83], s[82:83] op_sel:[0,0,1] op_sel_hi:[1,0,1]\n"
    "v_pk_fma_f32 v[172:173], v[172:173], s[82:83], s[82:83] op_sel:[0,0,1] op_sel_hi:[1,0,1]\n"
    "v_pk_fma_f32 v[174:175], v[174:175], s[82:83], s[82:83] op_sel:[0,0,1] op_sel_hi:[1,0,1]\n"
    "s_add_u32 s51, s51, 3840\n"
    "buffer_load_dwordx4 a[48:51], v2, s[16:19], s51 offen sc1\n"
    "v_pk_fma_f32 v[176:177], v[176:177], s[82:83], s[82:83] op_sel:[0,0,1] op_sel_hi:[1,0,1]\n"
    "v_pk_fma_f32 v[178:179], v[178:179], s[82:83], s[82:83] op_sel:[0,0,1] op_sel_hi:[1,0,1]\n"
    "v_pk_fma_f32 v[180:181], v[180:181], s[84:85], s[84:85] op_sel:[0,0,1] op_sel_hi:[1,0,1]\n"
    "v_pk_fma_f32 v[182:183], v[182:183], s[84:85], s[84:85] op_sel:[0,0,1] op_sel_hi:[1,0,1]\n"
    "v_pk_fma_f32 v[184:185], v[184:185], s[84:85], s[84:85] op_sel:[0,0,1] op_sel_hi:[1,0,1]\n"
    "v_pk_fma_f32 v[186:187], v[186:187], s[84:85], s[84:85] op_sel:[0,0,1] op_sel_hi:[1,0,1]\n"
    "v_pk_fma_f32 v[188:189], v[188:189], s[84:85], s[84:85] op_sel:[0,0,1] op_sel_hi:[1,0,1]\n"
    "v_pk_fma_f32 v[190:191], v[190:191], s[86:87], s[86:87] op_sel:[0,0,1] op_sel_hi:[1,0,1]\n"
    "v_pk_fma_f32 v[192:193], v[192:193], s[86:87], s[86:87] op_sel:[0,0,1] op_sel_hi:[1,0,1]\n"
    "v_pk_fma_f32 v[194:195], v[194:195], s[86:87], s[86:87] op_sel:[0,0,1] op_sel_hi:[1,0,1]\n"
    "v_pk_fma_f32 v[196:197], v[196:197], s[86:87], s[86:87] op_sel:[0,0,1] op_sel_hi:[1,0,1]\n"
    "v_pk_fma_f32 v[198:199], v[198:199], s[86:87], s[86:87] op_sel:[0,0,1] op_sel_hi:[1,0,1]\n"
    "v_pk_fma_f32 v[200:201], v[200:201], s[88:89], s[88:89] op_sel:[0,0,1] op_sel_hi:[1,0,1]\n"
    "v_pk_fma_f32 v[202:203], v[202:203], s[88:89], s[88:89] op_sel:[0,0,1] op_sel_hi:[1,0,1]\n"
    "v_pk_fma_f32 v[204:205], v[204:205], s[88:89], s[88:89] op_sel:[0,0,1] op_sel_hi:[1,0,1]\n"
    "v_pk_fma_f32 v[206:207], v[206:207], s[88:89], s[88:89] op_sel:[0,0,1] op_sel_hi:[1,0,1]\n"
    "buffer_load_dwordx4 a[52:55], v2, s[16:19], s51 offen offset:1024 sc1\n"
    "v_pk_fma_f32 v[208:209], v[208:209], s[88:89], s[88:89] op_sel:[0,0,1] op_sel_hi:[1,0,1]\n"
    "v_pk_fma_f32 v[210:211], v[210:211], s[90:91], s[90:91] op_sel:[0,0,1] op_sel_hi:[1,0,1]\n"
    "v_pk_fma_f32 v[212:213], v[212:213], s[90:91], s[90:91] op_sel:[0,0,1] op_sel_hi:[1,0,1]\n"
    "v_pk_fma_f32 v[214:215], v[214:215], s[90:91], s[90:91] op_sel:[0,0,1] op_sel_hi:[1,0,1]\n"
    "v_pk_fma_f32 v[216:217], v[216:217], s[90:91], s[90:91] op_sel:[0,0,1] op_sel_hi:[1,0,1]\n"
    "v_pk_fma_f32 v[218:219], v[218:219], s[90:91], s[90:91] op_sel:[0,0,1] op_sel_hi:[1,0,1]\n"
    "v_lshrrev_b32 v24, 1, v6\n"
    "v_add_u32 v32, 0x26100, v24\n"
    "v_lshrrev_b32 v24, 1, v34\n"
    "v_add_u32 v33, 0x26900, v24\n"
    "v_lshlrev_b32 v24, 4, v1\n"
    "v_add_u32 v38, 0x26100, v24\n"
    "v_lshlrev_b32 v24, 2, v1\n"
    "v_add_u32 v39, 0x26100, v24\n"
    "s_mov_b32 s14, 0x8000\n"
    "s_waitcnt vmcnt(17)\n"
    "v_xor_b32 v24, s40, v13\n"
    "v_bitop3_b32 v24, v24, v15, s40 bitop3:0xf6\n"
    "s_waitcnt vmcnt(16)\n"
    "v_bitop3_b32 v24, v24, v5, s40 bitop3:0xf6\n"
    "v_cmp_ne_u32 vcc, 0, v24\n"
    "s_nop 1\n"
    "s_cbranch_vccz .Ld2_%=\n"
    ".Lr2_%=:\n"
    "s_cmp_eq_u32 s42, 0\n"
    "s_cbranch_scc1 .Ld2_%=\n"
    "s_sub_u32 s42, s42, 1\n"
    "buffer_load_dwordx4 v[12:15], v6, s[16:19], s45 offen sc1\n"
    "buffer_load_dwordx2 v[4:5], v34, s[16:19], s46 offen sc1\n"
    "s_waitcnt vmcnt(0)\n"
    "v_xor_b32 v24, s40, v13\n"
    "v_bitop3_b32 v24, v24, v15, s40 bitop3:0xf6\n"
    "v_bitop3_b32 v24, v24, v5, s40 bitop3:0xf6\n"
    "v_cmp_ne_u32 vcc, 0, v24\n"
    "s_nop 1\n"
    "s_cbranch_vccnz .Lr2_%=\n"
    ".Ld2_%=:\n"
    "ds_write2_b32 v32, v12, v14 offset1:1\n"
    "ds_write_b32 v33, v4\n"
    "s_waitcnt lgkmcnt(0)\n"
    "s_barrier\n"
    "ds_read_b128 v[24:27], v38 offset:1280\n"
    "ds_read_b32 v32, v39 offset:2304\n"
    "ds_read_b128 v[28:31], v38\n"
    "ds_read_b32 v33, v39 offset:1024\n"
    "s_mul_i32 s96, s36, 0x5c40\n"
    "v_lshl_add_u32 v38, v1, 4, s96\n"
    "v_lshl_add_u32 v39, v1, 2, s96\n"
    "s_waitcnt lgkmcnt(2)\n"
    "v_lshlrev_b32 v220, 16, v32\n"
    "v_and_b32 v221, 0xffff0000, v32\n"
    "v_lshlrev_b32 v222, 16, v24\n"
    "v_and_b32 v223, 0xffff0000, v24\n"
    "v_lshlrev_b32 v224, 16, v25\n"
    "v_and_b32 v225, 0xffff0000, v25\n"
    "v_lshlrev_b32 v226, 16, v26\n"
    "v_and_b32 v227, 0xffff0000, v26\n"
    "v_lshlrev_b32 v228, 16, v27\n"
    "v_and_b32 v229, 0xffff0000, v27\n"
    "s_waitcnt lgkmcnt(0)\n"
    "v_lshlrev_b32 v230, 16, v33\n"
    "v_and_b32 v231, 0xffff0000, v33\n"
    "v_lshlrev_b32 v232, 16, v28\n"
    "v_and_b32 v233, 0xffff0000, v28\n"
    "v_lshlrev_b32 v234, 16, v29\n"
    "v_and_b32 v235, 0xffff0000, v29\n"
    "v_lshlrev_b32 v236, 16, v30\n"
    "v_and_b32 v237, 0xffff0000, v30\n"
    "v_lshlrev_b32 v238, 16, v31\n"
    "v_and_b32 v239, 0xffff0000, v31\n"
    "v_lshrrev_b32 v35, 4, v1\n"
    "v_and_b32 v36, 15, v1\n"
    "v_add_u32 v36, 8, v36\n"
    "v_mul_u32_u24 v36, 0x520, v36\n"
    "v_lshl_add_u32 v4, v35, 4, v36\n"
    "v_lshrrev_b32 v35, 4, v1\n"
    "v_and_b32 v36, 7, v1\n"
    "v_mul_u32_u24 v36, 0x520, v36\n"
    "v_lshl_add_u32 v5, v35, 4, v36\n"
    "v_mov_b32 v6, v16\n"
    "s_cmp_lt_u32 s38, 2\n"
    "s_cselect_b32 s76, 0xf149f2ca, 0\n"
    "v_pk_fma_f32 v[40:41], v[40:41], v[220:221], v[230:231]\n"
    "buffer_load_dwordx4 a[56:59], v2, s[16:19], s51 offen offset:2048 sc1\n"
    "v_pk_fma_f32 v[42:43], v[42:43], v[222:223], v[232:233]\n"
    "v_pk_fma_f32 v[44:45], v[44:45], v[224:225], v[234:235]\n"
    "v_pk_fma_f32 v[46:47], v[46:47], v[226:227], v[236:237]\n"
    "v_pk_fma_f32 v[48:49], v[48:49], v[228:229], v[238:239]\n"
    "v_cvt_pk_bf16_f32 v40, v40, v41\n"
    "v_cvt_pk_bf16_f32 v42, v42, v43\n"
    "v_cvt_pk_bf16_f32 v43, v44, v45\n"
    "v_cvt_pk_bf16_f32 v44, v46, v47\n"
    "buffer_load_dwordx4 a[60:63], v6, s[16:19], s51 offen offset:3072 sc1\n"
    "v_cvt_pk_bf16_f32 v45, v48, v49\n"
    "ds_write_b128 v38, v[42:45]\n"
    "ds_write_b32 v39, v40 offset:1024\n"
    "v_pk_fma_f32 v[50:51], v[50:51], v[220:221], v[230:231]\n"
    "v_pk_fma_f32 v[52:53], v[52:53], v[222:223], v[232:233]\n"
    "v_pk_fma_f32 v[54:55], v[54:55], v[224:225], v[234:235]\n"
    "v_pk_fma_f32 v[56:57], v[56:57], v[226:227], v[236:237]\n"
    "buffer_store_dwordx4 a[244:247], v18, s[16:19], s32 offen offset:2048 sc1\n"
    "v_pk_fma_f32 v[58:59], v[58:59], v[228:229], v[238:239]\n"
    "v_cvt_pk_bf16_f32 v50, v50, v51\n"
    "v_cvt_pk_bf16_f32 v52, v52, v53\n"
    "v_cvt_pk_bf16_f32 v53, v54, v55\n"
    "v_cvt_pk_bf16_f32 v54, v56, v57\n"
    "v_cvt_pk_bf16_f32 v55, v58, v59\n"
    "ds_write_b128 v38, v[52:55] offset:1312\n"
    "ds_write_b32 v39, v50 offset:2336\n"
    "s_add_u32 s51, s51, 3840\n"
    "buffer_load_dwordx4 a[64:67], v2, s[16:19], s51 offen sc1\n"
    "v_pk_fma_f32 v[60:61], v[60:61], v[220:221], v[230:231]\n"
    "v_pk_fma_f32 v[62:63], v[62:63], v[222:223], v[232:233]\n"
    "v_pk_fma_f32 v[64:65], v[64:65], v[224:225], v[234:235]\n"
    "v_pk_fma_f32 v[66:67], v[66:67], v[226:227], v[236:237]\n"
    "v_pk_fma_f32 v[68:69], v[68:69], v[228:229], v[238:239]\n"
    "v_cvt_pk_bf16_f32 v60, v60, v61\n"
    "v_cvt_pk_bf16_f32 v62, v62, v63\n"
    "buffer_load_dwordx4 a[68:71], v2, s[16:19], s51 offen offset:1024 sc1\n"
    "v_cvt_pk_bf16_f32 v63, v64, v65\n"
    "v_cvt_pk_bf16_f32 v64, v66, v67\n"
    "v_cvt_pk_bf16_f32 v65, v68, v69\n"
    "ds_write_b128 v38, v[62:65] offset:2624\n"
    "ds_write_b32 v39, v60 offset:3648\n"
    "v_pk_fma_f32 v[70:71], v[70:71], v[220:221], v[230:231]\n"
    "v_pk_fma_f32 v[72:73], v[72:73], v[222:223], v[232:233]\n"
    "v_pk_fma_f32 v[74:75], v[74:75], v[224:225], v[234:235]\n"
    "buffer_load_dwordx4 a[72:75], v2, s[16:19], s51 offen offset:2048 sc1\n"
    "v_pk_fma_f32 v[76:77], v[76:77], v[226:227], v[236:237]\n"
    "v_pk_fma_f32 v[78:79], v[78:79], v[228:229], v[238:239]\n"
    "v_cvt_pk_bf16_f32 v70, v70, v71\n"
    "v_cvt_pk_bf16_f32 v72, v72, v73\n"
    "v_cvt_pk_bf16_f32 v73, v74, v75\n"
    "v_cvt_pk_bf16_f32 v74, v76, v77\n"
    "v_cvt_pk_bf16_f32 v75, v78, v79\n"
    "s_cmp_lt_u32 s2, 0x80\n"
    "s_cbranch_scc1 .Lsk2_%=\n"
    "buffer_store_dwordx4 a[224:227], v2, s[16:19], s10 offen sc1\n"
    ".Lsk2_%=:\n"
    "ds_write_b128 v38, v[72:75] offset:3936\n"
    "ds_write_b32 v39, v70 offset:4960\n"
    "v_pk_fma_f32 v[80:81], v[80:81], v[220:221], v[230:231]\n"
    "v_pk_fma_f32 v[82:83], v[82:83], v[222:223], v[232:233]\n"
    "v_pk_fma_f32 v[84:85], v[84:85], v[224:225], v[234:235]\n"
    "v_pk_fma_f32 v[86:87], v[86:87], v[226:227], v[236:237]\n"
    "v_pk_fma_f32 v[88:89], v[88:89], v[228:229], v[238:239]\n"
    "v_cvt_pk_bf16_f32 v80, v80, v81\n"
    "buffer_load_dwordx4 a[76:79], v6, s[16:19], s51 offen offset:3072 sc1\n"
    "v_cvt_pk_bf16_f32 v82, v82, v83\n"
    "v_cvt_pk_bf16_f32 v83, v84, v85\n"
    "v_cvt_pk_bf16_f32 v84, v86, v87\n"
    "v_cvt_pk_bf16_f32 v85, v88, v89\n"
    "ds_write_b128 v38, v[82:85] offset:5248\n"
    "ds_write_b32 v39, v80 offset:6272\n"
    "v_pk_fma_f32 v[90:91], v[90:91], v[220:221], v[230:231]\n"
    "s_add_u32 s51, s51, 3840\n"
    "buffer_load_dwordx4 a[80:83], v2, s[16:19], s51 offen sc1\n"
    "v_pk_fma_f32 v[92:93], v[92:93], v[222:223], v[232:233]\n"
    "v_pk_fma_f32 v[94:95], v[94:95], v[224:225], v[234:235]\n"
    "v_pk_fma_f32 v[96:97], v[96:97], v[226:227], v[236:237]\n"
    "v_pk_fma_f32 v[98:99], v[98:99], v[228:229], v[238:239]\n"
    "v_cvt_pk_bf16_f32 v90, v90, v91\n"
    "v_cvt_pk_bf16_f32 v92, v92, v93\n"
    "v_cvt_pk_bf16_f32 v93, v94, v95\n"
    "v_cvt_pk_bf16_f32 v94, v96, v97\n"
    "buffer_load_dwordx4 a[84:87], v2, s[16:19], s51 offen offset:1024 sc1\n"
    "v_cvt_pk_bf16_f32 v95, v98, v99\n"
    "ds_write_b128 v38, v[92:95] offset:6560\n"
    "ds_write_b32 v39, v90 offset:7584\n"
    "v_pk_fma_f32 v[100:101], v[100:101], v[220:221], v[230:231]\n"
    "v_pk_fma_f32 v[102:103], v[102:103], v[222:223], v[232:233]\n"
    "v_pk_fma_f32 v[104:105], v[104:105], v[224:225], v[234:235]\n"
    "v_pk_fma_f32 v[106:107], v[106:107], v[226:227], v[236:237]\n"
    "buffer_store_dwordx4 a[244:247], v17, s[16:19], 0 offen sc1\n"
    "v_pk_fma_f32 v[108:109], v[108:109], v[228:229], v[238:239]\n"
    "v_cvt_pk_bf16_f32 v100, v100, v101\n"
    "v_cvt_pk_bf16_f32 v102, v102, v103\n"
    "v_cvt_pk_bf16_f32 v103, v104, v105\n"
    "v_cvt_pk_bf16_f32 v104, v106, v107\n"
    "v_cvt_pk_bf16_f32 v105, v108, v109\n"
    "ds_write_b128 v38, v[102:105] offset:7872\n"
    "buffer_load_dwordx4 a[88:91], v2, s[16:19], s51 offen offset:2048 sc1\n"
    "ds_write_b32 v39, v100 offset:8896\n"
    "v_pk_fma_f32 v[110:111], v[110:111], v[220:221], v[230:231]\n"
    "v_pk_fma_f32 v[112:113], v[112:113], v[222:223], v[232:233]\n"
    "v_pk_fma_f32 v[114:115], v[114:115], v[224:225], v[234:235]\n"
    "v_pk_fma_f32 v[116:117], v[116:117], v[226:227], v[236:237]\n"
    "v_pk_fma_f32 v[118:119], v[118:119], v[228:229], v[238:239]\n"
    "v_cvt_pk_bf16_f32 v110, v110, v111\n"
    "v_cvt_pk_bf16_f32 v112, v112, v113\n"
    "buffer_load_dwordx4 a[92:95], v6, s[16:19], s51 offen offset:3072 sc1\n"
    "v_cvt_pk_bf16_f32 v113, v114, v115\n"
    "v_cvt_pk_bf16_f32 v114, v116, v117\n"
    "v_cvt_pk_bf16_f32 v115, v118, v119\n"
    "ds_write_b128 v38, v[112:115] offset:9184\n"
    "ds_write_b32 v39, v110 offset:10208\n"
    "v_pk_fma_f32 v[120:121], v[120:121], v[220:221], v[230:231]\n"
    "v_pk_fma_f32 v[122:123], v[122:123], v[222:223], v[232:233]\n"
    "s_add_u32 s51, s51, 3840\n"
    "buffer_load_dwordx4 a[96:99], v2, s[16:19], s51 offen sc1\n"
    "v_pk_fma_f32 v[124:125], v[124:125], v[224:225], v[234:235]\n"
    "v_pk_fma_f32 v[126:127], v[126:127], v[226:227], v[236:237]\n"
    "v_pk_fma_f32 v[128:129], v[128:129], v[228:229], v[238:239]\n"
    "v_cvt_pk_bf16_f32 v120, v120, v121\n"
    "v_cvt_pk_bf16_f32 v122, v122, v123\n"
    "v_cvt_pk_bf16_f32 v123, v124, v125\n"
    "v_cvt_pk_bf16_f32 v124, v126, v127\n"
    "v_cvt_pk_bf16_f32 v125, v128, v129\n"
    "s_cmp_lt_u32 s2, 0x80\n"
    "s_cbranch_scc1 .Lsk3_%=\n"
    "buffer_store_dwordx4 a[228:231], v2, s[16:19], s11 offen sc1\n"
    ".Lsk3_%=:\n"
    "ds_write_b128 v38, v[122:125] offset:10496\n"
    "ds_write_b32 v39, v120 offset:11520\n"
    "v_pk_fma_f32 v[130:131], v[130:131], v[220:221], v[230:231]\n"
    "v_pk_fma_f32 v[132:133], v[132:133], v[222:223], v[232:233]\n"
    "v_pk_fma_f32 v[134:135], v[134:135], v[224:225], v[234:235]\n"
    "v_pk_fma_f32 v[136:137], v[136:137], v[226:227], v[236:237]\n"
    "v_pk_fma_f32 v[138:139], v[138:139], v[228:229], v[238:239]\n"
    "buffer_load_dwordx4 a[100:103], v2, s[16:19], s51 offen offset:1024 sc1\n"
    "v_cvt_pk_bf16_f32 v130, v130, v131\n"
    "v_cvt_pk_bf16_f32 v132, v132, v133\n"
    "v_cvt_pk_bf16_f32 v133, v134, v135\n"
    "v_cvt_pk_bf16_f32 v134, v136, v137\n"
    "v_cvt_pk_bf16_f32 v135, v138, v139\n"
    "ds_write_b128 v38, v[132:135] offset:11808\n"
    "ds_write_b32 v39, v130 offset:12832\n"
    "v_pk_fma_f32 v[140:141], v[140:141], v[220:221], v[230:231]\n"
    "buffer_load_dwordx4 a[104:107], v2, s[16:19], s51 offen offset:2048 sc1\n"
    "v_pk_fma_f32 v[142:143], v[142:143], v[222:223], v[232:233]\n"
    "v_pk_fma_f32 v[144:145], v[144:145], v[224:225], v[234:235]\n"
    "v_pk_fma_f32 v[146:147], v[146:147], v[226:227], v[236:237]\n"
    "v_pk_fma_f32 v[148:149], v[148:149], v[228:229], v[238:239]\n"
    "v_cvt_pk_bf16_f32 v140, v140, v141\n"
    "v_cvt_pk_bf16_f32 v142, v142, v143\n"
    "v_cvt_pk_bf16_f32 v143, v144, v145\n"
    "buffer_load_dwordx4 a[108:111], v6, s[16:19], s51 offen offset:3072 sc1\n"
    "v_cvt_pk_bf16_f32 v144, v146, v147\n"
    "v_cvt_pk_bf16_f32 v145, v148, v149\n"
    "ds_write_b128 v38, v[142:145] offset:13120\n"
    "ds_write_b32 v39, v140 offset:14144\n"
    "v_pk_fma_f32 v[150:151], v[150:151], v[220:221], v[230:231]\n"
    "v_pk_fma_f32 v[152:153], v[152:153], v[222:223], v[232:233]\n"
    "v_pk_fma_f32 v[154:155], v[154:155], v[224:225], v[234:235]\n"
    "v_pk_fma_f32 v[156:157], v[156:157], v[226:227], v[236:237]\n"
    "s_cmp_lt_u32 s2, 0x80\n"
    "s_cbranch_scc1 .Lsk4_%=\n"
    "buffer_store_dwordx4 a[232:235], v23, s[16:19], s8 offen sc1\n"
    ".Lsk4_%=:\n"
    "v_pk_fma_f32 v[158:159], v[158:159], v[228:229], v[238:239]\n"
    "v_cvt_pk_bf16_f32 v150, v150, v151\n"
    "v_cvt_pk_bf16_f32 v152, v152, v153\n"
    "v_cvt_pk_bf16_f32 v153, v154, v155\n"
    "v_cvt_pk_bf16_f32 v154, v156, v157\n"
    "v_cvt_pk_bf16_f32 v155, v158, v159\n"
    "ds_write_b128 v38, v[152:155] offset:14432\n"
    "s_add_u32 s51, s51, 3840\n"
    "buffer_load_dwordx4 a[112:115], v2, s[16:19], s51 offen sc1\n"
    "ds_write_b32 v39, v150 offset:15456\n"
    "v_pk_fma_f32 v[160:161], v[160:161], v[220:221], v[230:231]\n"
    "v_pk_fma_f32 v[162:163], v[162:163], v[222:223], v[232:233]\n"
    "v_pk_fma_f32 v[164:165], v[164:165], v[224:225], v[234:235]\n"
    "v_pk_fma_f32 v[166:167], v[166:167], v[226:227], v[236:237]\n"
    "v_pk_fma_f32 v[168:169], v[168:169], v[228:229], v[238:239]\n"
    "v_cvt_pk_bf16_f32 v160, v160, v161\n"
    "buffer_load_dwordx4 a[116:119], v2, s[16:19], s51 offen offset:1024 sc1\n"
    "v_cvt_pk_bf16_f32 v162, v162, v163\n"
    "v_cvt_pk_bf16_f32 v163, v164, v165\n"
    "v_cvt_pk_bf16_f32 v164, v166, v167\n"
    "v_cvt_pk_bf16_f32 v165, v168, v169\n"
    "ds_write_b128 v38, v[162:165] offset:15744\n"
    "ds_write_b32 v39, v160 offset:16768\n"
    "v_pk_fma_f32 v[170:171], v[170:171], v[220:221], v[230:231]\n"
    "v_pk_fma_f32 v[172:173], v[172:173], v[222:223], v[232:233]\n"
    "buffer_load_dwordx4 a[120:123], v2, s[16:19], s51 offen offset:2048 sc1\n"
    "v_pk_fma_f32 v[174:175], v[174:175], v[224:225], v[234:235]\n"
    "v_pk_fma_f32 v[176:177], v[176:177], v[226:227], v[236:237]\n"
    "v_pk_fma_f32 v[178:179], v[178:179], v[228:229], v[238:239]\n"
    "v_cvt_pk_bf16_f32 v170, v170, v171\n"
    "v_cvt_pk_bf16_f32 v172, v172, v173\n"
    "v_cvt_pk_bf16_f32 v173, v174, v175\n"
    "v_cvt_pk_bf16_f32 v174, v176, v177\n"
    "buffer_store_dwordx4 a[236:239], v2, s[16:19], s12 offen sc1\n"
    "v_cvt_pk_bf16_f32 v175, v178, v179\n"
    "ds_write_b128 v38, v[172:175] offset:17056\n"
    "ds_write_b32 v39, v170 offset:18080\n"
    "v_pk_fma_f32 v[180:181], v[180:181], v[220:221], v[230:231]\n"
    "v_pk_fma_f32 v[182:183], v[182:183], v[222:223], v[232:233]\n"
    "v_pk_fma_f32 v[184:185], v[184:185], v[224:225], v[234:235]\n"
    "v_pk_fma_f32 v[186:187], v[186:187], v[226:227], v[236:237]\n"
    "v_pk_fma_f32 v[188:189], v[188:189], v[228:229], v[238:239]\n"
    "buffer_load_dwordx4 a[124:127], v6, s[16:19], s51 offen offset:3072 sc1\n"
    "v_cvt_pk_bf16_f32 v180, v180, v181\n"
    "v_cvt_pk_bf16_f32 v182, v182, v183\n"
    "v_cvt_pk_bf16_f32 v183, v184, v185\n"
    "v_cvt_pk_bf16_f32 v184, v186, v187\n"
    "v_cvt_pk_bf16_f32 v185, v188, v189\n"
    "ds_write_b128 v38, v[182:185] offset:18368\n"
    "ds_write_b32 v39, v180 offset:19392\n"
    "s_add_u32 s51, s51, 3840\n"
    "buffer_load_dwordx4 a[128:131], v2, s[16:19], s51 offen sc1\n"
    "v_pk_fma_f32 v[190:191], v[190:191], v[220:221], v[230:231]\n"
    "v_pk_fma_f32 v[192:193], v[192:193], v[222:223], v[232:233]\n"
    "v_pk_fma_f32 v[194:195], v[194:195], v[224:225], v[234:235]\n"
    "v_pk_fma_f32 v[196:197], v[196:197], v[226:227], v[236:237]\n"
    "v_pk_fma_f32 v[198:199], v[198:199], v[228:229], v[238:239]\n"
    "v_cvt_pk_bf16_f32 v190, v190, v191\n"
    "v_cvt_pk_bf16_f32 v192, v192, v193\n"
    "v_cvt_pk_bf16_f32 v193, v194, v195\n"
    "buffer_load_dwordx4 a[132:135], v2, s[16:19], s51 offen offset:1024 sc1\n"
    "v_cvt_pk_bf16_f32 v194, v196, v197\n"
    "v_cvt_pk_bf16_f32 v195, v198, v199\n"
    "ds_write_b128 v38, v[192:195] offset:19680\n"
    "ds_write_b32 v39, v190 offset:20704\n"
    "v_pk_fma_f32 v[200:201], v[200:201], v[220:221], v[230:231]\n"
    "v_pk_fma_f32 v[202:203], v[202:203], v[222:223], v[232:233]\n"
    "v_pk_fma_f32 v[204:205], v[204:205], v[224:225], v[234:235]\n"
    "buffer_load_dwordx4 a[136:139], v2, s[16:19], s51 offen offset:2048 sc1\n"
    "v_pk_fma_f32 v[206:207], v[206:207], v[226:227], v[236:237]\n"
    "v_pk_fma_f32 v[208:209], v[208:209], v[228:229], v[238:239]\n"
    "v_cvt_pk_bf16_f32 v200, v200, v201\n"
    "v_cvt_pk_bf16_f32 v202, v202, v203\n"
    "v_cvt_pk_bf16_f32 v203, v204, v205\n"
    "v_cvt_pk_bf16_f32 v204, v206, v207\n"
    "v_cvt_pk_bf16_f32 v205, v208, v209\n"
    "ds_write_b128 v38, v[202:205] offset:20992\n"
    "buffer_store_dwordx4 a[240:243], v2, s[16:19], s13 offen sc1\n"
    "ds_write_b32 v39, v200 offset:22016\n"
    "v_pk_fma_f32 v[210:211], v[210:211], v[220:221], v[230:231]\n"
    "v_pk_fma_f32 v[212:213], v[212:213], v[222:223], v[232:233]\n"
    "v_pk_fma_f32 v[214:215], v[214:215], v[224:225], v[234:235]\n"
    "v_pk_fma_f32 v[216:217], v[216:217], v[226:227], v[236:237]\n"
    "v_pk_fma_f32 v[218:219], v[218:219], v[228:229], v[238:239]\n"
    "v_cvt_pk_bf16_f32 v210, v210, v211\n"
    "buffer_load_dwordx4 a[140:143], v6, s[16:19], s51 offen offset:3072 sc1\n"
    "v_cvt_pk_bf16_f32 v212, v212, v213\n"
    "v_cvt_pk_bf16_f32 v213, v214, v215\n"
    "v_cvt_pk_bf16_f32 v214, v216, v217\n"
    "v_cvt_pk_bf16_f32 v215, v218, v219\n"
    "ds_write_b128 v38, v[212:215] offset:22304\n"
    "ds_write_b32 v39, v210 offset:23328\n"
    "s_waitcnt lgkmcnt(0)\n"
    "s_barrier\n"
    "ds_read_b128 v[116:119], v5\n"
    "ds_read_b128 v[120:123], v4\n"
    "ds_read_b128 v[124:127], v4 offset:20992\n"
    "ds_read_b128 v[128:131], v4 offset:41984\n"
    "ds_read_b128 v[132:135], v4 offset:62976\n"
    "s_waitcnt vmcnt(39) lgkmcnt(4)\n"
    "v_mfma_f32_16x16x32_bf16 v[56:59], a[4:7], v[116:119], 0\n"
    "ds_read_b128 v[136:139], v5 offset:64\n"
    "s_add_u32 s51, s51, 3840\n"
    "s_waitcnt vmcnt(38)\n"
    "v_mfma_f32_16x16x32_bf16 v[96:99], v[116:119], a[8:11], 0\n"
    "buffer_load_dwordx4 a[144:147], v2, s[16:19], s51 offen sc1\n"
    "s_waitcnt vmcnt(38)\n"
    "v_mfma_f32_16x16x32_bf16 v[76:79], a[12:15], v[116:119], 0\n"
    "ds_read_b128 v[140:143], v4 offset:64\n"
    "v_mov_b32 v250, -1\n"
    "s_waitcnt lgkmcnt(5)\n"
    "v_mfma_f32_16x16x32_bf16 v[40:43], a[0:3], v[120:123], 0\n"
    "v_mov_b32 v251, -1\n"
    "v_mov_b32 v252, -1\n"
    "v_mfma_f32_16x16x32_bf16 v[60:63], a[4:7], v[120:123], 0\n"
    "ds_read_b128 v[144:147], v4 offset:21056\n"
    "v_mov_b32 v253, -1\n"
    "v_mfma_f32_16x16x32_bf16 v[100:103], v[120:123], a[8:11], 0\n"
    "buffer_load_dwordx4 a[148:151], v2, s[16:19], s51 offen offset:1024 sc1\n"
    "v_mfma_f32_16x16x32_bf16 v[80:83], a[12:15], v[120:123], 0\n"
    "ds_read_b128 v[148:151], v4 offset:42048\n"
    "s_lshl_b32 s92, s41, 12\n"
    "s_waitcnt lgkmcnt(6)\n"
    "v_mfma_f32_16x16x32_bf16 v[44:47], a[0:3], v[124:127], 0\n"
    "s_lshl_b32 s93, s37, 9\n"
    "s_add_u32 s92, s92, s93\n"
    "v_mfma_f32_16x16x32_bf16 v[64:67], a[4:7], v[124:127], 0\n"
    "ds_read_b128 v[152:155], v4 offset:63040\n"
    "s_add_u32 s92, s92, 0x9000\n"
    "v_mfma_f32_16x16x32_bf16 v[104:107], v[124:127], a[8:11], 0\n"
    "v_lshl_add_u32 v32, v1, 3, s92\n"
    "v_and_b32 v24, 31, v1\n"
    "v_mfma_f32_16x16x32_bf16 v[84:87], a[12:15], v[124:127], 0\n"
    "buffer_load_dwordx4 a[152:155], v2, s[16:19], s51 offen offset:2048 sc1\n"
    "s_waitcnt lgkmcnt(6)\n"
    "v_mfma_f32_16x16x32_bf16 v[48:51], a[0:3], v[128:131], 0\n"
    "s_lshl_b32 s92, s38, 7\n"
    "s_add_u32 s92, s92, 0xa8000\n"
    "v_mfma_f32_16x16x32_bf16 v[68:71], a[4:7], v[128:131], 0\n"
    "v_lshl_add_u32 v33, v24, 2, s92\n"
    "v_mfma_f32_16x16x32_bf16 v[108:111], v[128:131], a[8:11], 0\n"
    "v_lshlrev_b32 v208, 4, v0\n"
    "v_mfma_f32_16x16x32_bf16 v[88:91], a[12:15], v[128:131], 0\n"
    "v_min_u32 v209, 0x7f0, v208\n"
    "s_waitcnt lgkmcnt(5)\n"
    "v_mfma_f32_16x16x32_bf16 v[52:55], a[0:3], v[132:135], 0\n"
    "buffer_load_dwordx4 a[156:159], v6, s[16:19], s51 offen offset:3072 sc1\n"
    "v_mfma_f32_16x16x32_bf16 v[72:75], a[4:7], v[132:135], 0\n"
    "s_add_u32 s47, s46, 0x1000\n"
    "s_add_u32 s48, s46, 0x2000\n"
    "v_mfma_f32_16x16x32_bf16 v[112:115], v[132:135], a[8:11], 0\n"
    "v_lshlrev_b32 v24, 3, v0\n"
    "v_mfma_f32_16x16x32_bf16 v[92:95], a[12:15], v[132:135], 0\n"
    "s_waitcnt vmcnt(37) lgkmcnt(0)\n"
    "v_add_u32 v210, 0x26b00, v24\n"
    "v_mfma_f32_16x16x32_bf16 v[56:59], a[20:23], v[136:139], v[56:59]\n"
    "ds_read_b128 v[116:119], v5 offset:128\n"
    "v_add_u32 v211, 0x800, v210\n"
    "v_mfma_f32_16x16x32_bf16 v[96:99], v[136:139], a[24:27], v[96:99]\n"
    "v_lshrrev_b32 v24, 1, v209\n"
    "v_mfma_f32_16x16x32_bf16 v[76:79], a[28:31], v[136:139], v[76:79]\n"
    "ds_read_b128 v[120:123], v4 offset:128\n"
    "s_add_u32 s51, s51, 3840\n"
    "v_mfma_f32_16x16x32_bf16 v[40:43], a[16:19], v[140:143], v[40:43]\n"
    "buffer_load_dwordx4 a[160:163], v2, s[16:19], s51 offen sc1\n"
    "v_mfma_f32_16x16x32_bf16 v[60:63], a[20:23], v[140:143], v[60:63]\n"
    "ds_read_b128 v[124:127], v4 offset:21120\n"
    "v_add_u32 v212, 0x27b00, v24\n"
    "v_mfma_f32_16x16x32_bf16 v[100:103], v[140:143], a[24:27], v[100:103]\n"
    "v_lshrrev_b32 v38, 4, v1\n"
    "v_and_b32 v39, 15, v1\n"
    "v_mfma_f32_16x16x32_bf16 v[80:83], a[28:31], v[140:143], v[80:83]\n"
    "ds_read_b128 v[128:131], v4 offset:42112\n"
    "s_lshr_b32 s78, s37, 1\n"
    "v_mfma_f32_16x16x32_bf16 v[44:47], a[16:19], v[144:147], v[44:47]\n"
    "buffer_load_dwordx4 a[164:167], v2, s[16:19], s51 offen offset:1024 sc1\n"
    "v_mfma_f32_16x16x32_bf16 v[64:67], a[20:23], v[144:147], v[64:67]\n"
    "ds_read_b128 v[132:135], v4 offset:63104\n"
    "s_and_b32 s79, s37, 1\n"
    "v_mfma_f32_16x16x32_bf16 v[104:107], v[144:147], a[24:27], v[104:107]\n"
    "v_cmp_gt_u32 vcc, 32, v1\n"
    "s_nop 1\n"
    "v_cndmask_b32 v14, 0x40000000, v2, vcc\n"
    "v_mfma_f32_16x16x32_bf16 v[84:87], a[28:31], v[144:147], v[84:87]\n"
    "s_lshl_b32 s92, s38, 3\n"
    "s_add_u32 s92, s92, s37\n"
    "v_mfma_f32_16x16x32_bf16 v[48:51], a[16:19], v[148:151], v[48:51]\n"
    "s_waitcnt vmcnt(7)\n"
    "buffer_store_dword v3, v9, s[16:19], s14 offen sc1\n"
    "v_mfma_f32_16x16x32_bf16 v[68:71], a[20:23], v[148:151], v[68:71]\n"
    "buffer_load_dwordx4 a[168:171], v2, s[16:19], s51 offen offset:2048 sc1\n"
    "v_mfma_f32_16x16x32_bf16 v[108:111], v[148:151], a[24:27], v[108:111]\n"
    "s_lshl_b32 s92, s92, 9\n"
    "s_lshl_b32 s93, s36, 7\n"
    "v_mfma_f32_16x16x32_bf16 v[88:91], a[28:31], v[148:151], v[88:91]\n"
    "s_add_u32 s92, s92, s93\n"
    "s_add_u32 s92, s92, 0xb0000\n"
    "v_mfma_f32_16x16x32_bf16 v[52:55], a[16:19], v[152:155], v[52:55]\n"
    "v_lshl_add_u32 v24, v1, 3, s92\n"
    "v_mfma_f32_16x16x32_bf16 v[72:75], a[20:23], v[152:155], v[72:75]\n"
    "v_cmp_gt_u32 vcc, 16, v1\n"
    "v_mfma_f32_16x16x32_bf16 v[112:115], v[152:155], a[24:27], v[112:115]\n"
    "buffer_load_dwordx4 a[172:175], v6, s[16:19], s51 offen offset:3072 sc1\n"
    "v_mfma_f32_16x16x32_bf16 v[92:95], a[28:31], v[152:155], v[92:95]\n"
    "s_waitcnt lgkmcnt(0)\n"
    "v_cndmask_b32 v15, 0x40000000, v24, vcc\n"
    "v_mfma_f32_16x16x32_bf16 v[56:59], a[36:39], v[116:119], v[56:59]\n"
    "ds_read_b128 v[136:139], v5 offset:192\n"
    "v_lshlrev_b32 v24, 2, v38\n"
    "v_mfma_f32_16x16x32_bf16 v[96:99], v[116:119], a[40:43], v[96:99]\n"
    "v_sub_u32 v156, v39, v24\n"
    "v_mov_b32 v157, 0xf149f2ca\n"
    "v_mfma_f32_16x16x32_bf16 v[76:79], a[44:47], v[116:119], v[76:79]\n"
    "ds_read_b128 v[140:143], v4 offset:192\n"
    "v_cmp_lt_u32 vcc, 7, v156\n"
    "v_mfma_f32_16x16x32_bf16 v[40:43], a[32:35], v[120:123], v[40:43]\n"
    "s_nop 0\n"
    "v_cndmask_b32 v220, 0, v157, vcc\n"
    "v_mfma_f32_16x16x32_bf16 v[60:63], a[36:39], v[120:123], v[60:63]\n"
    "ds_read_b128 v[144:147], v4 offset:21184\n"
    "s_add_u32 s51, s51, 3840\n"
    "v_mfma_f32_16x16x32_bf16 v[100:103], v[120:123], a[40:43], v[100:103]\n"
    "buffer_load_dwordx4 a[176:179], v2, s[16:19], s51 offen sc1\n"
    "v_mfma_f32_16x16x32_bf16 v[80:83], a[44:47], v[120:123], v[80:83]\n"
    "ds_read_b128 v[148:151], v4 offset:42176\n"
    "v_add_u32 v24, 16, v156\n"
    "v_mfma_f32_16x16x32_bf16 v[44:47], a[32:35], v[124:127], v[44:47]\n"
    "v_cmp_lt_u32 vcc, 7, v24\n"
    "s_nop 1\n"
    "v_cndmask_b32 v224, 0, v157, vcc\n"
    "v_mfma_f32_16x16x32_bf16 v[64:67], a[36:39], v[124:127], v[64:67]\n"
    "ds_read_b128 v[152:155], v4 offset:63168\n"
    "v_add_u32 v24, -1, v156\n"
    "v_mfma_f32_16x16x32_bf16 v[104:107], v[124:127], a[40:43], v[104:107]\n"
    "buffer_load_dwordx4 a[180:183], v2, s[16:19], s51 offen offset:1024 sc1\n"
    "v_mfma_f32_16x16x32_bf16 v[84:87], a[44:47], v[124:127], v[84:87]\n"
    "v_cmp_lt_u32 vcc, 7, v24\n"
    "s_nop 1\n"
    "v_cndmask_b32 v221, 0, v157, vcc\n"
    "v_mfma_f32_16x16x32_bf16 v[48:51], a[32:35], v[128:131], v[48:51]\n"
    "v_add_u32 v24, 15, v156\n"
    "v_cmp_lt_u32 vcc, 7, v24\n"
    "v_mfma_f32_16x16x32_bf16 v[68:71], a[36:39], v[128:131], v[68:71]\n"
    "s_nop 0\n"
    "v_cndmask_b32 v225, 0, v157, vcc\n"
    "v_mfma_f32_16x16x32_bf16 v[108:111], v[128:131], a[40:43], v[108:111]\n"
    "v_add_u32 v24, -2, v156\n"
    "v_mfma_f32_16x16x32_bf16 v[88:91], a[44:47], v[128:131], v[88:91]\n"
    "buffer_load_dwordx4 a[184:187], v2, s[16:19], s51 offen offset:2048 sc1\n"
    "v_mfma_f32_16x16x32_bf16 v[52:55], a[32:35], v[132:135], v[52:55]\n"
    "v_cmp_lt_u32 vcc, 7, v24\n"
    "s_nop 1\n"
    "v_cndmask_b32 v222, 0, v157, vcc\n"
    "v_mfma_f32_16x16x32_bf16 v[72:75], a[36:39], v[132:135], v[72:75]\n"
    "v_add_u32 v24, 14, v156\n"
    "v_mfma_f32_16x16x32_bf16 v[112:115], v[132:135], a[40:43], v[112:115]\n"
    "v_cmp_lt_u32 vcc, 7, v24\n"
    "v_mfma_f32_16x16x32_bf16 v[92:95], a[44:47], v[132:135], v[92:95]\n"
    "s_waitcnt lgkmcnt(0)\n"
    "v_cndmask_b32 v226, 0, v157, vcc\n"
    "v_mfma_f32_16x16x32_bf16 v[56:59], a[52:55], v[136:139], v[56:59]\n"
    "ds_read_b128 v[116:119], v5 offset:256\n"
    "v_add_u32 v24, -3, v156\n"
    "v_mfma_f32_16x16x32_bf16 v[96:99], v[136:139], a[56:59], v[96:99]\n"
    "buffer_load_dwordx4 a[188:191], v6, s[16:19], s51 offen offset:3072 sc1\n"
    "v_mfma_f32_16x16x32_bf16 v[76:79], a[60:63], v[136:139], v[76:79]\n"
    "ds_read_b128 v[120:123], v4 offset:256\n"
    "v_cmp_lt_u32 vcc, 7, v24\n"
    "v_mfma_f32_16x16x32_bf16 v[40:43], a[48:51], v[140:143], v[40:43]\n"
    "buffer_store_dwordx4 v[250:253], v2, s[16:19], s33 offen sc1\n"
    "v_mfma_f32_16x16x32_bf16 v[60:63], a[52:55], v[140:143], v[60:63]\n"
    "ds_read_b128 v[124:127], v4 offset:21248\n"
    "v_cndmask_b32 v223, 0, v157, vcc\n"
    "v_mfma_f32_16x16x32_bf16 v[100:103], v[140:143], a[56:59], v[100:103]\n"
    "v_add_u32 v24, 13, v156\n"
    "v_cmp_lt_u32 vcc, 7, v24\n"
    "v_mfma_f32_16x16x32_bf16 v[80:83], a[60:63], v[140:143], v[80:83]\n"
    "ds_read_b128 v[128:131], v4 offset:42240\n"
    "s_add_u32 s51, s51, 3840\n"
    "v_mfma_f32_16x16x32_bf16 v[44:47], a[48:51], v[144:147], v[44:47]\n"
    "buffer_load_dwordx4 a[192:195], v2, s[16:19], s51 offen sc1\n"
    "v_mfma_f32_16x16x32_bf16 v[64:67], a[52:55], v[144:147], v[64:67]\n"
    "ds_read_b128 v[132:135], v4 offset:63232\n"
    "v_cndmask_b32 v227, 0, v157, vcc\n"
    "v_mfma_f32_16x16x32_bf16 v[104:107], v[144:147], a[56:59], v[104:107]\n"
    "v_and_b32 v24, 1, v1\n"
    "v_lshlrev_b32 v156, 4, v24\n"
    "v_mfma_f32_16x16x32_bf16 v[84:87], a[60:63], v[144:147], v[84:87]\n"
    "v_mov_b32 v157, 0x3f80\n"
    "v_lshlrev_b32 v157, v156, v157\n"
    "v_mfma_f32_16x16x32_bf16 v[48:51], a[48:51], v[148:151], v[48:51]\n"
    "buffer_load_dwordx4 a[196:199], v2, s[16:19], s51 offen offset:1024 sc1\n"
    "v_mfma_f32_16x16x32_bf16 v[68:71], a[52:55], v[148:151], v[68:71]\n"
    "v_and_b32 v156, 50, v1\n"
    "v_cmp_eq_u32 vcc, 48, v156\n"
    "v_mfma_f32_16x16x32_bf16 v[108:111], v[148:151], a[56:59], v[108:111]\n"
    "s_nop 0\n"
    "v_cndmask_b32 v234, 0, v157, vcc\n"
    "v_cmp_eq_u32 vcc, 50, v156\n"
    "v_mfma_f32_16x16x32_bf16 v[88:91], a[60:63], v[148:151], v[88:91]\n"
    "s_nop 0\n"
    "v_cndmask_b32 v235, 0, v157, vcc\n"
    "v_mfma_f32_16x16x32_bf16 v[52:55], a[48:51], v[152:155], v[52:55]\n"
    "s_mul_i32 s80, s37, 0xa0\n"
    "v_mfma_f32_16x16x32_bf16 v[72:75], a[52:55], v[152:155], v[72:75]\n"
    "buffer_load_dwordx4 a[200:203], v2, s[16:19], s51 offen offset:2048 sc1\n"
    "v_mfma_f32_16x16x32_bf16 v[112:115], v[152:155], a[56:59], v[112:115]\n"
    "v_lshl_add_u32 v16, v38, 4, s80\n"
    "v_and_b32 v24, 1, v38\n"
    "v_mfma_f32_16x16x32_bf16 v[92:95], a[60:63], v[152:155], v[92:95]\n"
    "s_waitcnt lgkmcnt(0)\n"
    "v_lshrrev_b32 v25, 5, v1\n"
    "v_mfma_f32_16x16x32_bf16 v[56:59], a[68:71], v[116:119], v[56:59]\n"
    "ds_read_b128 v[136:139], v5 offset:320\n"
    "s_add_u32 s92, s80, 0x80\n"
    "v_mfma_f32_16x16x32_bf16 v[96:99], v[116:119], a[72:75], v[96:99]\n"
    "v_lshl_add_u32 v25, v25, 3, s92\n"
    "v_mfma_f32_16x16x32_bf16 v[76:79], a[76:79], v[116:119], v[76:79]\n"
    "ds_read_b128 v[140:143], v4 offset:320\n"
    "v_lshl_add_u32 v17, v24, 4, v25\n"
    "v_mfma_f32_16x16x32_bf16 v[40:43], a[64:67], v[120:123], v[40:43]\n"
    "buffer_load_dwordx4 a[204:207], v6, s[16:19], s51 offen offset:3072 sc1\n"
    "v_mfma_f32_16x16x32_bf16 v[60:63], a[68:71], v[120:123], v[60:63]\n"
    "ds_read_b128 v[144:147], v4 offset:21312\n"
    "v_mul_u32_u24 v24, 0xa00, v39\n"
    "v_mfma_f32_16x16x32_bf16 v[100:103], v[120:123], a[72:75], v[100:103]\n"
    "s_mul_i32 s92, s36, 0xa000\n"
    "s_add_u32 s92, s92, s44\n"
    "v_mfma_f32_16x16x32_bf16 v[80:83], a[76:79], v[120:123], v[80:83]\n"
    "ds_read_b128 v[148:151], v4 offset:42304\n"
    "v_add_u32 v13, s92, v24\n"
    "v_mfma_f32_16x16x32_bf16 v[44:47], a[64:67], v[124:127], v[44:47]\n"
    "v_add_u32 v12, v16, v13\n"
    "v_mfma_f32_16x16x32_bf16 v[64:67], a[68:71], v[124:127], v[64:67]\n"
    "ds_read_b128 v[152:155], v4 offset:63296\n"
    "s_add_u32 s51, s51, 3840\n"
    "v_mfma_f32_16x16x32_bf16 v[104:107], v[124:127], a[72:75], v[104:107]\n"
    "buffer_load_dwordx4 a[208:211], v2, s[16:19], s51 offen sc1\n"
    "v_mfma_f32_16x16x32_bf16 v[84:87], a[76:79], v[124:127], v[84:87]\n"
    "v_add_u32 v13, v17, v13\n"
    "s_lshl_b32 s92, s36, 10\n"
    "v_mfma_f32_16x16x32_bf16 v[48:51], a[64:67], v[128:131], v[48:51]\n"
    "v_add_u32 v18, s92, v2\n"
    "s_lshl_b32 s92, s36, 9\n"
    "v_mfma_f32_16x16x32_bf16 v[68:71], a[68:71], v[128:131], v[68:71]\n"
    "s_add_u32 s92, s92, 0x1000\n"
    "v_mfma_f32_16x16x32_bf16 v[108:111], v[128:131], a[72:75], v[108:111]\n"
    "buffer_load_dwordx4 a[212:215], v2, s[16:19], s51 offen offset:1024 sc1\n"
    "v_mfma_f32_16x16x32_bf16 v[88:91], a[76:79], v[128:131], v[88:91]\n"
    "v_add_u32 v19, s92, v14\n"
    "s_mul_i32 s92, s36, 0x1400\n"
    "v_mfma_f32_16x16x32_bf16 v[52:55], a[64:67], v[132:135], v[52:55]\n"
    "s_add_u32 s92, s49, s92\n"
    "v_mfma_f32_16x16x32_bf16 v[72:75], a[68:71], v[132:135], v[72:75]\n"
    "s_add_u32 s80, s92, 0x280000\n"
    "v_mfma_f32_16x16x32_bf16 v[112:115], v[132:135], a[72:75], v[112:115]\n"
    "s_lshl_b32 s92, s79, 11\n"
    "v_mfma_f32_16x16x32_bf16 v[92:95], a[76:79], v[132:135], v[92:95]\n"
    "s_waitcnt lgkmcnt(0)\n"
    "s_add_u32 s55, s80, s92\n"
    "v_mfma_f32_16x16x32_bf16 v[56:59], a[84:87], v[136:139], v[56:59]\n"
    "ds_read_b128 v[116:119], v5 offset:384\n"
    "s_lshl_b32 s92, s79, 9\n"
    "v_mfma_f32_16x16x32_bf16 v[96:99], v[136:139], a[88:91], v[96:99]\n"
    "buffer_load_dwordx4 a[216:219], v2, s[16:19], s51 offen offset:2048 sc1\n"
    "v_mfma_f32_16x16x32_bf16 v[76:79], a[92:95], v[136:139], v[76:79]\n"
    "ds_read_b128 v[120:123], v4 offset:384\n"
    "s_add_u32 s92, s80, s92\n"
    "v_mfma_f32_16x16x32_bf16 v[40:43], a[80:83], v[140:143], v[40:43]\n"
    "s_add_u32 s56, s92, 0x1000\n"
    "s_lshl_b32 s92, s38, 2\n"
    "v_mfma_f32_16x16x32_bf16 v[60:63], a[84:87], v[140:143], v[60:63]\n"
    "ds_read_b128 v[124:127], v4 offset:21376\n"
    "s_add_u32 s92, s92, s78\n"
    "v_mfma_f32_16x16x32_bf16 v[100:103], v[140:143], a[88:91], v[100:103]\n"
    "buffer_load_dwordx4 a[220:223], v6, s[16:19], s51 offen offset:3072 sc1\n"
    "v_mfma_f32_16x16x32_bf16 v[80:83], a[92:95], v[140:143], v[80:83]\n"
    "ds_read_b128 v[128:131], v4 offset:42368\n"
    "s_mul_i32 s92, s92, 0xc000\n"
    "v_mfma_f32_16x16x32_bf16 v[44:47], a[80:83], v[144:147], v[44:47]\n"
    "s_mul_i32 s93, s79, 0x1800\n"
    "s_add_u32 s92, s92, s93\n"
    "v_mfma_f32_16x16x32_bf16 v[64:67], a[84:87], v[144:147], v[64:67]\n"
    "ds_read_b128 v[132:135], v4 offset:63360\n"
    "s_add_u32 s80, s92, 0xd00000\n"
    "v_mfma_f32_16x16x32_bf16 v[104:107], v[144:147], a[88:91], v[104:107]\n"
    "s_lshl_b32 s92, s38, 2\n"
    "v_mfma_f32_16x16x32_bf16 v[84:87], a[92:95], v[144:147], v[84:87]\n"
    "s_add_u32 s51, s51, 3840\n"
    "s_add_u32 s81, s92, s36\n"
    "v_mfma_f32_16x16x32_bf16 v[48:51], a[80:83], v[148:151], v[48:51]\n"
    "buffer_load_dwordx4 a[224:227], v2, s[16:19], s51 offen sc1\n"
    "v_mfma_f32_16x16x32_bf16 v[68:71], a[84:87], v[148:151], v[68:71]\n"
    "s_mul_i32 s92, s81, 0x5000\n"
    "s_add_u32 s82, s92, 0xa80000\n"
    "v_mfma_f32_16x16x32_bf16 v[108:111], v[148:151], a[88:91], v[108:111]\n"
    "s_add_u32 s83, s82, 0xffd80000\n"
    "v_mfma_f32_16x16x32_bf16 v[88:91], a[92:95], v[148:151], v[88:91]\n"
    "s_mul_i32 s92, s81, 0xc000\n"
    "v_mfma_f32_16x16x32_bf16 v[52:55], a[80:83], v[152:155], v[52:55]\n"
    "buffer_load_dwordx4 a[228:231], v2, s[16:19], s51 offen offset:1024 sc1\n"
    "v_mfma_f32_16x16x32_bf16 v[72:75], a[84:87], v[152:155], v[72:75]\n"
    "s_add_u32 s81, s92, 0xd00000\n"
    "s_add_u32 s92, s78, 1\n"
    "v_mfma_f32_16x16x32_bf16 v[112:115], v[152:155], a[88:91], v[112:115]\n"
    "s_and_b32 s84, s92, 3\n"
    "v_mfma_f32_16x16x32_bf16 v[92:95], a[92:95], v[152:155], v[92:95]\n"
    "s_waitcnt lgkmcnt(0)\n"
    "s_mul_i32 s92, s84, 0x3000\n"
    "v_mfma_f32_16x16x32_bf16 v[56:59], a[100:103], v[116:119], v[56:59]\n"
    "ds_read_b128 v[136:139], v5 offset:448\n"
    "s_add_u32 s60, s92, s80\n"
    "v_mfma_f32_16x16x32_bf16 v[96:99], v[116:119], a[104:107], v[96:99]\n"
    "buffer_load_dwordx4 a[232:235], v2, s[16:19], s51 offen offset:2048 sc1\n"
    "v_mfma_f32_16x16x32_bf16 v[76:79], a[108:111], v[116:119], v[76:79]\n"
    "ds_read_b128 v[140:143], v4 offset:448\n"
    "s_mul_i32 s92, s84, 0x1400\n"
    "v_mfma_f32_16x16x32_bf16 v[40:43], a[96:99], v[120:123], v[40:43]\n"
    "s_add_u32 s68, s92, s82\n"
    "s_add_u32 s92, s78, 2\n"
    "v_mfma_f32_16x16x32_bf16 v[60:63], a[100:103], v[120:123], v[60:63]\n"
    "ds_read_b128 v[144:147], v4 offset:21440\n"
    "s_and_b32 s84, s92, 3\n"
    "v_mfma_f32_16x16x32_bf16 v[100:103], v[120:123], a[104:107], v[100:103]\n"
    "s_mul_i32 s92, s84, 0x3000\n"
    "v_mfma_f32_16x16x32_bf16 v[80:83], a[108:111], v[120:123], v[80:83]\n"
    "ds_read_b128 v[148:151], v4 offset:42432\n"
    "s_add_u32 s61, s92, s80\n"
    "v_mfma_f32_16x16x32_bf16 v[44:47], a[96:99], v[124:127], v[44:47]\n"
    "buffer_store_dwordx4 v[250:253], v2, s[16:19], s33 offen offset:1024 sc1\n"
    "v_mfma_f32_16x16x32_bf16 v[64:67], a[100:103], v[124:127], v[64:67]\n"
    "ds_read_b128 v[152:155], v4 offset:63424\n"
    "s_mul_i32 s92, s84, 0x1400\n"
    "v_mfma_f32_16x16x32_bf16 v[104:107], v[124:127], a[104:107], v[104:107]\n"
    "buffer_load_dwordx4 a[236:239], v6, s[16:19], s51 offen offset:3072 sc1\n"
    "v_mfma_f32_16x16x32_bf16 v[84:87], a[108:111], v[124:127], v[84:87]\n"
    "s_add_u32 s69, s92, s82\n"
    "s_add_u32 s92, s78, 3\n"
    "v_mfma_f32_16x16x32_bf16 v[48:51], a[96:99], v[128:131], v[48:51]\n"
    "s_and_b32 s84, s92, 3\n"
    "s_mul_i32 s92, s84, 0x3000\n"
    "v_mfma_f32_16x16x32_bf16 v[68:71], a[100:103], v[128:131], v[68:71]\n"
    "s_add_u32 s51, s51, 3840\n"
    "s_add_u32 s62, s92, s80\n"
    "v_mfma_f32_16x16x32_bf16 v[108:111], v[128:131], a[104:107], v[108:111]\n"
    "buffer_load_dwordx4 a[240:243], v2, s[16:19], s51 offen sc1\n"
    "v_mfma_f32_16x16x32_bf16 v[88:91], a[108:111], v[128:131], v[88:91]\n"
    "s_mul_i32 s92, s84, 0x1400\n"
    "s_add_u32 s70, s92, s82\n"
    "v_mfma_f32_16x16x32_bf16 v[52:55], a[96:99], v[132:135], v[52:55]\n"
    "s_add_u32 s92, s78, 4\n"
    "v_mfma_f32_16x16x32_bf16 v[72:75], a[100:103], v[132:135], v[72:75]\n"
    "s_and_b32 s84, s92, 3\n"
    "v_mfma_f32_16x16x32_bf16 v[112:115], v[132:135], a[104:107], v[112:115]\n"
    "buffer_load_dwordx4 a[244:247], v2, s[16:19], s51 offen offset:1024 sc1\n"
    "v_mfma_f32_16x16x32_bf16 v[92:95], a[108:111], v[132:135], v[92:95]\n"
    "s_waitcnt lgkmcnt(0)\n"
    "s_mul_i32 s92, s84, 0x3000\n"
    "v_mfma_f32_16x16x32_bf16 v[56:59], a[116:119], v[136:139], v[56:59]\n"
    "ds_read_b128 v[116:119], v5 offset:512\n"
    "s_add_u32 s63, s92, s80\n"
    "v_mfma_f32_16x16x32_bf16 v[96:99], v[136:139], a[120:123], v[96:99]\n"
    "s_mul_i32 s92, s84, 0x1400\n"
    "s_add_u32 s71, s92, s82\n"
    "v_mfma_f32_16x16x32_bf16 v[76:79], a[124:127], v[136:139], v[76:79]\n"
    "ds_read_b128 v[120:123], v4 offset:512\n"
    "s_lshl_b32 s92, s37, 2\n"
    "v_mfma_f32_16x16x32_bf16 v[40:43], a[112:115], v[140:143], v[40:43]\n"
    "buffer_load_dwordx4 a[248:251], v2, s[16:19], s51 offen offset:2048 sc1\n"
    "v_mfma_f32_16x16x32_bf16 v[60:63], a[116:119], v[140:143], v[60:63]\n"
    "ds_read_b128 v[124:127], v4 offset:21504\n"
    "s_add_u32 s84, s92, s36\n"
    "v_mfma_f32_16x16x32_bf16 v[100:103], v[140:143], a[120:123], v[100:103]\n"
    "s_mul_i32 s92, s84, 0x6400\n"
    "s_add_u32 s52, s92, 0x360000\n"
    "v_mfma_f32_16x16x32_bf16 v[80:83], a[124:127], v[140:143], v[80:83]\n"
    "ds_read_b128 v[128:131], v4 offset:42496\n"
    "s_mul_i32 s92, s84, 0xf000\n"
    "v_mfma_f32_16x16x32_bf16 v[44:47], a[112:115], v[144:147], v[44:47]\n"
    "s_add_u32 s53, s92, 0x610000\n"
    "v_mfma_f32_16x16x32_bf16 v[64:67], a[116:119], v[144:147], v[64:67]\n"
    "ds_read_b128 v[132:135], v4 offset:63488\n"
    "s_add_u32 s54, s53, 0xffe20000\n"
    "v_mfma_f32_16x16x32_bf16 v[104:107], v[144:147], a[120:123], v[104:107]\n"
    "buffer_load_dwordx4 a[252:255], v6, s[16:19], s51 offen offset:3072 sc1\n"
    "v_mfma_f32_16x16x32_bf16 v[84:87], a[124:127], v[144:147], v[84:87]\n"
    "s_add_u32 s92, s36, 1\n"
    "s_and_b32 s84, s92, 3\n"
    "v_mfma_f32_16x16x32_bf16 v[48:51], a[112:115], v[148:151], v[48:51]\n"
    "s_mul_i32 s92, s84, 0x1400\n"
    "v_mfma_f32_16x16x32_bf16 v[68:71], a[116:119], v[148:151], v[68:71]\n"
    "s_add_u32 s64, s92, s83\n"
    "v_mfma_f32_16x16x32_bf16 v[108:111], v[148:151], a[120:123], v[108:111]\n"
    "s_mul_i32 s92, s84, 0x3000\n"
    "v_mfma_f32_16x16x32_bf16 v[88:91], a[124:127], v[148:151], v[88:91]\n"
    "s_add_u32 s51, s51, 3840\n"
    "s_add_u32 s72, s92, s81\n"
    "v_mfma_f32_16x16x32_bf16 v[52:55], a[112:115], v[152:155], v[52:55]\n"
    "buffer_load_dwordx4 a[0:3], v2, s[16:19], s51 offen sc1\n"
    "v_mfma_f32_16x16x32_bf16 v[72:75], a[116:119], v[152:155], v[72:75]\n"
    "s_mul_i32 s92, s84, 0x3c00\n"
    "s_add_u32 s92, s92, 0x17100\n"
    "v_mfma_f32_16x16x32_bf16 v[112:115], v[152:155], a[120:123], v[112:115]\n"
    "v_add_u32 v20, s92, v2\n"
    "v_mfma_f32_16x16x32_bf16 v[92:95], a[124:127], v[152:155], v[92:95]\n"
    "s_waitcnt vmcnt(32) lgkmcnt(0)\n"
    "s_add_u32 s92, s36, 2\n"
    "v_mfma_f32_16x16x32_bf16 v[56:59], a[132:135], v[116:119], v[56:59]\n"
    "ds_read_b128 v[136:139], v5 offset:576\n"
    "s_and_b32 s84, s92, 3\n"
    "v_mfma_f32_16x16x32_bf16 v[96:99], v[116:119], a[136:139], v[96:99]\n"
    "buffer_load_dwordx4 a[4:7], v2, s[16:19], s51 offen offset:1024 sc1\n"
    "v_mfma_f32_16x16x32_bf16 v[76:79], a[140:143], v[116:119], v[76:79]\n"
    "ds_read_b128 v[140:143], v4 offset:576\n"
    "s_mul_i32 s92, s84, 0x1400\n"
    "v_mfma_f32_16x16x32_bf16 v[40:43], a[128:131], v[120:123], v[40:43]\n"
    "s_add_u32 s65, s92, s83\n"
    "s_mul_i32 s92, s84, 0x3000\n"
    "v_mfma_f32_16x16x32_bf16 v[60:63], a[132:135], v[120:123], v[60:63]\n"
    "ds_read_b128 v[144:147], v4 offset:21568\n"
    "s_add_u32 s73, s92, s81\n"
    "v_mfma_f32_16x16x32_bf16 v[100:103], v[120:123], a[136:139], v[100:103]\n"
    "buffer_load_dwordx4 a[8:11], v2, s[16:19], s51 offen offset:2048 sc1\n"
    "v_mfma_f32_16x16x32_bf16 v[80:83], a[140:143], v[120:123], v[80:83]\n"
    "ds_read_b128 v[148:151], v4 offset:42560\n"
    "s_mul_i32 s92, s84, 0x3c00\n"
    "v_mfma_f32_16x16x32_bf16 v[44:47], a[128:131], v[124:127], v[44:47]\n"
    "s_add_u32 s92, s92, 0x18500\n"
    "v_add_u32 v21, s92, v2\n"
    "v_mfma_f32_16x16x32_bf16 v[64:67], a[132:135], v[124:127], v[64:67]\n"
    "ds_read_b128 v[152:155], v4 offset:63552\n"
    "s_add_u32 s92, s36, 3\n"
    "v_mfma_f32_16x16x32_bf16 v[104:107], v[124:127], a[136:139], v[104:107]\n"
    "s_and_b32 s84, s92, 3\n"
    "v_mfma_f32_16x16x32_bf16 v[84:87], a[140:143], v[124:127], v[84:87]\n"
    "buffer_load_dwordx4 a[12:15], v6, s[16:19], s51 offen offset:3072 sc1\n"
    "v_mfma_f32_16x16x32_bf16 v[48:51], a[128:131], v[128:131], v[48:51]\n"
    "s_mul_i32 s92, s84, 0x1400\n"
    "s_add_u32 s66, s92, s83\n"
    "v_mfma_f32_16x16x32_bf16 v[68:71], a[132:135], v[128:131], v[68:71]\n"
    "s_mul_i32 s92, s84, 0x3000\n"
    "v_mfma_f32_16x16x32_bf16 v[108:111], v[128:131], a[136:139], v[108:111]\n"
    "s_add_u32 s74, s92, s81\n"
    "v_mfma_f32_16x16x32_bf16 v[88:91], a[140:143], v[128:131], v[88:91]\n"
    "s_mul_i32 s92, s84, 0x3c00\n"
    "v_mfma_f32_16x16x32_bf16 v[52:55], a[128:131], v[132:135], v[52:55]\n"
    "s_add_u32 s92, s92, 0x19900\n"
    "v_mfma_f32_16x16x32_bf16 v[72:75], a[132:135], v[132:135], v[72:75]\n"
    "s_add_u32 s51, s51, 3840\n"
    "v_add_u32 v22, s92, v2\n"
    "v_mfma_f32_16x16x32_bf16 v[112:115], v[132:135], a[136:139], v[112:115]\n"
    "buffer_load_dwordx4 a[16:19], v2, s[16:19], s51 offen sc1\n"
    "v_mfma_f32_16x16x32_bf16 v[92:95], a[140:143], v[132:135], v[92:95]\n"
    "s_waitcnt vmcnt(32) lgkmcnt(0)\n"
    "s_add_u32 s92, s36, 4\n"
    "v_mfma_f32_16x16x32_bf16 v[56:59], a[148:151], v[136:139], v[56:59]\n"
    "ds_read_b128 v[116:119], v5 offset:640\n"
    "s_and_b32 s84, s92, 3\n"
    "v_mfma_f32_16x16x32_bf16 v[96:99], v[136:139], a[152:155], v[96:99]\n"
    "s_mul_i32 s92, s84, 0x1400\n"
    "s_add_u32 s67, s92, s83\n"
    "v_mfma_f32_16x16x32_bf16 v[76:79], a[156:159], v[136:139], v[76:79]\n"
    "ds_read_b128 v[120:123], v4 offset:640\n"
    "s_mul_i32 s92, s84, 0x3000\n"
    "v_mfma_f32_16x16x32_bf16 v[40:43], a[144:147], v[140:143], v[40:43]\n"
    "buffer_load_dwordx4 a[20:23], v2, s[16:19], s51 offen offset:1024 sc1\n"
    "v_mfma_f32_16x16x32_bf16 v[60:63], a[148:151], v[140:143], v[60:63]\n"
    "ds_read_b128 v[124:127], v4 offset:21632\n"
    "s_add_u32 s75, s92, s81\n"
    "v_mfma_f32_16x16x32_bf16 v[100:103], v[140:143], a[152:155], v[100:103]\n"
    "s_mul_i32 s92, s84, 0x3c00\n"
    "s_add_u32 s92, s92, 0x17100\n"
    "v_mfma_f32_16x16x32_bf16 v[80:83], a[156:159], v[140:143], v[80:83]\n"
    "ds_read_b128 v[128:131], v4 offset:42624\n"
    "v_add_u32 v23, s92, v2\n"
    "v_mfma_f32_16x16x32_bf16 v[44:47], a[144:147], v[144:147], v[44:47]\n"
    "buffer_load_dwordx4 a[24:27], v2, s[16:19], s51 offen offset:2048 sc1\n"
    "v_mfma_f32_16x16x32_bf16 v[64:67], a[148:151], v[144:147], v[64:67]\n"
    "ds_read_b128 v[132:135], v4 offset:63616\n"
    "v_add_u32 v16, 0x26100, v16\n"
    "v_mfma_f32_16x16x32_bf16 v[104:107], v[144:147], a[152:155], v[104:107]\n"
    "buffer_store_dwordx4 v[250:253], v18, s[16:19], s60 offen sc1\n"
    "v_mfma_f32_16x16x32_bf16 v[84:87], a[156:159], v[144:147], v[84:87]\n"
    "v_add_u32 v17, 0x26100, v17\n"
    "v_lshlrev_b32 v24, 3, v1\n"
    "v_mfma_f32_16x16x32_bf16 v[48:51], a[144:147], v[148:151], v[48:51]\n"
    "v_add_u32 v11, 0x1000, v24\n"
    "v_add_u32 v10, 0x1000, v2\n"
    "v_mfma_f32_16x16x32_bf16 v[68:71], a[148:151], v[148:151], v[68:71]\n"
    "buffer_load_dwordx4 a[28:31], v6, s[16:19], s51 offen offset:3072 sc1\n"
    "v_mfma_f32_16x16x32_bf16 v[108:111], v[148:151], a[152:155], v[108:111]\n"
    "v_min_f32 v228, s76, v224\n"
    "v_mov_b32 v236, s43\n"
    "v_mfma_f32_16x16x32_bf16 v[88:91], a[156:159], v[148:151], v[88:91]\n"
    "v_min_f32 v229, s76, v225\n"
    "v_mfma_f32_16x16x32_bf16 v[52:55], a[144:147], v[152:155], v[52:55]\n"
    "v_mov_b32 v237, s43\n"
    "v_mfma_f32_16x16x32_bf16 v[72:75], a[148:151], v[152:155], v[72:75]\n"
    "v_min_f32 v230, s76, v226\n"
    "v_mfma_f32_16x16x32_bf16 v[112:115], v[152:155], a[152:155], v[112:115]\n"
    "v_mov_b32 v238, s43\n"
    "v_mfma_f32_16x16x32_bf16 v[92:95], a[156:159], v[152:155], v[92:95]\n"
    "s_waitcnt vmcnt(31) lgkmcnt(0)\n"
    "v_min_f32 v231, s76, v227\n"
    "v_mfma_f32_16x16x32_bf16 v[56:59], a[164:167], v[116:119], v[56:59]\n"
    "ds_read_b128 v[136:139], v5 offset:704\n"
    "v_mov_b32 v239, s43\n"
    "v_mfma_f32_16x16x32_bf16 v[96:99], v[116:119], a[168:171], v[96:99]\n"
    "buffer_load_dwordx2 v[34:35], v32, s[16:19], 0 offen sc1\n"
    "v_mfma_f32_16x16x32_bf16 v[76:79], a[172:175], v[116:119], v[76:79]\n"
    "ds_read_b128 v[140:143], v4 offset:704\n"
    "v_mov_b32 v232, 0\n"
    "v_mfma_f32_16x16x32_bf16 v[40:43], a[160:163], v[120:123], v[40:43]\n"
    "v_mov_b32 v233, 0\n"
    "v_mov_b32 v178, 0\n"
    "v_mfma_f32_16x16x32_bf16 v[60:63], a[164:167], v[120:123], v[60:63]\n"
    "ds_read_b128 v[144:147], v4 offset:21696\n"
    "v_mov_b32 v179, 0\n"
    "v_mfma_f32_16x16x32_bf16 v[100:103], v[120:123], a[168:171], v[100:103]\n"
    "buffer_load_dword v36, v33, s[16:19], 0 offen sc1\n"
    "v_mfma_f32_16x16x32_bf16 v[80:83], a[172:175], v[120:123], v[80:83]\n"
    "ds_read_b128 v[148:151], v4 offset:42688\n"
    "v_mov_b32 v182, 0\n"
    "v_mfma_f32_16x16x32_bf16 v[44:47], a[160:163], v[124:127], v[44:47]\n"
    "v_mov_b32 v183, 0\n"
    "v_mov_b32 v186, 0\n"
    "v_mfma_f32_16x16x32_bf16 v[64:67], a[164:167], v[124:127], v[64:67]\n"
    "ds_read_b128 v[152:155], v4 offset:63680\n"
    "v_mov_b32 v187, 0\n"
    "v_mfma_f32_16x16x32_bf16 v[104:107], v[124:127], a[168:171], v[104:107]\n"
    "buffer_load_dwordx4 v[196:199], v208, s[16:19], s46 offen offset:1024 sc1\n"
    "v_mfma_f32_16x16x32_bf16 v[84:87], a[172:175], v[124:127], v[84:87]\n"
    "v_mov_b32 v190, 0\n"
    "v_mov_b32 v191, 0\n"
    "v_mfma_f32_16x16x32_bf16 v[48:51], a[160:163], v[128:131], v[48:51]\n"
    "v_mfma_f32_16x16x32_bf16 v[68:71], a[164:167], v[128:131], v[68:71]\n"
    "v_mfma_f32_16x16x32_bf16 v[108:111], v[128:131], a[168:171], v[108:111]\n"
    "v_mfma_f32_16x16x32_bf16 v[88:91], a[172:175], v[128:131], v[88:91]\n"
    "buffer_load_dwordx4 v[200:203], v208, s[16:19], s47 offen offset:1024 sc1\n"
    "v_mfma_f32_16x16x32_bf16 v[52:55], a[160:163], v[132:135], v[52:55]\n"
    "v_mfma_f32_16x16x32_bf16 v[72:75], a[164:167], v[132:135], v[72:75]\n"
    "v_mfma_f32_16x16x32_bf16 v[112:115], v[132:135], a[168:171], v[112:115]\n"
    "v_mfma_f32_16x16x32_bf16 v[92:95], a[172:175], v[132:135], v[92:95]\n"
    "s_waitcnt vmcnt(31) lgkmcnt(0)\n"
    "v_mfma_f32_16x16x32_bf16 v[56:59], a[180:183], v[136:139], v[56:59]\n"
    "ds_read_b128 v[116:119], v5 offset:768\n"
    "v_mfma_f32_16x16x32_bf16 v[96:99], v[136:139], a[184:187], v[96:99]\n"
    "buffer_load_dwordx4 v[204:207], v209, s[16:19], s48 offen offset:1024 sc1\n"
    "v_mfma_f32_16x16x32_bf16 v[76:79], a[188:191], v[136:139], v[76:79]\n"
    "ds_read_b128 v[120:123], v4 offset:768\n"
    "v_mfma_f32_16x16x32_bf16 v[40:43], a[176:179], v[140:143], v[40:43]\n"
    "v_mfma_f32_16x16x32_bf16 v[60:63], a[180:183], v[140:143], v[60:63]\n"
    "ds_read_b128 v[124:127], v4 offset:21760\n"
    "v_mfma_f32_16x16x32_bf16 v[100:103], v[140:143], a[184:187], v[100:103]\n"
    "v_mfma_f32_16x16x32_bf16 v[80:83], a[188:191], v[140:143], v[80:83]\n"
    "ds_read_b128 v[128:131], v4 offset:42752\n"
    "s_add_u32 s51, s51, 3840\n"
    "v_mfma_f32_16x16x32_bf16 v[44:47], a[176:179], v[144:147], v[44:47]\n"
    "buffer_load_dwordx4 a[32:35], v2, s[16:19], s51 offen sc1\n"
    "v_mfma_f32_16x16x32_bf16 v[64:67], a[180:183], v[144:147], v[64:67]\n"
    "ds_read_b128 v[132:135], v4 offset:63744\n"
    "v_mfma_f32_16x16x32_bf16 v[104:107], v[144:147], a[184:187], v[104:107]\n"
    "v_mfma_f32_16x16x32_bf16 v[84:87], a[188:191], v[144:147], v[84:87]\n"
    "v_mfma_f32_16x16x32_bf16 v[48:51], a[176:179], v[148:151], v[48:51]\n"
    "buffer_load_dwordx4 a[36:39], v2, s[16:19], s51 offen offset:1024 sc1\n"
    "v_mfma_f32_16x16x32_bf16 v[68:71], a[180:183], v[148:151], v[68:71]\n"
    "v_mfma_f32_16x16x32_bf16 v[108:111], v[148:151], a[184:187], v[108:111]\n"
    "v_mfma_f32_16x16x32_bf16 v[88:91], a[188:191], v[148:151], v[88:91]\n"
    "v_mfma_f32_16x16x32_bf16 v[52:55], a[176:179], v[152:155], v[52:55]\n"
    "v_mfma_f32_16x16x32_bf16 v[72:75], a[180:183], v[152:155], v[72:75]\n"
    "buffer_load_dwordx4 a[40:43], v2, s[16:19], s51 offen offset:2048 sc1\n"
    "v_mfma_f32_16x16x32_bf16 v[112:115], v[152:155], a[184:187], v[112:115]\n"
    "v_mfma_f32_16x16x32_bf16 v[92:95], a[188:191], v[152:155], v[92:95]\n"
    "s_waitcnt vmcnt(30) lgkmcnt(0)\n"
    "v_mfma_f32_16x16x32_bf16 v[56:59], a[196:199], v[116:119], v[56:59]\n"
    "ds_read_b128 v[136:139], v5 offset:832\n"
    "v_mfma_f32_16x16x32_bf16 v[96:99], v[116:119], a[200:203], v[96:99]\n"
    "v_mfma_f32_16x16x32_bf16 v[76:79], a[204:207], v[116:119], v[76:79]\n"
    "ds_read_b128 v[140:143], v4 offset:832\n"
    "v_mfma_f32_16x16x32_bf16 v[40:43], a[192:195], v[120:123], v[40:43]\n"
    "buffer_load_dwordx4 a[44:47], v6, s[16:19], s51 offen offset:3072 sc1\n"
    "v_mfma_f32_16x16x32_bf16 v[60:63], a[196:199], v[120:123], v[60:63]\n"
    "ds_read_b128 v[144:147], v4 offset:21824\n"
    "v_mfma_f32_16x16x32_bf16 v[100:103], v[120:123], a[200:203], v[100:103]\n"
    "v_mfma_f32_16x16x32_bf16 v[80:83], a[204:207], v[120:123], v[80:83]\n"
    "ds_read_b128 v[148:151], v4 offset:42816\n"
    "v_mfma_f32_16x16x32_bf16 v[44:47], a[192:195], v[124:127], v[44:47]\n"
    "v_mfma_f32_16x16x32_bf16 v[64:67], a[196:199], v[124:127], v[64:67]\n"
    "ds_read_b128 v[152:155], v4 offset:63808\n"
    "s_add_u32 s51, s51, 3840\n"
    "v_mfma_f32_16x16x32_bf16 v[104:107], v[124:127], a[200:203], v[104:107]\n"
    "buffer_load_dwordx4 a[48:51], v2, s[16:19], s51 offen sc1\n"
    "v_mfma_f32_16x16x32_bf16 v[84:87], a[204:207], v[124:127], v[84:87]\n"
    "v_mfma_f32_16x16x32_bf16 v[48:51], a[192:195], v[128:131], v[48:51]\n"
    "v_mfma_f32_16x16x32_bf16 v[68:71], a[196:199], v[128:131], v[68:71]\n"
    "buffer_store_dwordx4 v[250:253], v19, s[16:19], s60 offen sc1\n"
    "v_mfma_f32_16x16x32_bf16 v[108:111], v[128:131], a[200:203], v[108:111]\n"
    "buffer_load_dwordx4 a[52:55], v2, s[16:19], s51 offen offset:1024 sc1\n"
    "v_mfma_f32_16x16x32_bf16 v[88:91], a[204:207], v[128:131], v[88:91]\n"
    "v_mfma_f32_16x16x32_bf16 v[52:55], a[192:195], v[132:135], v[52:55]\n"
    "v_mfma_f32_16x16x32_bf16 v[72:75], a[196:199], v[132:135], v[72:75]\n"
    "v_mfma_f32_16x16x32_bf16 v[112:115], v[132:135], a[200:203], v[112:115]\n"
    "v_mfma_f32_16x16x32_bf16 v[92:95], a[204:207], v[132:135], v[92:95]\n"
    "s_waitcnt vmcnt(30) lgkmcnt(0)\n"
    "v_mfma_f32_16x16x32_bf16 v[56:59], a[212:215], v[136:139], v[56:59]\n"
    "ds_read_b128 v[116:119], v5 offset:896\n"
    "v_mfma_f32_16x16x32_bf16 v[96:99], v[136:139], a[216:219], v[96:99]\n"
    "buffer_load_dwordx4 a[56:59], v2, s[16:19], s51 offen offset:2048 sc1\n"
    "v_mfma_f32_16x16x32_bf16 v[76:79], a[220:223], v[136:139], v[76:79]\n"
    "ds_read_b128 v[120:123], v4 offset:896\n"
    "v_mfma_f32_16x16x32_bf16 v[40:43], a[208:211], v[140:143], v[40:43]\n"
    "v_mfma_f32_16x16x32_bf16 v[60:63], a[212:215], v[140:143], v[60:63]\n"
    "ds_read_b128 v[124:127], v4 offset:21888\n"
    "v_mfma_f32_16x16x32_bf16 v[100:103], v[140:143], a[216:219], v[100:103]\n"
    "buffer_load_dwordx4 a[60:63], v6, s[16:19], s51 offen offset:3072 sc1\n"
    "v_mfma_f32_16x16x32_bf16 v[80:83], a[220:223], v[140:143], v[80:83]\n"
    "ds_read_b128 v[128:131], v4 offset:42880\n"
    "v_mfma_f32_16x16x32_bf16 v[44:47], a[208:211], v[144:147], v[44:47]\n"
    "v_mfma_f32_16x16x32_bf16 v[64:67], a[212:215], v[144:147], v[64:67]\n"
    "ds_read_b128 v[132:135], v4 offset:63872\n"
    "v_mfma_f32_16x16x32_bf16 v[104:107], v[144:147], a[216:219], v[104:107]\n"
    "v_mfma_f32_16x16x32_bf16 v[84:87], a[220:223], v[144:147], v[84:87]\n"
    "buffer_load_dwordx4 v[240:243], v12, s[4:7], 0 offen\n"
    "v_mfma_f32_16x16x32_bf16 v[48:51], a[208:211], v[148:151], v[48:51]\n"
    "v_mfma_f32_16x16x32_bf16 v[68:71], a[212:215], v[148:151], v[68:71]\n"
    "v_mfma_f32_16x16x32_bf16 v[108:111], v[148:151], a[216:219], v[108:111]\n"
    "v_mfma_f32_16x16x32_bf16 v[88:91], a[220:223], v[148:151], v[88:91]\n"
    "v_mfma_f32_16x16x32_bf16 v[52:55], a[208:211], v[152:155], v[52:55]\n"
    "buffer_load_dwordx4 v[244:247], v12, s[4:7], 0 offen offset:64\n"
    "v_mfma_f32_16x16x32_bf16 v[72:75], a[212:215], v[152:155], v[72:75]\n"
    "v_mfma_f32_16x16x32_bf16 v[112:115], v[152:155], a[216:219], v[112:115]\n"
    "v_mfma_f32_16x16x32_bf16 v[92:95], a[220:223], v[152:155], v[92:95]\n"
    "s_waitcnt vmcnt(29) lgkmcnt(0)\n"
    "v_mfma_f32_16x16x32_bf16 v[56:59], a[228:231], v[116:119], v[56:59]\n"
    "ds_read_b128 v[136:139], v5 offset:960\n"
    "v_mfma_f32_16x16x32_bf16 v[96:99], v[116:119], a[232:235], v[96:99]\n"
    "buffer_load_dwordx2 v[248:249], v13, s[4:7], 0 offen\n"
    "v_mfma_f32_16x16x32_bf16 v[76:79], a[236:239], v[116:119], v[76:79]\n"
    "ds_read_b128 v[140:143], v4 offset:960\n"
    "v_mfma_f32_16x16x32_bf16 v[40:43], a[224:227], v[120:123], v[40:43]\n"
    "v_mfma_f32_16x16x32_bf16 v[60:63], a[228:231], v[120:123], v[60:63]\n"
    "ds_read_b128 v[144:147], v4 offset:21952\n"
    "v_mfma_f32_16x16x32_bf16 v[100:103], v[120:123], a[232:235], v[100:103]\n"
    "v_mfma_f32_16x16x32_bf16 v[80:83], a[236:239], v[120:123], v[80:83]\n"
    "ds_read_b128 v[148:151], v4 offset:42944\n"
    "v_mfma_f32_16x16x32_bf16 v[44:47], a[224:227], v[124:127], v[44:47]\n"
    "s_waitcnt vmcnt(16)\n"
    "v_xor_b32 v37, s40, v34\n"
    "v_bitop3_b32 v37, v37, v35, s40 bitop3:0xf6\n"
    "s_waitcnt vmcnt(15)\n"
    "v_bitop3_b32 v37, v37, v36, s40 bitop3:0xf6\n"
    "s_waitcnt vmcnt(14)\n"
    "v_bitop3_b32 v37, v37, v197, s40 bitop3:0xf6\n"
    "v_bitop3_b32 v37, v37, v199, s40 bitop3:0xf6\n"
    "s_waitcnt vmcnt(13)\n"
    "v_bitop3_b32 v37, v37, v201, s40 bitop3:0xf6\n"
    "v_bitop3_b32 v37, v37, v203, s40 bitop3:0xf6\n"
    "s_waitcnt vmcnt(12)\n"
    "v_bitop3_b32 v37, v37, v205, s40 bitop3:0xf6\n"
    "v_bitop3_b32 v37, v37, v207, s40 bitop3:0xf6\n"
    "v_cmp_ne_u32 vcc, 0, v37\n"
    "s_nop 1\n"
    "s_cbranch_vccnz .Lr3_%=\n"
    ".Ld3_%=:\n"
    "ds_write2_b32 v210, v196, v198 offset1:1\n"
    "ds_write2_b32 v211, v200, v202 offset1:1\n"
    "ds_write2_b32 v212, v204, v206 offset1:1\n"
    "v_mfma_f32_16x16x32_bf16 v[64:67], a[228:231], v[124:127], v[64:67]\n"
    "ds_read_b128 v[152:155], v4 offset:63936\n"
    "v_mfma_f32_16x16x32_bf16 v[104:107], v[124:127], a[232:235], v[104:107]\n"
    "v_mfma_f32_16x16x32_bf16 v[84:87], a[236:239], v[124:127], v[84:87]\n"
    "v_mfma_f32_16x16x32_bf16 v[48:51], a[224:227], v[128:131], v[48:51]\n"
    "v_mfma_f32_16x16x32_bf16 v[68:71], a[228:231], v[128:131], v[68:71]\n"
    "buffer_load_dwordx4 a[64:67], v2, s[16:19], s52 offen sc1\n"
    "v_mfma_f32_16x16x32_bf16 v[108:111], v[128:131], a[232:235], v[108:111]\n"
    "v_mfma_f32_16x16x32_bf16 v[88:91], a[236:239], v[128:131], v[88:91]\n"
    "v_mfma_f32_16x16x32_bf16 v[52:55], a[224:227], v[132:135], v[52:55]\n"
    "v_mfma_f32_16x16x32_bf16 v[72:75], a[228:231], v[132:135], v[72:75]\n"
    "v_mfma_f32_16x16x32_bf16 v[112:115], v[132:135], a[232:235], v[112:115]\n"
    "buffer_load_dwordx4 a[68:71], v2, s[16:19], s52 offen offset:1024 sc1\n"
    "v_mfma_f32_16x16x32_bf16 v[92:95], a[236:239], v[132:135], v[92:95]\n"
    "s_waitcnt lgkmcnt(0)\n"
    "v_mfma_f32_16x16x32_bf16 v[56:59], a[244:247], v[136:139], v[56:59]\n"
    "ds_read_b128 v[116:119], v5 offset:1024\n"
    "v_mfma_f32_16x16x32_bf16 v[96:99], v[136:139], a[248:251], v[96:99]\n"
    "v_mfma_f32_16x16x32_bf16 v[76:79], a[252:255], v[136:139], v[76:79]\n"
    "ds_read_b128 v[120:123], v4 offset:1024\n"
    "v_mfma_f32_16x16x32_bf16 v[40:43], a[240:243], v[140:143], v[40:43]\n"
    "buffer_load_dwordx4 a[72:75], v2, s[16:19], s52 offen offset:2048 sc1\n"
    "v_mfma_f32_16x16x32_bf16 v[60:63], a[244:247], v[140:143], v[60:63]\n"
    "ds_read_b128 v[124:127], v4 offset:22016\n"
    "v_mfma_f32_16x16x32_bf16 v[100:103], v[140:143], a[248:251], v[100:103]\n"
    "v_mfma_f32_16x16x32_bf16 v[80:83], a[252:255], v[140:143], v[80:83]\n"
    "ds_read_b128 v[128:131], v4 offset:43008\n"
    "v_mfma_f32_16x16x32_bf16 v[44:47], a[240:243], v[144:147], v[44:47]\n"
    "v_mfma_f32_16x16x32_bf16 v[64:67], a[244:247], v[144:147], v[64:67]\n"
    "ds_read_b128 v[132:135], v4 offset:64000\n"
    "v_mfma_f32_16x16x32_bf16 v[104:107], v[144:147], a[248:251], v[104:107]\n"
    "buffer_load_dwordx4 a[76:79], v2, s[16:19], s52 offen offset:3072 sc1\n"
    "v_mfma_f32_16x16x32_bf16 v[84:87], a[252:255], v[144:147], v[84:87]\n"
    "v_mfma_f32_16x16x32_bf16 v[48:51], a[240:243], v[148:151], v[48:51]\n"
    "v_mfma_f32_16x16x32_bf16 v[68:71], a[244:247], v[148:151], v[68:71]\n"
    "v_mfma_f32_16x16x32_bf16 v[108:111], v[148:151], a[248:251], v[108:111]\n"
    "v_mfma_f32_16x16x32_bf16 v[88:91], a[252:255], v[148:151], v[88:91]\n"
    "s_add_u32 s52, s52, 4096\n"
    "v_mfma_f32_16x16x32_bf16 v[52:55], a[240:243], v[152:155], v[52:55]\n"
    "buffer_load_dwordx4 a[80:83], v2, s[16:19], s52 offen sc1\n"
    "v_mfma_f32_16x16x32_bf16 v[72:75], a[244:247], v[152:155], v[72:75]\n"
    "v_mfma_f32_16x16x32_bf16 v[112:115], v[152:155], a[248:251], v[112:115]\n"
    "v_mfma_f32_16x16x32_bf16 v[92:95], a[252:255], v[152:155], v[92:95]\n"
    "s_waitcnt lgkmcnt(0)\n"
    "v_mfma_f32_16x16x32_bf16 v[56:59], a[4:7], v[116:119], v[56:59]\n"
    "ds_read_b128 v[136:139], v5 offset:1088\n"
    "v_mfma_f32_16x16x32_bf16 v[96:99], v[116:119], a[8:11], v[96:99]\n"
    "buffer_load_dwordx4 a[84:87], v2, s[16:19], s52 offen offset:1024 sc1\n"
    "v_mfma_f32_16x16x32_bf16 v[76:79], a[12:15], v[116:119], v[76:79]\n"
    "ds_read_b128 v[140:143], v4 offset:1088\n"
    "v_mfma_f32_16x16x32_bf16 v[40:43], a[0:3], v[120:123], v[40:43]\n"
    "v_mfma_f32_16x16x32_bf16 v[60:63], a[4:7], v[120:123], v[60:63]\n"
    "ds_read_b128 v[144:147], v4 offset:22080\n"
    "v_mfma_f32_16x16x32_bf16 v[100:103], v[120:123], a[8:11], v[100:103]\n"
    "buffer_load_dwordx4 a[88:91], v2, s[16:19], s52 offen offset:2048 sc1\n"
    "v_mfma_f32_16x16x32_bf16 v[80:83], a[12:15], v[120:123], v[80:83]\n"
    "ds_read_b128 v[148:151], v4 offset:43072\n"
    "v_mfma_f32_16x16x32_bf16 v[44:47], a[0:3], v[124:127], v[44:47]\n"
    "v_mfma_f32_16x16x32_bf16 v[64:67], a[4:7], v[124:127], v[64:67]\n"
    "ds_read_b128 v[152:155], v4 offset:64064\n"
    "v_mfma_f32_16x16x32_bf16 v[104:107], v[124:127], a[8:11], v[104:107]\n"
    "v_mfma_f32_16x16x32_bf16 v[84:87], a[12:15], v[124:127], v[84:87]\n"
    "v_mfma_f32_16x16x32_bf16 v[48:51], a[0:3], v[128:131], v[48:51]\n"
    "buffer_load_dwordx4 a[92:95], v2, s[16:19], s52 offen offset:3072 sc1\n"
    "v_mfma_f32_16x16x32_bf16 v[68:71], a[4:7], v[128:131], v[68:71]\n"
    "v_mfma_f32_16x16x32_bf16 v[108:111], v[128:131], a[8:11], v[108:111]\n"
    "v_mfma_f32_16x16x32_bf16 v[88:91], a[12:15], v[128:131], v[88:91]\n"
    "v_mfma_f32_16x16x32_bf16 v[52:55], a[0:3], v[132:135], v[52:55]\n"
    "v_mfma_f32_16x16x32_bf16 v[72:75], a[4:7], v[132:135], v[72:75]\n"
    "s_add_u32 s52, s52, 4096\n"
    "v_mfma_f32_16x16x32_bf16 v[112:115], v[132:135], a[8:11], v[112:115]\n"
    "buffer_load_dwordx4 a[96:99], v2, s[16:19], s52 offen sc1\n"
    "v_mfma_f32_16x16x32_bf16 v[92:95], a[12:15], v[132:135], v[92:95]\n"
    "s_waitcnt lgkmcnt(0)\n"
    "v_mfma_f32_16x16x32_bf16 v[56:59], a[20:23], v[136:139], v[56:59]\n"
    "ds_read_b128 v[116:119], v5 offset:1152\n"
    "v_mfma_f32_16x16x32_bf16 v[96:99], v[136:139], a[24:27], v[96:99]\n"
    "v_mfma_f32_16x16x32_bf16 v[76:79], a[28:31], v[136:139], v[76:79]\n"
    "ds_read_b128 v[120:123], v4 offset:1152\n"
    "v_mfma_f32_16x16x32_bf16 v[40:43], a[16:19], v[140:143], v[40:43]\n"
    "buffer_load_dwordx4 a[100:103], v2, s[16:19], s52 offen offset:1024 sc1\n"
    "v_mfma_f32_16x16x32_bf16 v[60:63], a[20:23], v[140:143], v[60:63]\n"
    "ds_read_b128 v[124:127], v4 offset:22144\n"
    "v_mfma_f32_16x16x32_bf16 v[100:103], v[140:143], a[24:27], v[100:103]\n"
    "v_mfma_f32_16x16x32_bf16 v[80:83], a[28:31], v[140:143], v[80:83]\n"
    "ds_read_b128 v[128:131], v4 offset:43136\n"
    "v_mfma_f32_16x16x32_bf16 v[44:47], a[16:19], v[144:147], v[44:47]\n"
    "buffer_load_dwordx4 a[104:107], v2, s[16:19], s52 offen offset:2048 sc1\n"
    "v_mfma_f32_16x16x32_bf16 v[64:67], a[20:23], v[144:147], v[64:67]\n"
    "ds_read_b128 v[132:135], v4 offset:64128\n"
    "v_mfma_f32_16x16x32_bf16 v[104:107], v[144:147], a[24:27], v[104:107]\n"
    "v_mfma_f32_16x16x32_bf16 v[84:87], a[28:31], v[144:147], v[84:87]\n"
    "v_mfma_f32_16x16x32_bf16 v[48:51], a[16:19], v[148:151], v[48:51]\n"
    "v_mfma_f32_16x16x32_bf16 v[68:71], a[20:23], v[148:151], v[68:71]\n"
    "v_mfma_f32_16x16x32_bf16 v[108:111], v[148:151], a[24:27], v[108:111]\n"
    "buffer_load_dwordx4 a[108:111], v2, s[16:19], s52 offen offset:3072 sc1\n"
    "v_mfma_f32_16x16x32_bf16 v[88:91], a[28:31], v[148:151], v[88:91]\n"
    "v_mfma_f32_16x16x32_bf16 v[52:55], a[16:19], v[152:155], v[52:55]\n"
    "v_mfma_f32_16x16x32_bf16 v[72:75], a[20:23], v[152:155], v[72:75]\n"
    "v_mfma_f32_16x16x32_bf16 v[112:115], v[152:155], a[24:27], v[112:115]\n"
    "v_mfma_f32_16x16x32_bf16 v[92:95], a[28:31], v[152:155], v[92:95]\n"
    "s_waitcnt vmcnt(20) lgkmcnt(0)\n"
    "s_add_u32 s52, s52, 4096\n"
    "v_mfma_f32_16x16x32_bf16 v[56:59], a[36:39], v[116:119], v[56:59]\n"
    "ds_read_b128 v[136:139], v5 offset:1216\n"
    "v_mfma_f32_16x16x32_bf16 v[96:99], v[116:119], a[40:43], v[96:99]\n"
    "buffer_load_dwordx4 a[112:115], v2, s[16:19], s52 offen sc1\n"
    "v_mfma_f32_16x16x32_bf16 v[76:79], a[44:47], v[116:119], v[76:79]\n"
    "ds_read_b128 v[140:143], v4 offset:1216\n"
    "v_mfma_f32_16x16x32_bf16 v[40:43], a[32:35], v[120:123], v[40:43]\n"
    "v_mfma_f32_16x16x32_bf16 v[60:63], a[36:39], v[120:123], v[60:63]\n"
    "ds_read_b128 v[144:147], v4 offset:22208\n"
    "v_mfma_f32_16x16x32_bf16 v[100:103], v[120:123], a[40:43], v[100:103]\n"
    "buffer_load_dwordx4 a[116:119], v2, s[16:19], s52 offen offset:1024 sc1\n"
    "v_mfma_f32_16x16x32_bf16 v[80:83], a[44:47], v[120:123], v[80:83]\n"
    "ds_read_b128 v[148:151], v4 offset:43200\n"
    "v_mfma_f32_16x16x32_bf16 v[44:47], a[32:35], v[124:127], v[44:47]\n"
    "v_mfma_f32_16x16x32_bf16 v[64:67], a[36:39], v[124:127], v[64:67]\n"
    "ds_read_b128 v[152:155], v4 offset:64192\n"
    "v_mfma_f32_16x16x32_bf16 v[104:107], v[124:127], a[40:43], v[104:107]\n"
    "buffer_load_dwordx4 a[120:123], v2, s[16:19], s52 offen offset:2048 sc1\n"
    "v_mfma_f32_16x16x32_bf16 v[84:87], a[44:47], v[124:127], v[84:87]\n"
    "v_mfma_f32_16x16x32_bf16 v[48:51], a[32:35], v[128:131], v[48:51]\n"
    "v_mfma_f32_16x16x32_bf16 v[68:71], a[36:39], v[128:131], v[68:71]\n"
    "v_mfma_f32_16x16x32_bf16 v[108:111], v[128:131], a[40:43], v[108:111]\n"
    "v_mfma_f32_16x16x32_bf16 v[88:91], a[44:47], v[128:131], v[88:91]\n"
    "v_mfma_f32_16x16x32_bf16 v[52:55], a[32:35], v[132:135], v[52:55]\n"
    "buffer_load_dwordx4 a[124:127], v2, s[16:19], s52 offen offset:3072 sc1\n"
    "v_mfma_f32_16x16x32_bf16 v[72:75], a[36:39], v[132:135], v[72:75]\n"
    "v_mfma_f32_16x16x32_bf16 v[112:115], v[132:135], a[40:43], v[112:115]\n"
    "v_mfma_f32_16x16x32_bf16 v[92:95], a[44:47], v[132:135], v[92:95]\n"
    "s_waitcnt vmcnt(19) lgkmcnt(0)\n"
    "v_mfma_f32_16x16x32_bf16 v[56:59], a[52:55], v[136:139], v[56:59]\n"
    "v_mfma_f32_16x16x32_bf16 v[96:99], v[136:139], a[56:59], v[96:99]\n"
    "s_add_u32 s52, s52, 4096\n"
    "v_mfma_f32_16x16x32_bf16 v[76:79], a[60:63], v[136:139], v[76:79]\n"
    "buffer_load_dwordx4 a[128:131], v2, s[16:19], s52 offen sc1\n"
    "v_mfma_f32_16x16x32_bf16 v[40:43], a[48:51], v[140:143], v[40:43]\n"
    "v_mfma_f32_16x16x32_bf16 v[60:63], a[52:55], v[140:143], v[60:63]\n"
    "v_mfma_f32_16x16x32_bf16 v[100:103], v[140:143], a[56:59], v[100:103]\n"
    "v_mfma_f32_16x16x32_bf16 v[80:83], a[60:63], v[140:143], v[80:83]\n"
    "buffer_load_dwordx4 a[132:135], v2, s[16:19], s52 offen offset:1024 sc1\n"
    "v_mfma_f32_16x16x32_bf16 v[44:47], a[48:51], v[144:147], v[44:47]\n"
    "v_mfma_f32_16x16x32_bf16 v[64:67], a[52:55], v[144:147], v[64:67]\n"
    "v_mfma_f32_16x16x32_bf16 v[104:107], v[144:147], a[56:59], v[104:107]\n"
    "v_mfma_f32_16x16x32_bf16 v[84:87], a[60:63], v[144:147], v[84:87]\n"
    "v_mfma_f32_16x16x32_bf16 v[48:51], a[48:51], v[148:151], v[48:51]\n"
    "buffer_load_dwordx4 a[136:139], v2, s[16:19], s52 offen offset:2048 sc1\n"
    "v_mfma_f32_16x16x32_bf16 v[68:71], a[52:55], v[148:151], v[68:71]\n"
    "v_mfma_f32_16x16x32_bf16 v[108:111], v[148:151], a[56:59], v[108:111]\n"
    "v_mfma_f32_16x16x32_bf16 v[88:91], a[60:63], v[148:151], v[88:91]\n"
    "v_mfma_f32_16x16x32_bf16 v[52:55], a[48:51], v[152:155], v[52:55]\n"
    "v_mfma_f32_16x16x32_bf16 v[72:75], a[52:55], v[152:155], v[72:75]\n"
    "v_mfma_f32_16x16x32_bf16 v[112:115], v[152:155], a[56:59], v[112:115]\n"
    "v_mfma_f32_16x16x32_bf16 v[92:95], a[60:63], v[152:155], v[92:95]\n"
    "s_barrier\n"
    "v_cvt_pk_bf16_f32 v158, v76, v77\n"
    "buffer_load_dwordx4 a[140:143], v2, s[16:19], s52 offen offset:3072 sc1\n"
    "v_cvt_pk_bf16_f32 v159, v78, v79\n"
    "v_cvt_pk_bf16_f32 v156, v56, v57\n"
    "v_cvt_pk_bf16_f32 v157, v58, v59\n"
    "v_cvt_pk_bf16_f32 v162, v80, v81\n"
    "v_cvt_pk_bf16_f32 v163, v82, v83\n"
    "v_cvt_pk_bf16_f32 v160, v60, v61\n"
    "v_cvt_pk_bf16_f32 v161, v62, v63\n"
    "v_cvt_pk_bf16_f32 v176, v40, v41\n"
    "v_cvt_pk_bf16_f32 v177, v42, v43\n"
    "v_permlane16_swap_b32 v178, v162\n"
    "v_permlane16_swap_b32 v179, v163\n"
    "s_add_u32 s52, s52, 4096\n"
    "buffer_load_dwordx4 a[144:147], v2, s[16:19], s52 offen sc1\n"
    "v_cvt_pk_bf16_f32 v166, v84, v85\n"
    "v_cvt_pk_bf16_f32 v167, v86, v87\n"
    "v_cvt_pk_bf16_f32 v164, v64, v65\n"
    "v_cvt_pk_bf16_f32 v165, v66, v67\n"
    "v_cvt_pk_bf16_f32 v180, v44, v45\n"
    "v_cvt_pk_bf16_f32 v181, v46, v47\n"
    "v_permlane16_swap_b32 v182, v166\n"
    "v_permlane16_swap_b32 v183, v167\n"
    "v_mfma_f32_16x16x32_bf16 v[116:119], v[156:159], v[176:179], v[228:231]\n"
    "v_mfma_f32_16x16x32_bf16 v[120:123], v[160:163], v[176:179], v[220:223]\n"
    "v_cvt_pk_bf16_f32 v170, v88, v89\n"
    "v_cvt_pk_bf16_f32 v171, v90, v91\n"
    "v_cvt_pk_bf16_f32 v168, v68, v69\n"
    "v_cvt_pk_bf16_f32 v169, v70, v71\n"
    "v_cvt_pk_bf16_f32 v184, v48, v49\n"
    "v_cvt_pk_bf16_f32 v185, v50, v51\n"
    "buffer_load_dwordx4 a[148:151], v2, s[16:19], s52 offen offset:1024 sc1\n"
    "v_permlane16_swap_b32 v186, v170\n"
    "v_permlane16_swap_b32 v187, v171\n"
    "v_mfma_f32_16x16x32_bf16 v[124:127], v[160:163], v[180:183], v[224:227]\n"
    "v_mfma_f32_16x16x32_bf16 v[128:131], v[164:167], v[180:183], v[220:223]\n"
    "v_cvt_pk_bf16_f32 v174, v92, v93\n"
    "v_cvt_pk_bf16_f32 v175, v94, v95\n"
    "v_cvt_pk_bf16_f32 v172, v72, v73\n"
    "v_cvt_pk_bf16_f32 v173, v74, v75\n"
    "v_cvt_pk_bf16_f32 v188, v52, v53\n"
    "v_cvt_pk_bf16_f32 v189, v54, v55\n"
    "v_permlane16_swap_b32 v190, v174\n"
    "buffer_load_dwordx4 a[152:155], v2, s[16:19], s52 offen offset:2048 sc1\n"
    "v_permlane16_swap_b32 v191, v175\n"
    "v_mfma_f32_16x16x32_bf16 v[132:135], v[164:167], v[184:187], v[224:227]\n"
    "v_mfma_f32_16x16x32_bf16 v[136:139], v[168:171], v[184:187], v[220:223]\n"
    "v_cvt_pk_bf16_f32 v192, v96, v97\n"
    "v_cvt_pk_bf16_f32 v193, v98, v99\n"
    "v_mfma_f32_16x16x32_bf16 v[140:143], v[168:171], v[188:191], v[224:227]\n"
    "v_cvt_pk_bf16_f32 v194, v100, v101\n"
    "v_cvt_pk_bf16_f32 v195, v102, v103\n"
    "v_mfma_f32_16x16x32_bf16 v[144:147], v[172:175], v[188:191], v[220:223]\n"
    "v_mfma_f32_16x16x32_bf16 v[56:59], v[156:159], v[232:235], 0\n"
    "v_cvt_pk_bf16_f32 v196, v104, v105\n"
    "v_cvt_pk_bf16_f32 v197, v106, v107\n"
    "buffer_load_dwordx4 a[156:159], v2, s[16:19], s52 offen offset:3072 sc1\n"
    "v_cvt_pk_bf16_f32 v198, v108, v109\n"
    "v_cvt_pk_bf16_f32 v199, v110, v111\n"
    "v_cvt_pk_bf16_f32 v200, v112, v113\n"
    "v_mfma_f32_16x16x32_bf16 v[60:63], v[160:163], v[232:235], 0\n"
    "v_cvt_pk_bf16_f32 v201, v114, v115\n"
    "v_max3_f32 v24, v116, v117, v118\n"
    "v_max3_f32 v28, v119, v120, v121\n"
    "v_max3_f32 v24, v24, v122, v123\n"
    "v_mfma_f32_16x16x32_bf16 v[64:67], v[164:167], v[232:235], 0\n"
    "v_max_f32 v24, v24, v28\n"
    "v_max3_f32 v25, v124, v125, v126\n"
    "s_add_u32 s52, s52, 4096\n"
    "buffer_load_dwordx4 a[160:163], v2, s[16:19], s52 offen sc1\n"
    "v_max3_f32 v29, v127, v128, v129\n"
    "v_max3_f32 v25, v25, v130, v131\n"
    "v_max_f32 v25, v25, v29\n"
    "v_mfma_f32_16x16x32_bf16 v[68:71], v[168:171], v[232:235], 0\n"
    "v_max3_f32 v26, v132, v133, v134\n"
    "v_max3_f32 v30, v135, v136, v137\n"
    "v_max3_f32 v26, v26, v138, v139\n"
    "v_max_f32 v26, v26, v30\n"
    "v_mfma_f32_16x16x32_bf16 v[72:75], v[172:175], v[232:235], 0\n"
    "v_max3_f32 v27, v140, v141, v142\n"
    "v_max3_f32 v31, v143, v144, v145\n"
    "v_max3_f32 v27, v27, v146, v147\n"
    "v_max_f32 v27, v27, v31\n"
    "v_permlane32_swap_b32 v24, v25\n"
    "s_nop 0\n"
    "v_permlane32_swap_b32 v26, v27\n"
    "v_max_f32 v24, v24, v25\n"
    "buffer_load_dwordx4 a[164:167], v2, s[16:19], s53 offen sc1\n"
    "v_max_f32 v26, v26, v27\n"
    "v_cvt_pk_bf16_f32 v202, v56, v57\n"
    "v_cvt_pk_bf16_f32 v203, v58, v59\n"
    "v_permlane16_swap_b32 v24, v26\n"
    "v_max_f32 v24, v24, v26\n"
    "v_mul_f32 v24, 0xbea52b5c, v24\n"
    "v_mov_b32 v26, v24\n"
    "v_cvt_pk_bf16_f32 v204, v60, v61\n"
    "v_cvt_pk_bf16_f32 v205, v62, v63\n"
    "v_permlane16_swap_b32 v24, v26\n"
    "v_mov_b32 v25, v24\n"
    "buffer_load_dwordx4 a[168:171], v2, s[16:19], s53 offen offset:1024 sc1\n"
    "v_mov_b32 v27, v26\n"
    "v_cvt_pk_bf16_f32 v206, v64, v65\n"
    "v_cvt_pk_bf16_f32 v207, v66, v67\n"
    "v_permlane32_swap_b32 v24, v25\n"
    "v_permlane32_swap_b32 v26, v27\n"
    "s_mov_b32 s76, 0x3ea52b5c\n"
    "v_cvt_pk_bf16_f32 v208, v68, v69\n"
    "v_cvt_pk_bf16_f32 v209, v70, v71\n"
    "v_cvt_pk_bf16_f32 v210, v72, v73\n"
    "v_cvt_pk_bf16_f32 v211, v74, v75\n"
    "v_fma_f32 v116, v116, s76, v24\n"
    "v_fma_f32 v117, v117, s76, v24\n"
    "buffer_load_dwordx4 a[172:175], v2, s[16:19], s53 offen offset:2048 sc1\n"
    "v_fma_f32 v118, v118, s76, v24\n"
    "v_fma_f32 v119, v119, s76, v24\n"
    "v_fma_f32 v120, v120, s76, v24\n"
    "v_fma_f32 v121, v121, s76, v24\n"
    "v_fma_f32 v122, v122, s76, v24\n"
    "v_fma_f32 v123, v123, s76, v24\n"
    "v_exp_f32 v116, v116\n"
    "v_exp_f32 v117, v117\n"
    "v_exp_f32 v118, v118\n"
    "v_exp_f32 v119, v119\n"
    "v_exp_f32 v120, v120\n"
    "buffer_load_dwordx4 a[176:179], v2, s[16:19], s53 offen offset:3072 sc1\n"
    "v_exp_f32 v121, v121\n"
    "v_exp_f32 v122, v122\n"
    "v_exp_f32 v123, v123\n"
    "v_cvt_pk_bf16_f32 v40, v116, v117\n"
    "v_cvt_pk_bf16_f32 v41, v118, v119\n"
    "v_cvt_pk_bf16_f32 v42, v120, v121\n"
    "v_cvt_pk_bf16_f32 v43, v122, v123\n"
    "v_fma_f32 v124, v124, s76, v25\n"
    "v_fma_f32 v125, v125, s76, v25\n"
    "v_mfma_f32_16x16x32_bf16 v[56:59], v[236:239], v[40:43], 0\n"
    "v_fma_f32 v126, v126, s76, v25\n"
    "s_add_u32 s53, s53, 4096\n"
    "buffer_load_dwordx4 a[180:183], v2, s[16:19], s53 offen sc1\n"
    "v_fma_f32 v127, v127, s76, v25\n"
    "v_fma_f32 v128, v128, s76, v25\n"
    "v_mfma_f32_16x16x32_bf16 v[76:79], v[192:195], v[40:43], 0\n"
    "v_fma_f32 v129, v129, s76, v25\n"
    "v_fma_f32 v130, v130, s76, v25\n"
    "v_fma_f32 v131, v131, s76, v25\n"
    "v_mfma_f32_16x16x32_bf16 v[96:99], v[202:205], v[40:43], 0\n"
    "v_exp_f32 v124, v124\n"
    "v_exp_f32 v125, v125\n"
    "v_exp_f32 v126, v126\n"
    "v_exp_f32 v127, v127\n"
    "v_exp_f32 v128, v128\n"
    "v_exp_f32 v129, v129\n"
    "v_exp_f32 v130, v130\n"
    "v_exp_f32 v131, v131\n"
    "v_cvt_pk_bf16_f32 v44, v124, v125\n"
    "buffer_load_dwordx4 a[184:187], v2, s[16:19], s53 offen offset:1024 sc1\n"
    "v_cvt_pk_bf16_f32 v45, v126, v127\n"
    "v_cvt_pk_bf16_f32 v46, v128, v129\n"
    "v_cvt_pk_bf16_f32 v47, v130, v131\n"
    "v_fma_f32 v132, v132, s76, v26\n"
    "v_fma_f32 v133, v133, s76, v26\n"
    "v_mfma_f32_16x16x32_bf16 v[60:63], v[236:239], v[44:47], 0\n"
    "v_fma_f32 v134, v134, s76, v26\n"
    "v_fma_f32 v135, v135, s76, v26\n"
    "v_fma_f32 v136, v136, s76, v26\n"
    "v_mfma_f32_16x16x32_bf16 v[80:83], v[194:197], v[44:47], 0\n"
    "v_fma_f32 v137, v137, s76, v26\n"
    "v_fma_f32 v138, v138, s76, v26\n"
    "v_fma_f32 v139, v139, s76, v26\n"
    "v_mfma_f32_16x16x32_bf16 v[100:103], v[204:207], v[44:47], 0\n"
    "v_exp_f32 v132, v132\n"
    "v_exp_f32 v133, v133\n"
    "v_exp_f32 v134, v134\n"
    "v_exp_f32 v135, v135\n"
    "v_exp_f32 v136, v136\n"
    "v_exp_f32 v137, v137\n"
    "v_exp_f32 v138, v138\n"
    "v_exp_f32 v139, v139\n"
    "v_cvt_pk_bf16_f32 v48, v132, v133\n"
    "v_cvt_pk_bf16_f32 v49, v134, v135\n"
    "v_cvt_pk_bf16_f32 v50, v136, v137\n"
    "v_cvt_pk_bf16_f32 v51, v138, v139\n"
    "v_fma_f32 v140, v140, s76, v27\n"
    "v_fma_f32 v141, v141, s76, v27\n"
    "v_mfma_f32_16x16x32_bf16 v[64:67], v[236:239], v[48:51], 0\n"
    "v_fma_f32 v142, v142, s76, v27\n"
    "v_fma_f32 v143, v143, s76, v27\n"
    "v_fma_f32 v144, v144, s76, v27\n"
    "v_mfma_f32_16x16x32_bf16 v[84:87], v[196:199], v[48:51], 0\n"
    "v_fma_f32 v145, v145, s76, v27\n"
    "v_fma_f32 v146, v146, s76, v27\n"
    "v_fma_f32 v147, v147, s76, v27\n"
    "v_mfma_f32_16x16x32_bf16 v[104:107], v[206:209], v[48:51], 0\n"
    "v_exp_f32 v140, v140\n"
    "v_exp_f32 v141, v141\n"
    "v_exp_f32 v142, v142\n"
    "v_exp_f32 v143, v143\n"
    "v_exp_f32 v144, v144\n"
    "v_exp_f32 v145, v145\n"
    "v_exp_f32 v146, v146\n"
    "v_exp_f32 v147, v147\n"
    "v_cvt_pk_bf16_f32 v52, v140, v141\n"
    "v_cvt_pk_bf16_f32 v53, v142, v143\n"
    "v_cvt_pk_bf16_f32 v54, v144, v145\n"
    "v_cvt_pk_bf16_f32 v55, v146, v147\n"
    "v_rcp_f32 v56, v56\n"
    "s_nop 0\n"
    "v_mul_f32_dpp v36, v96, v56 quad_perm:[0,1,2,3] row_mask:0x1 bank_mask:0xf\n"
    "v_mfma_f32_16x16x32_bf16 v[68:71], v[236:239], v[52:55], 0\n"
    "v_mul_f32_dpp v37, v97, v56 quad_perm:[0,1,2,3] row_mask:0x1 bank_mask:0xf\n"
    "v_mul_f32_dpp v38, v98, v56 quad_perm:[0,1,2,3] row_mask:0x1 bank_mask:0xf\n"
    "v_mul_f32_dpp v39, v99, v56 quad_perm:[0,1,2,3] row_mask:0x1 bank_mask:0xf\n"
    "v_mfma_f32_16x16x32_bf16 v[88:91], v[198:201], v[52:55], 0\n"
    "v_mul_f32 v76, v76, v56\n"
    "v_mul_f32 v77, v77, v56\n"
    "v_mul_f32 v78, v78, v56\n"
    "v_mfma_f32_16x16x32_bf16 v[108:111], v[208:211], v[52:55], 0\n"
    "v_mul_f32 v79, v79, v56\n"
    "v_cvt_pk_bf16_f32 v28, v76, v77\n"
    "v_cvt_pk_bf16_f32 v29, v78, v79\n"
    "v_rcp_f32 v60, v60\n"
    "s_nop 0\n"
    "v_mul_f32_dpp v36, v100, v60 quad_perm:[0,1,2,3] row_mask:0x2 bank_mask:0xf\n"
    "v_mul_f32_dpp v37, v101, v60 quad_perm:[0,1,2,3] row_mask:0x2 bank_mask:0xf\n"
    "v_mul_f32_dpp v38, v102, v60 quad_perm:[0,1,2,3] row_mask:0x2 bank_mask:0xf\n"
    "v_mul_f32_dpp v39, v103, v60 quad_perm:[0,1,2,3] row_mask:0x2 bank_mask:0xf\n"
    "v_mul_f32 v80, v80, v60\n"
    "v_mul_f32 v81, v81, v60\n"
    "v_mul_f32 v82, v82, v60\n"
    "v_mul_f32 v83, v83, v60\n"
    "v_cvt_pk_bf16_f32 v30, v80, v81\n"
    "v_cvt_pk_bf16_f32 v31, v82, v83\n"
    "s_nop 0\n"
    "v_permlane32_swap_b32 v28, v30\n"
    "v_permlane32_swap_b32 v29, v31\n"
    "v_min_u32 v28, -2, v28\n"
    "buffer_store_dwordx4 v[28:31], v7, s[16:19], 0 offen sc1\n"
    "v_rcp_f32 v64, v64\n"
    "s_nop 0\n"
    "v_mul_f32_dpp v36, v104, v64 quad_perm:[0,1,2,3] row_mask:0x4 bank_mask:0xf\n"
    "v_mul_f32_dpp v37, v105, v64 quad_perm:[0,1,2,3] row_mask:0x4 bank_mask:0xf\n"
    "v_mul_f32_dpp v38, v106, v64 quad_perm:[0,1,2,3] row_mask:0x4 bank_mask:0xf\n"
    "v_mul_f32_dpp v39, v107, v64 quad_perm:[0,1,2,3] row_mask:0x4 bank_mask:0xf\n"
    "v_mul_f32 v84, v84, v64\n"
    "v_mul_f32 v85, v85, v64\n"
    "v_mul_f32 v86, v86, v64\n"
    "v_mul_f32 v87, v87, v64\n"
    "v_cvt_pk_bf16_f32 v32, v84, v85\n"
    "v_cvt_pk_bf16_f32 v33, v86, v87\n"
    "v_rcp_f32 v68, v68\n"
    "s_nop 0\n"
    "v_mul_f32_dpp v36, v108, v68 quad_perm:[0,1,2,3] row_mask:0x8 bank_mask:0xf\n"
    "v_mul_f32_dpp v37, v109, v68 quad_perm:[0,1,2,3] row_mask:0x8 bank_mask:0xf\n"
    "v_mul_f32_dpp v38, v110, v68 quad_perm:[0,1,2,3] row_mask:0x8 bank_mask:0xf\n"
    "v_mul_f32_dpp v39, v111, v68 quad_perm:[0,1,2,3] row_mask:0x8 bank_mask:0xf\n"
    "v_mul_f32 v88, v88, v68\n"
    "v_mul_f32 v89, v89, v68\n"
    "v_mul_f32 v90, v90, v68\n"
    "v_mul_f32 v91, v91, v68\n"
    "v_cvt_pk_bf16_f32 v34, v88, v89\n"
    "v_cvt_pk_bf16_f32 v35, v90, v91\n"
    "v_cvt_pk_bf16_f32 v36, v36, v37\n"
    "v_cvt_pk_bf16_f32 v37, v38, v39\n"
    "v_permlane32_swap_b32 v32, v34\n"
    "v_permlane32_swap_b32 v33, v35\n"
    "v_min_u32 v36, -2, v36\n"
    "v_min_u32 v32, -2, v32\n"
    "buffer_store_dwordx4 v[32:35], v7, s[16:19], s50 offen sc1\n"
    "buffer_store_dwordx2 v[36:37], v8, s[16:19], 0 offen sc1\n"
    "ds_read_b128 v[228:231], v16 offset:2560\n"
    "buffer_load_dwordx4 a[188:191], v2, s[16:19], s53 offen offset:2048 sc1\n"
    "ds_read_b128 v[232:235], v16 offset:2624\n"
    "ds_read_b64 v[236:237], v17 offset:2560\n"
    "ds_read_b128 v[60:63], v16 offset:5120\n"
    "ds_read_b128 v[64:67], v16 offset:5184\n"
    "buffer_load_dwordx4 a[192:195], v2, s[16:19], s53 offen offset:3072 sc1\n"
    "ds_read_b64 v[68:69], v17 offset:5120\n"
    "s_waitcnt vmcnt(38)\n"
    "v_lshlrev_b32 v80, 16, v240\n"
    "v_and_b32 v81, 0xffff0000, v240\n"
    "v_lshlrev_b32 v82, 16, v241\n"
    "s_add_u32 s53, s53, 4096\n"
    "buffer_load_dwordx4 a[196:199], v2, s[16:19], s53 offen sc1\n"
    "v_and_b32 v83, 0xffff0000, v241\n"
    "v_lshlrev_b32 v84, 16, v242\n"
    "v_and_b32 v85, 0xffff0000, v242\n"
    "v_lshlrev_b32 v86, 16, v243\n"
    "v_and_b32 v87, 0xffff0000, v243\n"
    "s_waitcnt vmcnt(38)\n"
    "v_lshlrev_b32 v88, 16, v244\n"
    "buffer_load_dwordx4 a[200:203], v2, s[16:19], s53 offen offset:1024 sc1\n"
    "v_and_b32 v89, 0xffff0000, v244\n"
    "v_lshlrev_b32 v90, 16, v245\n"
    "v_and_b32 v91, 0xffff0000, v245\n"
    "v_lshlrev_b32 v92, 16, v246\n"
    "buffer_load_dwordx4 a[204:207], v2, s[16:19], s53 offen offset:2048 sc1\n"
    "v_and_b32 v93, 0xffff0000, v246\n"
    "v_lshlrev_b32 v94, 16, v247\n"
    "v_and_b32 v95, 0xffff0000, v247\n"
    "s_waitcnt vmcnt(39)\n"
    "v_lshlrev_b32 v96, 16, v248\n"
    "buffer_load_dwordx4 a[208:211], v2, s[16:19], s53 offen offset:3072 sc1\n"
    "v_and_b32 v97, 0xffff0000, v248\n"
    "v_lshlrev_b32 v98, 16, v249\n"
    "v_and_b32 v99, 0xffff0000, v249\n"
    "s_waitcnt lgkmcnt(5)\n"
    "v_lshlrev_b32 v100, 16, v228\n"
    "s_add_u32 s53, s53, 4096\n"
    "buffer_load_dwordx4 a[212:215], v2, s[16:19], s53 offen sc1\n"
    "v_and_b32 v101, 0xffff0000, v228\n"
    "v_lshlrev_b32 v102, 16, v229\n"
    "v_and_b32 v103, 0xffff0000, v229\n"
    "v_lshlrev_b32 v104, 16, v230\n"
    "v_and_b32 v105, 0xffff0000, v230\n"
    "v_lshlrev_b32 v106, 16, v231\n"
    "v_and_b32 v107, 0xffff0000, v231\n"
    "buffer_load_dwordx4 a[216:219], v2, s[16:19], s53 offen offset:1024 sc1\n"
    "s_waitcnt lgkmcnt(4)\n"
    "v_lshlrev_b32 v108, 16, v232\n"
    "v_and_b32 v109, 0xffff0000, v232\n"
    "v_lshlrev_b32 v110, 16, v233\n"
    "v_and_b32 v111, 0xffff0000, v233\n"
    "buffer_load_dwordx4 a[220:223], v2, s[16:19], s53 offen offset:2048 sc1\n"
    "v_lshlrev_b32 v112, 16, v234\n"
    "v_and_b32 v113, 0xffff0000, v234\n"
    "v_lshlrev_b32 v114, 16, v235\n"
    "v_and_b32 v115, 0xffff0000, v235\n"
    "buffer_load_dwordx4 a[224:227], v2, s[16:19], s53 offen offset:3072 sc1\n"
    "s_waitcnt lgkmcnt(3)\n"
    "v_lshlrev_b32 v116, 16, v236\n"
    "v_and_b32 v117, 0xffff0000, v236\n"
    "v_lshlrev_b32 v118, 16, v237\n"
    "v_and_b32 v119, 0xffff0000, v237\n"
    "s_add_u32 s53, s53, 4096\n"
    "buffer_load_dwordx4 a[228:231], v2, s[16:19], s53 offen sc1\n"
    "s_waitcnt lgkmcnt(2)\n"
    "v_lshlrev_b32 v208, 16, v60\n"
    "v_and_b32 v209, 0xffff0000, v60\n"
    "v_lshlrev_b32 v210, 16, v61\n"
    "v_and_b32 v211, 0xffff0000, v61\n"
    "v_lshlrev_b32 v212, 16, v62\n"
    "v_and_b32 v213, 0xffff0000, v62\n"
    "buffer_load_dwordx4 a[232:235], v2, s[16:19], s53 offen offset:1024 sc1\n"
    "v_lshlrev_b32 v214, 16, v63\n"
    "v_and_b32 v215, 0xffff0000, v63\n"
    "s_waitcnt lgkmcnt(1)\n"
    "v_lshlrev_b32 v216, 16, v64\n"
    "v_and_b32 v217, 0xffff0000, v64\n"
    "buffer_load_dwordx4 a[236:239], v2, s[16:19], s53 offen offset:2048 sc1\n"
    "v_lshlrev_b32 v218, 16, v65\n"
    "v_and_b32 v219, 0xffff0000, v65\n"
    "v_lshlrev_b32 v220, 16, v66\n"
    "v_and_b32 v221, 0xffff0000, v66\n"
    "v_lshlrev_b32 v222, 16, v67\n"
    "v_and_b32 v223, 0xffff0000, v67\n"
    "s_waitcnt lgkmcnt(0)\n"
    "v_lshlrev_b32 v224, 16, v68\n"
    "v_and_b32 v225, 0xffff0000, v68\n"
    "v_lshlrev_b32 v226, 16, v69\n"
    "v_and_b32 v227, 0xffff0000, v69\n"
    "buffer_load_dwordx4 v[128:131], v2, s[16:19], s64 offen sc1\n"
    "buffer_load_dwordx4 v[132:135], v2, s[16:19], s64 offen offset:1024 sc1\n"
    "buffer_load_dwordx4 v[136:139], v2, s[16:19], s64 offen offset:2048 sc1\n"
    "buffer_load_dwordx4 v[140:143], v2, s[16:19], s64 offen offset:3072 sc1\n"
    "buffer_load_dwordx2 v[144:145], v11, s[16:19], s64 offen sc1\n"
    "buffer_load_dwordx2 v[146:147], v11, s[16:19], s64 offen offset:512 sc1\n"
    "s_waitcnt vmcnt(3)\n"
    "v_max3_u32 v24, v128, v132, v136\n"
    "s_waitcnt vmcnt(1)\n"
    "v_max3_u32 v24, v24, v140, v144\n"
    "s_waitcnt vmcnt(0)\n"
    "v_max_u32 v24, v24, v146\n"
    "v_cmp_eq_u32 vcc, -1, v24\n"
    "s_nop 1\n"
    "s_cbranch_vccz .Ld4_%=\n"
    ".Lr4_%=:\n"
    "s_cmp_eq_u32 s42, 0\n"
    "s_cbranch_scc1 .Ld4_%=\n"
    "s_sub_u32 s42, s42, 1\n"
    "buffer_load_dwordx4 v[128:131], v2, s[16:19], s64 offen sc1\n"
    "buffer_load_dwordx4 v[132:135], v2, s[16:19], s64 offen offset:1024 sc1\n"
    "buffer_load_dwordx4 v[136:139], v2, s[16:19], s64 offen offset:2048 sc1\n"
    "buffer_load_dwordx4 v[140:143], v2, s[16:19], s64 offen offset:3072 sc1\n"
    "buffer_load_dwordx2 v[144:145], v11, s[16:19], s64 offen sc1\n"
    "buffer_load_dwordx2 v[146:147], v11, s[16:19], s64 offen offset:512 sc1\n"
    "s_waitcnt vmcnt(0)\n"
    "v_max3_u32 v24, v128, v132, v136\n"
    "v_max3_u32 v24, v24, v140, v144\n"
    "v_max_u32 v24, v24, v146\n"
    "v_cmp_eq_u32 vcc, -1, v24\n"
    "s_nop 1\n"
    "s_cbranch_vccnz .Lr4_%=\n"
    ".Ld4_%=:\n"
    "buffer_load_dwordx4 v[148:151], v2, s[16:19], s65 offen sc1\n"
    "buffer_load_dwordx4 v[152:155], v2, s[16:19], s65 offen offset:1024 sc1\n"
    "buffer_load_dwordx4 v[156:159], v2, s[16:19], s65 offen offset:2048 sc1\n"
    "buffer_load_dwordx4 v[160:163], v2, s[16:19], s65 offen offset:3072 sc1\n"
    "buffer_load_dwordx2 v[164:165], v11, s[16:19], s65 offen sc1\n"
    "buffer_load_dwordx2 v[166:167], v11, s[16:19], s65 offen offset:512 sc1\n"
    "buffer_load_dwordx4 v[168:171], v2, s[16:19], s66 offen sc1\n"
    "buffer_load_dwordx4 v[172:175], v2, s[16:19], s66 offen offset:1024 sc1\n"
    "buffer_load_dwordx4 v[176:179], v2, s[16:19], s66 offen offset:2048 sc1\n"
    "buffer_load_dwordx4 v[180:183], v2, s[16:19], s66 offen offset:3072 sc1\n"
    "buffer_load_dwordx2 v[184:185], v11, s[16:19], s66 offen sc1\n"
    "buffer_load_dwordx2 v[186:187], v11, s[16:19], s66 offen offset:512 sc1\n"
    "buffer_load_dwordx4 v[188:191], v2, s[16:19], s67 offen sc1\n"
    "buffer_load_dwordx4 v[192:195], v2, s[16:19], s67 offen offset:1024 sc1\n"
    "buffer_load_dwordx4 v[196:199], v2, s[16:19], s67 offen offset:2048 sc1\n"
    "buffer_load_dwordx4 v[200:203], v2, s[16:19], s67 offen offset:3072 sc1\n"
    "buffer_load_dwordx2 v[204:205], v11, s[16:19], s67 offen sc1\n"
    "buffer_load_dwordx2 v[206:207], v11, s[16:19], s67 offen offset:512 sc1\n"
    "v_mfma_f32_16x16x32_bf16 v[40:43], a[64:67], v[128:131], 0\n"
    "buffer_load_dwordx4 a[240:243], v2, s[16:19], s53 offen offset:3072 sc1\n"
    "v_mfma_f32_16x16x32_bf16 v[44:47], a[68:71], v[128:131], 0\n"
    "v_mfma_f32_16x16x32_bf16 v[48:51], a[72:75], v[128:131], 0\n"
    "v_mfma_f32_16x16x32_bf16 v[52:55], a[76:79], v[128:131], 0\n"
    "v_mfma_f32_16x16x32_bf16 v[56:59], a[80:83], v[128:131], 0\n"
    "s_add_u32 s53, s53, 4096\n"
    "v_mfma_f32_16x16x32_bf16 v[40:43], a[84:87], v[132:135], v[40:43]\n"
    "buffer_load_dwordx4 a[244:247], v2, s[16:19], s53 offen sc1\n"
    "v_mfma_f32_16x16x32_bf16 v[44:47], a[88:91], v[132:135], v[44:47]\n"
    "v_mfma_f32_16x16x32_bf16 v[48:51], a[92:95], v[132:135], v[48:51]\n"
    "v_mfma_f32_16x16x32_bf16 v[52:55], a[96:99], v[132:135], v[52:55]\n"
    "buffer_load_dwordx4 a[248:251], v2, s[16:19], s53 offen offset:1024 sc1\n"
    "v_mfma_f32_16x16x32_bf16 v[56:59], a[100:103], v[132:135], v[56:59]\n"
    "v_mfma_f32_16x16x32_bf16 v[40:43], a[104:107], v[136:139], v[40:43]\n"
    "v_mfma_f32_16x16x32_bf16 v[44:47], a[108:111], v[136:139], v[44:47]\n"
    "v_mfma_f32_16x16x32_bf16 v[48:51], a[112:115], v[136:139], v[48:51]\n"
    "buffer_load_dwordx4 a[252:255], v2, s[16:19], s53 offen offset:2048 sc1\n"
    "v_mfma_f32_16x16x32_bf16 v[52:55], a[116:119], v[136:139], v[52:55]\n"
    "v_mfma_f32_16x16x32_bf16 v[56:59], a[120:123], v[136:139], v[56:59]\n"
    "v_mfma_f32_16x16x32_bf16 v[40:43], a[124:127], v[140:143], v[40:43]\n"
    "v_mfma_f32_16x16x32_bf16 v[44:47], a[128:131], v[140:143], v[44:47]\n"
    "buffer_load_dwordx4 a[0:3], v2, s[16:19], s53 offen offset:3072 sc1\n"
    "v_mfma_f32_16x16x32_bf16 v[48:51], a[132:135], v[140:143], v[48:51]\n"
    "v_mfma_f32_16x16x32_bf16 v[52:55], a[136:139], v[140:143], v[52:55]\n"
    "v_mfma_f32_16x16x32_bf16 v[56:59], a[140:143], v[140:143], v[56:59]\n"
    "v_mfma_f32_16x16x32_bf16 v[40:43], a[144:147], v[144:147], v[40:43]\n"
    "s_add_u32 s53, s53, 4096\n"
    "v_mfma_f32_16x16x32_bf16 v[44:47], a[148:151], v[144:147], v[44:47]\n"
    "buffer_load_dwordx4 a[4:7], v2, s[16:19], s53 offen sc1\n"
    "v_mfma_f32_16x16x32_bf16 v[48:51], a[152:155], v[144:147], v[48:51]\n"
    "v_mfma_f32_16x16x32_bf16 v[52:55], a[156:159], v[144:147], v[52:55]\n"
    "v_mfma_f32_16x16x32_bf16 v[56:59], a[160:163], v[144:147], v[56:59]\n"
    "s_waitcnt vmcnt(21)\n"
    "v_max3_u32 v24, v148, v152, v156\n"
    "s_waitcnt vmcnt(19)\n"
    "v_max3_u32 v24, v24, v160, v164\n"
    "s_waitcnt vmcnt(18)\n"
    "v_max_u32 v24, v24, v166\n"
    "v_cmp_eq_u32 vcc, -1, v24\n"
    "s_nop 1\n"
    "s_cbranch_vccnz .Lr5_%=\n"
    ".Ld5_%=:\n"
    "v_mfma_f32_16x16x32_bf16 v[60:63], a[64:67], v[148:151], 0\n"
    "buffer_load_dwordx4 a[8:11], v2, s[16:19], s53 offen offset:1024 sc1\n"
    "v_mfma_f32_16x16x32_bf16 v[64:67], a[68:71], v[148:151], 0\n"
    "v_mfma_f32_16x16x32_bf16 v[68:71], a[72:75], v[148:151], 0\n"
    "v_mfma_f32_16x16x32_bf16 v[72:75], a[76:79], v[148:151], 0\n"
    "buffer_load_dwordx4 a[12:15], v2, s[16:19], s53 offen offset:2048 sc1\n"
    "v_mfma_f32_16x16x32_bf16 v[76:79], a[80:83], v[148:151], 0\n"
    "ds_write_b128 v20, v[40:43]\n"
    "v_mfma_f32_16x16x32_bf16 v[60:63], a[84:87], v[152:155], v[60:63]\n"
    "v_mfma_f32_16x16x32_bf16 v[64:67], a[88:91], v[152:155], v[64:67]\n"
    "v_mfma_f32_16x16x32_bf16 v[68:71], a[92:95], v[152:155], v[68:71]\n"
    "ds_write_b128 v20, v[44:47] offset:1024\n"
    "v_mfma_f32_16x16x32_bf16 v[72:75], a[96:99], v[152:155], v[72:75]\n"
    "buffer_load_dwordx4 a[16:19], v2, s[16:19], s53 offen offset:3072 sc1\n"
    "v_mfma_f32_16x16x32_bf16 v[76:79], a[100:103], v[152:155], v[76:79]\n"
    "v_mfma_f32_16x16x32_bf16 v[60:63], a[104:107], v[156:159], v[60:63]\n"
    "ds_write_b128 v20, v[48:51] offset:2048\n"
    "v_mfma_f32_16x16x32_bf16 v[64:67], a[108:111], v[156:159], v[64:67]\n"
    "s_add_u32 s53, s53, 4096\n"
    "v_mfma_f32_16x16x32_bf16 v[68:71], a[112:115], v[156:159], v[68:71]\n"
    "buffer_load_dwordx4 a[20:23], v2, s[16:19], s53 offen sc1\n"
    "v_mfma_f32_16x16x32_bf16 v[72:75], a[116:119], v[156:159], v[72:75]\n"
    "v_mfma_f32_16x16x32_bf16 v[76:79], a[120:123], v[156:159], v[76:79]\n"
    "ds_write_b128 v20, v[52:55] offset:3072\n"
    "v_mfma_f32_16x16x32_bf16 v[60:63], a[124:127], v[160:163], v[60:63]\n"
    "buffer_load_dwordx4 a[24:27], v2, s[16:19], s53 offen offset:1024 sc1\n"
    "v_mfma_f32_16x16x32_bf16 v[64:67], a[128:131], v[160:163], v[64:67]\n"
    "v_mfma_f32_16x16x32_bf16 v[68:71], a[132:135], v[160:163], v[68:71]\n"
    "ds_write_b128 v20, v[56:59] offset:4096\n"
    "v_mfma_f32_16x16x32_bf16 v[72:75], a[136:139], v[160:163], v[72:75]\n"
    "v_mfma_f32_16x16x32_bf16 v[76:79], a[140:143], v[160:163], v[76:79]\n"
    "buffer_load_dwordx4 a[28:31], v2, s[16:19], s53 offen offset:2048 sc1\n"
    "v_mfma_f32_16x16x32_bf16 v[60:63], a[144:147], v[164:167], v[60:63]\n"
    "v_mfma_f32_16x16x32_bf16 v[64:67], a[148:151], v[164:167], v[64:67]\n"
    "v_mfma_f32_16x16x32_bf16 v[68:71], a[152:155], v[164:167], v[68:71]\n"
    "v_mfma_f32_16x16x32_bf16 v[72:75], a[156:159], v[164:167], v[72:75]\n"
    "buffer_load_dwordx4 a[32:35], v2, s[16:19], s53 offen offset:3072 sc1\n"
    "v_mfma_f32_16x16x32_bf16 v[76:79], a[160:163], v[164:167], v[76:79]\n"
    "s_waitcnt vmcnt(22)\n"
    "v_max3_u32 v24, v168, v172, v176\n"
    "s_waitcnt vmcnt(20)\n"
    "v_max3_u32 v24, v24, v180, v184\n"
    "s_waitcnt vmcnt(19)\n"
    "v_max_u32 v24, v24, v186\n"
    "v_cmp_eq_u32 vcc, -1, v24\n"
    "s_nop 1\n"
    "s_cbranch_vccnz .Lr6_%=\n"
    ".Ld6_%=:\n"
    "v_mfma_f32_16x16x32_bf16 v[40:43], a[64:67], v[168:171], 0\n"
    "v_mfma_f32_16x16x32_bf16 v[44:47], a[68:71], v[168:171], 0\n"
    "v_mfma_f32_16x16x32_bf16 v[48:51], a[72:75], v[168:171], 0\n"
    "s_add_u32 s53, s53, 4096\n"
    "v_mfma_f32_16x16x32_bf16 v[52:55], a[76:79], v[168:171], 0\n"
    "buffer_load_dwordx4 a[36:39], v2, s[16:19], s53 offen sc1\n"
    "v_mfma_f32_16x16x32_bf16 v[56:59], a[80:83], v[168:171], 0\n"
    "ds_write_b128 v21, v[60:63]\n"
    "v_mfma_f32_16x16x32_bf16 v[40:43], a[84:87], v[172:175], v[40:43]\n"
    "v_mfma_f32_16x16x32_bf16 v[44:47], a[88:91], v[172:175], v[44:47]\n"
    "buffer_load_dwordx4 a[40:43], v2, s[16:19], s53 offen offset:1024 sc1\n"
    "v_mfma_f32_16x16x32_bf16 v[48:51], a[92:95], v[172:175], v[48:51]\n"
    "ds_write_b128 v21, v[64:67] offset:1024\n"
    "v_mfma_f32_16x16x32_bf16 v[52:55], a[96:99], v[172:175], v[52:55]\n"
    "v_mfma_f32_16x16x32_bf16 v[56:59], a[100:103], v[172:175], v[56:59]\n"
    "v_mfma_f32_16x16x32_bf16 v[40:43], a[104:107], v[176:179], v[40:43]\n"
    "ds_write_b128 v21, v[68:71] offset:2048\n"
    "v_mfma_f32_16x16x32_bf16 v[44:47], a[108:111], v[176:179], v[44:47]\n"
    "buffer_load_dwordx4 a[44:47], v2, s[16:19], s53 offen offset:2048 sc1\n"
    "v_mfma_f32_16x16x32_bf16 v[48:51], a[112:115], v[176:179], v[48:51]\n"
    "v_mfma_f32_16x16x32_bf16 v[52:55], a[116:119], v[176:179], v[52:55]\n"
    "v_mfma_f32_16x16x32_bf16 v[56:59], a[120:123], v[176:179], v[56:59]\n"
    "ds_write_b128 v21, v[72:75] offset:3072\n"
    "v_mfma_f32_16x16x32_bf16 v[40:43], a[124:127], v[180:183], v[40:43]\n"
    "buffer_load_dwordx4 a[48:51], v2, s[16:19], s53 offen offset:3072 sc1\n"
    "v_mfma_f32_16x16x32_bf16 v[44:47], a[128:131], v[180:183], v[44:47]\n"
    "v_mfma_f32_16x16x32_bf16 v[48:51], a[132:135], v[180:183], v[48:51]\n"
    "ds_write_b128 v21, v[76:79] offset:4096\n"
    "v_mfma_f32_16x16x32_bf16 v[52:55], a[136:139], v[180:183], v[52:55]\n"
    "s_add_u32 s53, s53, 4096\n"
    "v_mfma_f32_16x16x32_bf16 v[56:59], a[140:143], v[180:183], v[56:59]\n"
    "buffer_load_dwordx4 a[52:55], v2, s[16:19], s53 offen sc1\n"
    "v_mfma_f32_16x16x32_bf16 v[40:43], a[144:147], v[184:187], v[40:43]\n"
    "v_mfma_f32_16x16x32_bf16 v[44:47], a[148:151], v[184:187], v[44:47]\n"
    "v_mfma_f32_16x16x32_bf16 v[48:51], a[152:155], v[184:187], v[48:51]\n"
    "buffer_load_dwordx4 a[56:59], v2, s[16:19], s53 offen offset:1024 sc1\n"
    "v_mfma_f32_16x16x32_bf16 v[52:55], a[156:159], v[184:187], v[52:55]\n"
    "v_mfma_f32_16x16x32_bf16 v[56:59], a[160:163], v[184:187], v[56:59]\n"
    "s_waitcnt vmcnt(22)\n"
    "v_max3_u32 v24, v188, v192, v196\n"
    "s_waitcnt vmcnt(20)\n"
    "v_max3_u32 v24, v24, v200, v204\n"
    "s_waitcnt vmcnt(19)\n"
    "v_max_u32 v24, v24, v206\n"
    "v_cmp_eq_u32 vcc, -1, v24\n"
    "s_nop 1\n"
    "s_cbranch_vccnz .Lr7_%=\n"
    ".Ld7_%=:\n"
    "v_mfma_f32_16x16x32_bf16 v[60:63], a[64:67], v[188:191], 0\n"
    "v_mfma_f32_16x16x32_bf16 v[64:67], a[68:71], v[188:191], 0\n"
    "buffer_load_dwordx4 a[60:63], v2, s[16:19], s53 offen offset:2048 sc1\n"
    "v_mfma_f32_16x16x32_bf16 v[68:71], a[72:75], v[188:191], 0\n"
    "v_mfma_f32_16x16x32_bf16 v[72:75], a[76:79], v[188:191], 0\n"
    "v_mfma_f32_16x16x32_bf16 v[76:79], a[80:83], v[188:191], 0\n"
    "ds_write_b128 v22, v[40:43]\n"
    "v_mfma_f32_16x16x32_bf16 v[60:63], a[84:87], v[192:195], v[60:63]\n"
    "ds_write_b128 v22, v[44:47] offset:1024\n"
    "v_mfma_f32_16x16x32_bf16 v[64:67], a[88:91], v[192:195], v[64:67]\n"
    "ds_write_b128 v22, v[48:51] offset:2048\n"
    "v_mfma_f32_16x16x32_bf16 v[68:71], a[92:95], v[192:195], v[68:71]\n"
    "ds_write_b128 v22, v[52:55] offset:3072\n"
    "v_mfma_f32_16x16x32_bf16 v[72:75], a[96:99], v[192:195], v[72:75]\n"
    "ds_write_b128 v22, v[56:59] offset:4096\n"
    "v_mfma_f32_16x16x32_bf16 v[76:79], a[100:103], v[192:195], v[76:79]\n"
    "buffer_load_dwordx4 a[64:67], v2, s[16:19], s53 offen offset:3072 sc1\n"
    "v_mfma_f32_16x16x32_bf16 v[60:63], a[104:107], v[196:199], v[60:63]\n"
    "s_add_u32 s53, s53, 4096\n"
    "v_mfma_f32_16x16x32_bf16 v[64:67], a[108:111], v[196:199], v[64:67]\n"
    "buffer_load_dwordx4 a[68:71], v2, s[16:19], s53 offen sc1\n"
    "v_mfma_f32_16x16x32_bf16 v[68:71], a[112:115], v[196:199], v[68:71]\n"
    "s_waitcnt lgkmcnt(0)\n"
    "s_barrier\n"
    "ds_read_b128 v[128:131], v23\n"
    "ds_read_b128 v[148:151], v23 offset:5120\n"
    "v_mfma_f32_16x16x32_bf16 v[72:75], a[116:119], v[196:199], v[72:75]\n"
    "ds_read_b128 v[168:171], v23 offset:10240\n"
    "v_mfma_f32_16x16x32_bf16 v[76:79], a[120:123], v[196:199], v[76:79]\n"
    "ds_read_b128 v[132:135], v23 offset:1024\n"
    "ds_read_b128 v[152:155], v23 offset:6144\n"
    "v_mfma_f32_16x16x32_bf16 v[60:63], a[124:127], v[200:203], v[60:63]\n"
    "ds_read_b128 v[172:175], v23 offset:11264\n"
    "v_mfma_f32_16x16x32_bf16 v[64:67], a[128:131], v[200:203], v[64:67]\n"
    "ds_read_b128 v[136:139], v23 offset:2048\n"
    "ds_read_b128 v[156:159], v23 offset:7168\n"
    "v_mfma_f32_16x16x32_bf16 v[68:71], a[132:135], v[200:203], v[68:71]\n"
    "ds_read_b128 v[176:179], v23 offset:12288\n"
    "v_mfma_f32_16x16x32_bf16 v[72:75], a[136:139], v[200:203], v[72:75]\n"
    "ds_read_b128 v[140:143], v23 offset:3072\n"
    "ds_read_b128 v[160:163], v23 offset:8192\n"
    "v_mfma_f32_16x16x32_bf16 v[76:79], a[140:143], v[200:203], v[76:79]\n"
    "ds_read_b128 v[180:183], v23 offset:13312\n"
    "buffer_load_dwordx4 a[72:75], v2, s[16:19], s53 offen offset:1024 sc1\n"
    "v_mfma_f32_16x16x32_bf16 v[60:63], a[144:147], v[204:207], v[60:63]\n"
    "ds_read_b128 v[144:147], v23 offset:4096\n"
    "ds_read_b128 v[164:167], v23 offset:9216\n"
    "v_mfma_f32_16x16x32_bf16 v[64:67], a[148:151], v[204:207], v[64:67]\n"
    "ds_read_b128 v[184:187], v23 offset:14336\n"
    "v_mfma_f32_16x16x32_bf16 v[68:71], a[152:155], v[204:207], v[68:71]\n"
    "buffer_load_dwordx4 a[76:79], v2, s[16:19], s53 offen offset:2048 sc1\n"
    "v_mfma_f32_16x16x32_bf16 v[72:75], a[156:159], v[204:207], v[72:75]\n"
    "buffer_load_dwordx4 a[80:83], v2, s[16:19], s53 offen offset:3072 sc1\n"
    "v_mfma_f32_16x16x32_bf16 v[76:79], a[160:163], v[204:207], v[76:79]\n"
    "s_add_u32 s53, s53, 4096\n"
    "buffer_load_dwordx4 a[84:87], v2, s[16:19], s53 offen sc1\n"
    "buffer_load_dwordx4 a[88:91], v2, s[16:19], s53 offen offset:1024 sc1\n"
    "buffer_load_dwordx4 a[92:95], v2, s[16:19], s53 offen offset:2048 sc1\n"
    "buffer_load_dwordx4 a[96:99], v2, s[16:19], s53 offen offset:3072 sc1\n"
    "s_add_u32 s53, s53, 4096\n"
    "buffer_load_dwordx4 a[100:103], v2, s[16:19], s53 offen sc1\n"
    "s_waitcnt lgkmcnt(12)\n"
    "v_pk_add_f32 v[148:149], v[148:149], v[168:169]\n"
    "v_pk_add_f32 v[150:151], v[150:151], v[170:171]\n"
    "v_pk_add_f32 v[128:129], v[128:129], v[148:149]\n"
    "v_pk_add_f32 v[130:131], v[130:131], v[150:151]\n"
    "v_pk_add_f32 v[60:61], v[60:61], v[128:129]\n"
    "v_pk_add_f32 v[62:63], v[62:63], v[130:131]\n"
    "v_pk_fma_f32 v[80:81], v[60:61], v[100:101], v[80:81]\n"
    "v_pk_fma_f32 v[82:83], v[62:63], v[102:103], v[82:83]\n"
    "s_waitcnt lgkmcnt(9)\n"
    "v_pk_add_f32 v[152:153], v[152:153], v[172:173]\n"
    "v_pk_add_f32 v[154:155], v[154:155], v[174:175]\n"
    "v_pk_add_f32 v[132:133], v[132:133], v[152:153]\n"
    "v_pk_add_f32 v[134:135], v[134:135], v[154:155]\n"
    "v_pk_add_f32 v[64:65], v[64:65], v[132:133]\n"
    "v_pk_add_f32 v[66:67], v[66:67], v[134:135]\n"
    "v_pk_fma_f32 v[84:85], v[64:65], v[104:105], v[84:85]\n"
    "v_pk_fma_f32 v[86:87], v[66:67], v[106:107], v[86:87]\n"
    "v_pk_mul_f32 v[60:61], v[80:81], v[208:209]\n"
    "v_pk_mul_f32 v[62:63], v[82:83], v[210:211]\n"
    "v_cvt_pk_bf16_f32 v40, v60, v61\n"
    "v_cvt_pk_bf16_f32 v41, v62, v63\n"
    "v_pk_mul_f32 v[64:65], v[84:85], v[212:213]\n"
    "v_pk_mul_f32 v[66:67], v[86:87], v[214:215]\n"
    "v_cvt_pk_bf16_f32 v42, v64, v65\n"
    "v_cvt_pk_bf16_f32 v43, v66, v67\n"
    "v_min_u32 v40, -2, v40\n"
    "buffer_store_dwordx4 v[40:43], v2, s[16:19], s55 offen sc1\n"
    "s_waitcnt lgkmcnt(6)\n"
    "v_pk_add_f32 v[156:157], v[156:157], v[176:177]\n"
    "v_pk_add_f32 v[158:159], v[158:159], v[178:179]\n"
    "v_pk_add_f32 v[136:137], v[136:137], v[156:157]\n"
    "v_pk_add_f32 v[138:139], v[138:139], v[158:159]\n"
    "v_pk_add_f32 v[68:69], v[68:69], v[136:137]\n"
    "v_pk_add_f32 v[70:71], v[70:71], v[138:139]\n"
    "v_pk_fma_f32 v[88:89], v[68:69], v[108:109], v[88:89]\n"
    "v_pk_fma_f32 v[90:91], v[70:71], v[110:111], v[90:91]\n"
    "s_waitcnt lgkmcnt(3)\n"
    "v_pk_add_f32 v[160:161], v[160:161], v[180:181]\n"
    "v_pk_add_f32 v[162:163], v[162:163], v[182:183]\n"
    "v_pk_add_f32 v[140:141], v[140:141], v[160:161]\n"
    "v_pk_add_f32 v[142:143], v[142:143], v[162:163]\n"
    "v_pk_add_f32 v[72:73], v[72:73], v[140:141]\n"
    "v_pk_add_f32 v[74:75], v[74:75], v[142:143]\n"
    "v_pk_fma_f32 v[92:93], v[72:73], v[112:113], v[92:93]\n"
    "v_pk_fma_f32 v[94:95], v[74:75], v[114:115], v[94:95]\n"
    "v_pk_mul_f32 v[68:69], v[88:89], v[216:217]\n"
    "v_pk_mul_f32 v[70:71], v[90:91], v[218:219]\n"
    "v_cvt_pk_bf16_f32 v44, v68, v69\n"
    "v_cvt_pk_bf16_f32 v45, v70, v71\n"
    "v_pk_mul_f32 v[72:73], v[92:93], v[220:221]\n"
    "v_pk_mul_f32 v[74:75], v[94:95], v[222:223]\n"
    "v_cvt_pk_bf16_f32 v46, v72, v73\n"
    "v_cvt_pk_bf16_f32 v47, v74, v75\n"
    "v_min_u32 v44, -2, v44\n"
    "buffer_store_dwordx4 v[44:47], v2, s[16:19], s55 offen offset:1024 sc1\n"
    "s_waitcnt lgkmcnt(0)\n"
    "v_pk_add_f32 v[164:165], v[164:165], v[184:185]\n"
    "v_pk_add_f32 v[166:167], v[166:167], v[186:187]\n"
    "v_pk_add_f32 v[144:145], v[144:145], v[164:165]\n"
    "v_pk_add_f32 v[146:147], v[146:147], v[166:167]\n"
    "v_pk_add_f32 v[76:77], v[76:77], v[144:145]\n"
    "v_pk_add_f32 v[78:79], v[78:79], v[146:147]\n"
    "v_pk_fma_f32 v[96:97], v[76:77], v[116:117], v[96:97]\n"
    "v_pk_fma_f32 v[98:99], v[78:79], v[118:119], v[98:99]\n"
    "v_pk_mul_f32 v[76:77], v[96:97], v[224:225]\n"
    "v_pk_mul_f32 v[78:79], v[98:99], v[226:227]\n"
    "v_cvt_pk_bf16_f32 v48, v76, v77\n"
    "v_cvt_pk_bf16_f32 v49, v78, v79\n"
    "v_cvt_pk_bf16_f32 v240, v80, v81\n"
    "v_cvt_pk_bf16_f32 v241, v82, v83\n"
    "v_permlane32_swap_b32 v48, v50\n"
    "v_permlane32_swap_b32 v49, v51\n"
    "v_min_u32 v48, -2, v48\n"
    "buffer_store_dwordx4 v[48:51], v14, s[16:19], s56 offen sc1\n"
    "v_cvt_pk_bf16_f32 v242, v84, v85\n"
    "v_cvt_pk_bf16_f32 v243, v86, v87\n"
    "v_cvt_pk_bf16_f32 v244, v88, v89\n"
    "v_cvt_pk_bf16_f32 v245, v90, v91\n"
    "v_cvt_pk_bf16_f32 v246, v92, v93\n"
    "v_cvt_pk_bf16_f32 v247, v94, v95\n"
    "v_cvt_pk_bf16_f32 v248, v96, v97\n"
    "v_cvt_pk_bf16_f32 v249, v98, v99\n"
    "v_dot2_f32_bf16 v52, v240, s43, 0\n"
    "v_dot2_f32_bf16 v53, v240, v240, 0\n"
    "v_dot2_f32_bf16 v52, v241, s43, v52\n"
    "v_dot2_f32_bf16 v53, v241, v241, v53\n"
    "v_dot2_f32_bf16 v52, v242, s43, v52\n"
    "v_dot2_f32_bf16 v53, v242, v242, v53\n"
    "v_dot2_f32_bf16 v52, v243, s43, v52\n"
    "v_dot2_f32_bf16 v53, v243, v243, v53\n"
    "v_dot2_f32_bf16 v52, v244, s43, v52\n"
    "v_dot2_f32_bf16 v53, v244, v244, v53\n"
    "v_dot2_f32_bf16 v52, v245, s43, v52\n"
    "v_dot2_f32_bf16 v53, v245, v245, v53\n"
    "v_dot2_f32_bf16 v52, v246, s43, v52\n"
    "v_dot2_f32_bf16 v53, v246, v246, v53\n"
    "v_dot2_f32_bf16 v52, v247, s43, v52\n"
    "v_dot2_f32_bf16 v53, v247, v247, v53\n"
    "v_dot2_f32_bf16 v52, v248, s43, v52\n"
    "v_dot2_f32_bf16 v53, v248, v248, v53\n"
    "v_dot2_f32_bf16 v52, v249, s43, v52\n"
    "v_dot2_f32_bf16 v53, v249, v249, v53\n"
    "s_nop 2\n"
    "v_permlane32_swap_b32 v52, v53\n"
    "v_add_f32 v52, v52, v53\n"
    "v_mov_b32 v53, v52\n"
    "s_nop 1\n"
    "v_permlane16_swap_b32 v52, v53\n"
    "v_add_f32 v52, v52, v53\n"
    "s_nop 1\n"
    "v_permlane32_swap_b32 v52, v53\n"
    "v_min_u32 v52, -2, v52\n"
    "buffer_store_dwordx2 v[52:53], v15, s[16:19], 0 offen sc1\n"
    "v_or_b32 v24, s2, v0\n"
    "v_mov_b32 v25, 0\n"
    "v_cmp_eq_u32 vcc, 0, v24\n"
    "s_nop 1\n"
    "v_cndmask_b32 v32, 0x40000000, v25, vcc\n"
    "s_cmp_lg_u32 s2, 0\n"
    "s_cbranch_scc1 .Lep_%=\n"
    "buffer_store_dword v3, v32, s[16:19], 0 offen sc1\n"
    ".Lep_%=:\n"
    "buffer_load_dwordx4 a[104:107], v2, s[16:19], s53 offen offset:1024 sc1\n"
    "buffer_load_dwordx4 a[108:111], v2, s[16:19], s53 offen offset:2048 sc1\n"
    "buffer_load_dwordx4 a[112:115], v2, s[16:19], s53 offen offset:3072 sc1\n"
    "s_add_u32 s53, s53, 4096\n"
    "buffer_load_dwordx4 a[116:119], v2, s[16:19], s53 offen sc1\n"
    "buffer_load_dwordx4 a[120:123], v2, s[16:19], s53 offen offset:1024 sc1\n"
    "buffer_load_dwordx4 a[124:127], v2, s[16:19], s53 offen offset:2048 sc1\n"
    "buffer_load_dwordx4 a[128:131], v2, s[16:19], s53 offen offset:3072 sc1\n"
    "s_add_u32 s53, s53, 4096\n"
    "buffer_load_dwordx4 a[132:135], v2, s[16:19], s53 offen sc1\n"
    "buffer_load_dwordx4 a[136:139], v2, s[16:19], s53 offen offset:1024 sc1\n"
    "buffer_load_dwordx4 a[140:143], v2, s[16:19], s53 offen offset:2048 sc1\n"
    "buffer_load_dwordx4 a[144:147], v2, s[16:19], s53 offen offset:3072 sc1\n"
    "v_and_b32 v24, 1, v1\n"
    "v_mov_b32 v25, 0x27500\n"
    "v_mov_b32 v26, 0x27000\n"
    "v_cmp_eq_u32 vcc, 0, v24\n"
    "s_nop 1\n"
    "v_cndmask_b32 v33, v26, v25, vcc\n"
    "v_lshrrev_b32 v24, 4, v1\n"
    "v_lshl_add_u32 v32, v24, 4, v33\n"
    "v_lshrrev_b32 v24, 5, v1\n"
    "v_mul_u32_u24 v24, 0xa0, v24\n"
    "v_bfe_u32 v25, v1, 4, 1\n"
    "v_lshl_add_u32 v25, v25, 4, v33\n"
    "v_add_u32 v33, v24, v25\n"
    "ds_read_b128 v[88:91], v32\n"
    "ds_read_b128 v[92:95], v32 offset:64\n"
    "ds_read_b128 v[96:99], v32 offset:160\n"
    "ds_read_b128 v[100:103], v32 offset:224\n"
    "s_waitcnt lgkmcnt(3)\n"
    "v_mfma_f32_16x16x32_bf16 v[40:43], a[164:167], v[88:91], 0\n"
    "ds_read_b128 v[104:107], v33 offset:128\n"
    "s_lshl_b32 s92, s38, 3\n"
    "v_mfma_f32_16x16x32_bf16 v[44:47], a[168:171], v[88:91], 0\n"
    "s_lshl_b32 s93, s36, 1\n"
    "v_mfma_f32_16x16x32_bf16 v[48:51], a[172:175], v[88:91], 0\n"
    "s_add_u32 s92, s92, s93\n"
    "s_waitcnt lgkmcnt(3)\n"
    "v_mfma_f32_16x16x32_bf16 v[40:43], a[176:179], v[92:95], v[40:43]\n"
    "ds_read_b128 v[108:111], v32 offset:320\n"
    "s_lshl_b32 s92, s92, 9\n"
    "v_mfma_f32_16x16x32_bf16 v[44:47], a[180:183], v[92:95], v[44:47]\n"
    "s_add_u32 s77, s92, 0xaf000\n"
    "v_mfma_f32_16x16x32_bf16 v[48:51], a[184:187], v[92:95], v[48:51]\n"
    "s_waitcnt lgkmcnt(2)\n"
    "s_mul_i32 s92, s36, 0x1400\n"
    "v_mfma_f32_16x16x32_bf16 v[40:43], a[188:191], v[96:99], v[40:43]\n"
    "ds_read_b128 v[88:91], v32 offset:384\n"
    "v_lshl_add_u32 v4, v1, 4, s92\n"
    "v_mfma_f32_16x16x32_bf16 v[44:47], a[192:195], v[96:99], v[44:47]\n"
    "v_lshlrev_b32 v24, 4, v1\n"
    "v_mfma_f32_16x16x32_bf16 v[48:51], a[196:199], v[96:99], v[48:51]\n"
    "v_add_u32 v5, 0xa000, v24\n"
    "v_mfma_f32_16x16x32_bf16 v[40:43], a[200:203], v[100:103], v[40:43]\n"
    "ds_read_b128 v[92:95], v32 offset:480\n"
    "s_lshl_b32 s92, s36, 9\n"
    "v_mfma_f32_16x16x32_bf16 v[44:47], a[204:207], v[100:103], v[44:47]\n"
    "s_add_u32 s92, s92, 0x14000\n"
    "v_mfma_f32_16x16x32_bf16 v[48:51], a[208:211], v[100:103], v[48:51]\n"
    "s_waitcnt lgkmcnt(2)\n"
    "v_lshl_add_u32 v6, v1, 3, s92\n"
    "v_mfma_f32_16x16x32_bf16 v[40:43], a[212:215], v[104:107], v[40:43]\n"
    "ds_read_b128 v[96:99], v32 offset:544\n"
    "s_lshr_b32 s92, s37, 1\n"
    "v_mfma_f32_16x16x32_bf16 v[44:47], a[216:219], v[104:107], v[44:47]\n"
    "v_lshrrev_b32 v24, 4, v1\n"
    "v_mfma_f32_16x16x32_bf16 v[48:51], a[220:223], v[104:107], v[48:51]\n"
    "v_add_u32 v24, s92, v24\n"
    "v_mfma_f32_16x16x32_bf16 v[40:43], a[224:227], v[108:111], v[40:43]\n"
    "ds_read_b128 v[100:103], v33 offset:448\n"
    "v_add_u32 v24, 1, v24\n"
    "v_mfma_f32_16x16x32_bf16 v[44:47], a[228:231], v[108:111], v[44:47]\n"
    "v_and_b32 v24, 3, v24\n"
    "v_mfma_f32_16x16x32_bf16 v[48:51], a[232:235], v[108:111], v[48:51]\n"
    "s_waitcnt vmcnt(40) lgkmcnt(2)\n"
    "v_and_b32 v25, 15, v1\n"
    "v_mfma_f32_16x16x32_bf16 v[40:43], a[236:239], v[88:91], v[40:43]\n"
    "ds_read_b128 v[104:107], v32 offset:640\n"
    "v_lshl_add_u32 v24, v24, 4, v25\n"
    "v_mfma_f32_16x16x32_bf16 v[44:47], a[240:243], v[88:91], v[44:47]\n"
    "v_lshlrev_b32 v24, 3, v24\n"
    "v_mfma_f32_16x16x32_bf16 v[48:51], a[244:247], v[88:91], v[48:51]\n"
    "v_add_u32 v7, 0x14000, v24\n"
    "v_mfma_f32_16x16x32_bf16 v[40:43], a[248:251], v[92:95], v[40:43]\n"
    "ds_read_b128 v[108:111], v32 offset:704\n"
    "v_add_u32 v9, 0x2000, v2\n"
    "v_mfma_f32_16x16x32_bf16 v[44:47], a[252:255], v[92:95], v[44:47]\n"
    "s_mov_b32 s76, 0xbfb8aa3b\n"
    "v_mfma_f32_16x16x32_bf16 v[48:51], a[0:3], v[92:95], v[48:51]\n"
    "s_waitcnt vmcnt(34) lgkmcnt(2)\n"
    "v_mov_b32 v8, s76\n"
    "v_mfma_f32_16x16x32_bf16 v[40:43], a[4:7], v[96:99], v[40:43]\n"
    "ds_read_b128 v[88:91], v32 offset:800\n"
    "v_mfma_f32_16x16x32_bf16 v[44:47], a[8:11], v[96:99], v[44:47]\n"
    "v_mfma_f32_16x16x32_bf16 v[48:51], a[12:15], v[96:99], v[48:51]\n"
    "v_mfma_f32_16x16x32_bf16 v[40:43], a[16:19], v[100:103], v[40:43]\n"
    "ds_read_b128 v[92:95], v32 offset:864\n"
    "v_mfma_f32_16x16x32_bf16 v[44:47], a[20:23], v[100:103], v[44:47]\n"
    "v_mfma_f32_16x16x32_bf16 v[48:51], a[24:27], v[100:103], v[48:51]\n"
    "s_waitcnt vmcnt(28) lgkmcnt(2)\n"
    "v_mfma_f32_16x16x32_bf16 v[40:43], a[28:31], v[104:107], v[40:43]\n"
    "ds_read_b128 v[96:99], v33 offset:768\n"
    "v_mfma_f32_16x16x32_bf16 v[44:47], a[32:35], v[104:107], v[44:47]\n"
    "v_mfma_f32_16x16x32_bf16 v[48:51], a[36:39], v[104:107], v[48:51]\n"
    "v_mfma_f32_16x16x32_bf16 v[40:43], a[40:43], v[108:111], v[40:43]\n"
    "ds_read_b128 v[100:103], v32 offset:960\n"
    "v_mfma_f32_16x16x32_bf16 v[44:47], a[44:47], v[108:111], v[44:47]\n"
    "v_mfma_f32_16x16x32_bf16 v[48:51], a[48:51], v[108:111], v[48:51]\n"
    "s_waitcnt vmcnt(22) lgkmcnt(2)\n"
    "v_mfma_f32_16x16x32_bf16 v[40:43], a[52:55], v[88:91], v[40:43]\n"
    "ds_read_b128 v[104:107], v32 offset:1024\n"
    "v_mfma_f32_16x16x32_bf16 v[44:47], a[56:59], v[88:91], v[44:47]\n"
    "v_mfma_f32_16x16x32_bf16 v[48:51], a[60:63], v[88:91], v[48:51]\n"
    "v_mfma_f32_16x16x32_bf16 v[40:43], a[64:67], v[92:95], v[40:43]\n"
    "ds_read_b128 v[108:111], v32 offset:1120\n"
    "v_mfma_f32_16x16x32_bf16 v[44:47], a[68:71], v[92:95], v[44:47]\n"
    "buffer_load_dwordx4 v[128:131], v2, s[16:19], s68 offen sc1\n"
    "buffer_load_dwordx4 v[132:135], v2, s[16:19], s68 offen offset:1024 sc1\n"
    "buffer_load_dwordx4 v[136:139], v2, s[16:19], s68 offen offset:2048 sc1\n"
    "buffer_load_dwordx4 v[140:143], v2, s[16:19], s68 offen offset:3072 sc1\n"
    "buffer_load_dwordx4 v[144:147], v10, s[16:19], s68 offen sc1\n"
    "v_mfma_f32_16x16x32_bf16 v[48:51], a[72:75], v[92:95], v[48:51]\n"
    "s_waitcnt vmcnt(21) lgkmcnt(2)\n"
    "v_mfma_f32_16x16x32_bf16 v[40:43], a[76:79], v[96:99], v[40:43]\n"
    "ds_read_b128 v[88:91], v32 offset:1184\n"
    "v_mfma_f32_16x16x32_bf16 v[44:47], a[80:83], v[96:99], v[44:47]\n"
    "v_mfma_f32_16x16x32_bf16 v[48:51], a[84:87], v[96:99], v[48:51]\n"
    "v_mfma_f32_16x16x32_bf16 v[40:43], a[88:91], v[100:103], v[40:43]\n"
    "ds_read_b128 v[92:95], v33 offset:1088\n"
    "v_mfma_f32_16x16x32_bf16 v[44:47], a[92:95], v[100:103], v[44:47]\n"
    "v_mfma_f32_16x16x32_bf16 v[48:51], a[96:99], v[100:103], v[48:51]\n"
    "s_waitcnt vmcnt(11) lgkmcnt(2)\n"
    "v_mfma_f32_16x16x32_bf16 v[40:43], a[100:103], v[104:107], v[40:43]\n"
    "v_mfma_f32_16x16x32_bf16 v[44:47], a[104:107], v[104:107], v[44:47]\n"
    "v_mfma_f32_16x16x32_bf16 v[48:51], a[108:111], v[104:107], v[48:51]\n"
    "v_mfma_f32_16x16x32_bf16 v[40:43], a[112:115], v[108:111], v[40:43]\n"
    "v_mfma_f32_16x16x32_bf16 v[44:47], a[116:119], v[108:111], v[44:47]\n"
    "v_mfma_f32_16x16x32_bf16 v[48:51], a[120:123], v[108:111], v[48:51]\n"
    "s_waitcnt vmcnt(5) lgkmcnt(0)\n"
    "v_mfma_f32_16x16x32_bf16 v[40:43], a[124:127], v[88:91], v[40:43]\n"
    "v_mfma_f32_16x16x32_bf16 v[44:47], a[128:131], v[88:91], v[44:47]\n"
    "v_mfma_f32_16x16x32_bf16 v[48:51], a[132:135], v[88:91], v[48:51]\n"
    "v_mfma_f32_16x16x32_bf16 v[40:43], a[136:139], v[92:95], v[40:43]\n"
    "v_mfma_f32_16x16x32_bf16 v[44:47], a[140:143], v[92:95], v[44:47]\n"
    "v_mfma_f32_16x16x32_bf16 v[48:51], a[144:147], v[92:95], v[48:51]\n"
    "s_nop 5\n"
    "v_mov_b32_dpp v64, v40 quad_perm:[0,0,2,2] row_mask:0xf bank_mask:0xf\n"
    "v_mul_f32_dpp v76, v40, v8 quad_perm:[1,1,3,3] row_mask:0xf bank_mask:0xf\n"
    "v_mov_b32_dpp v65, v41 quad_perm:[0,0,2,2] row_mask:0xf bank_mask:0xf\n"
    "v_mul_f32_dpp v77, v41, v8 quad_perm:[1,1,3,3] row_mask:0xf bank_mask:0xf\n"
    "v_mov_b32_dpp v66, v42 quad_perm:[0,0,2,2] row_mask:0xf bank_mask:0xf\n"
    "v_mul_f32_dpp v78, v42, v8 quad_perm:[1,1,3,3] row_mask:0xf bank_mask:0xf\n"
    "v_mov_b32_dpp v67, v43 quad_perm:[0,0,2,2] row_mask:0xf bank_mask:0xf\n"
    "v_mul_f32_dpp v79, v43, v8 quad_perm:[1,1,3,3] row_mask:0xf bank_mask:0xf\n"
    "v_mov_b32_dpp v68, v44 quad_perm:[0,0,2,2] row_mask:0xf bank_mask:0xf\n"
    "v_mul_f32_dpp v80, v44, v8 quad_perm:[1,1,3,3] row_mask:0xf bank_mask:0xf\n"
    "v_mov_b32_dpp v69, v45 quad_perm:[0,0,2,2] row_mask:0xf bank_mask:0xf\n"
    "v_mul_f32_dpp v81, v45, v8 quad_perm:[1,1,3,3] row_mask:0xf bank_mask:0xf\n"
    "v_mov_b32_dpp v70, v46 quad_perm:[0,0,2,2] row_mask:0xf bank_mask:0xf\n"
    "v_mul_f32_dpp v82, v46, v8 quad_perm:[1,1,3,3] row_mask:0xf bank_mask:0xf\n"
    "v_mov_b32_dpp v71, v47 quad_perm:[0,0,2,2] row_mask:0xf bank_mask:0xf\n"
    "v_mul_f32_dpp v83, v47, v8 quad_perm:[1,1,3,3] row_mask:0xf bank_mask:0xf\n"
    "v_mov_b32_dpp v72, v48 quad_perm:[0,0,2,2] row_mask:0xf bank_mask:0xf\n"
    "v_mul_f32_dpp v84, v48, v8 quad_perm:[1,1,3,3] row_mask:0xf bank_mask:0xf\n"
    "v_mov_b32_dpp v73, v49 quad_perm:[0,0,2,2] row_mask:0xf bank_mask:0xf\n"
    "v_mul_f32_dpp v85, v49, v8 quad_perm:[1,1,3,3] row_mask:0xf bank_mask:0xf\n"
    "v_mov_b32_dpp v74, v50 quad_perm:[0,0,2,2] row_mask:0xf bank_mask:0xf\n"
    "v_mul_f32_dpp v86, v50, v8 quad_perm:[1,1,3,3] row_mask:0xf bank_mask:0xf\n"
    "v_mov_b32_dpp v75, v51 quad_perm:[0,0,2,2] row_mask:0xf bank_mask:0xf\n"
    "v_mul_f32_dpp v87, v51, v8 quad_perm:[1,1,3,3] row_mask:0xf bank_mask:0xf\n"
    "s_waitcnt vmcnt(2)\n"
    "v_max3_u32 v24, v128, v132, v136\n"
    "s_waitcnt vmcnt(0)\n"
    "v_max3_u32 v24, v24, v140, v144\n"
    "v_cmp_eq_u32 vcc, -1, v24\n"
    "s_nop 1\n"
    "s_cbranch_vccz .Ld8_%=\n"
    ".Lr8_%=:\n"
    "s_cmp_eq_u32 s42, 0\n"
    "s_cbranch_scc1 .Ld8_%=\n"
    "s_sub_u32 s42, s42, 1\n"
    "buffer_load_dwordx4 v[128:131], v2, s[16:19], s68 offen sc1\n"
    "buffer_load_dwordx4 v[132:135], v2, s[16:19], s68 offen offset:1024 sc1\n"
    "buffer_load_dwordx4 v[136:139], v2, s[16:19], s68 offen offset:2048 sc1\n"
    "buffer_load_dwordx4 v[140:143], v2, s[16:19], s68 offen offset:3072 sc1\n"
    "buffer_load_dwordx4 v[144:147], v10, s[16:19], s68 offen sc1\n"
    "s_waitcnt vmcnt(0)\n"
    "v_max3_u32 v24, v128, v132, v136\n"
    "v_max3_u32 v24, v24, v140, v144\n"
    "v_cmp_eq_u32 vcc, -1, v24\n"
    "s_nop 1\n"
    "s_cbranch_vccnz .Lr8_%=\n"
    ".Ld8_%=:\n"
    "buffer_load_dwordx4 v[148:151], v2, s[16:19], s69 offen sc1\n"
    "buffer_load_dwordx4 v[152:155], v2, s[16:19], s69 offen offset:1024 sc1\n"
    "buffer_load_dwordx4 v[156:159], v2, s[16:19], s69 offen offset:2048 sc1\n"
    "buffer_load_dwordx4 v[160:163], v2, s[16:19], s69 offen offset:3072 sc1\n"
    "buffer_load_dwordx4 v[164:167], v10, s[16:19], s69 offen sc1\n"
    "buffer_load_dwordx2 v[120:121], v11, s[16:19], s77 offen sc1\n"
    "buffer_load_dwordx2 v[122:123], v11, s[16:19], s77 offen offset:512 sc1\n"
    "ds_write_b128 v4, v[128:131]\n"
    "ds_write_b128 v4, v[132:135] offset:1024\n"
    "ds_write_b128 v4, v[136:139] offset:2048\n"
    "ds_write_b128 v4, v[140:143] offset:3072\n"
    "ds_write_b128 v4, v[144:147] offset:4096\n"
    "buffer_load_dwordx4 v[168:171], v2, s[16:19], s70 offen sc1\n"
    "buffer_load_dwordx4 v[172:175], v2, s[16:19], s70 offen offset:1024 sc1\n"
    "buffer_load_dwordx4 v[176:179], v2, s[16:19], s70 offen offset:2048 sc1\n"
    "buffer_load_dwordx4 v[180:183], v2, s[16:19], s70 offen offset:3072 sc1\n"
    "buffer_load_dwordx4 v[184:187], v10, s[16:19], s70 offen sc1\n"
    "buffer_load_dwordx4 v[188:191], v2, s[16:19], s71 offen sc1\n"
    "buffer_load_dwordx4 v[192:195], v2, s[16:19], s71 offen offset:1024 sc1\n"
    "buffer_load_dwordx4 v[196:199], v2, s[16:19], s71 offen offset:2048 sc1\n"
    "buffer_load_dwordx4 v[200:203], v2, s[16:19], s71 offen offset:3072 sc1\n"
    "buffer_load_dwordx4 v[204:207], v10, s[16:19], s71 offen sc1\n"
    "s_waitcnt lgkmcnt(0)\n"
    "s_barrier\n"
    "ds_read_b128 v[88:91], v2\n"
    "ds_read_b128 v[92:95], v2 offset:1024\n"
    "ds_read_b128 v[96:99], v2 offset:2048\n"
    "ds_read_b128 v[100:103], v2 offset:3072\n"
    "s_waitcnt lgkmcnt(3)\n"
    "v_mfma_f32_16x16x32_bf16 v[40:43], a[164:167], v[88:91], 0\n"
    "ds_read_b128 v[104:107], v2 offset:4096\n"
    "v_mfma_f32_16x16x32_bf16 v[44:47], a[168:171], v[88:91], 0\n"
    "v_mfma_f32_16x16x32_bf16 v[48:51], a[172:175], v[88:91], 0\n"
    "s_waitcnt lgkmcnt(3)\n"
    "v_mfma_f32_16x16x32_bf16 v[40:43], a[176:179], v[92:95], v[40:43]\n"
    "ds_read_b128 v[108:111], v2 offset:5120\n"
    "v_mfma_f32_16x16x32_bf16 v[44:47], a[180:183], v[92:95], v[44:47]\n"
    "buffer_load_dwordx4 v[208:211], v2, s[16:19], s54 offen sc1\n"
    "v_mfma_f32_16x16x32_bf16 v[48:51], a[184:187], v[92:95], v[48:51]\n"
    "s_waitcnt lgkmcnt(2)\n"
    "v_mfma_f32_16x16x32_bf16 v[40:43], a[188:191], v[96:99], v[40:43]\n"
    "ds_read_b128 v[88:91], v2 offset:6144\n"
    "v_mfma_f32_16x16x32_bf16 v[44:47], a[192:195], v[96:99], v[44:47]\n"
    "v_mfma_f32_16x16x32_bf16 v[48:51], a[196:199], v[96:99], v[48:51]\n"
    "buffer_load_dwordx4 v[212:215], v2, s[16:19], s54 offen offset:1024 sc1\n"
    "v_mfma_f32_16x16x32_bf16 v[40:43], a[200:203], v[100:103], v[40:43]\n"
    "ds_read_b128 v[92:95], v2 offset:7168\n"
    "v_mfma_f32_16x16x32_bf16 v[44:47], a[204:207], v[100:103], v[44:47]\n"
    "buffer_store_dwordx4 v[250:253], v18, s[16:19], s61 offen sc1\n"
    "v_mfma_f32_16x16x32_bf16 v[48:51], a[208:211], v[100:103], v[48:51]\n"
    "s_waitcnt lgkmcnt(2)\n"
    "v_mfma_f32_16x16x32_bf16 v[40:43], a[212:215], v[104:107], v[40:43]\n"
    "ds_read_b128 v[96:99], v2 offset:8192\n"
    "buffer_load_dwordx4 v[216:219], v2, s[16:19], s54 offen offset:2048 sc1\n"
    "v_mfma_f32_16x16x32_bf16 v[44:47], a[216:219], v[104:107], v[44:47]\n"
    "v_mfma_f32_16x16x32_bf16 v[48:51], a[220:223], v[104:107], v[48:51]\n"
    "v_mfma_f32_16x16x32_bf16 v[40:43], a[224:227], v[108:111], v[40:43]\n"
    "ds_read_b128 v[100:103], v2 offset:9216\n"
    "v_mfma_f32_16x16x32_bf16 v[44:47], a[228:231], v[108:111], v[44:47]\n"
    "buffer_load_dwordx4 v[220:223], v2, s[16:19], s54 offen offset:3072 sc1\n"
    "v_mfma_f32_16x16x32_bf16 v[48:51], a[232:235], v[108:111], v[48:51]\n"
    "s_waitcnt lgkmcnt(2)\n"
    "v_mfma_f32_16x16x32_bf16 v[40:43], a[236:239], v[88:91], v[40:43]\n"
    "ds_read_b128 v[104:107], v2 offset:10240\n"
    "v_mfma_f32_16x16x32_bf16 v[44:47], a[240:243], v[88:91], v[44:47]\n"
    "v_mfma_f32_16x16x32_bf16 v[48:51], a[244:247], v[88:91], v[48:51]\n"
    "s_add_u32 s54, s54, 4096\n"
    "buffer_load_dwordx4 v[224:227], v2, s[16:19], s54 offen sc1\n"
    "v_mfma_f32_16x16x32_bf16 v[40:43], a[248:251], v[92:95], v[40:43]\n"
    "ds_read_b128 v[108:111], v2 offset:11264\n"
    "buffer_store_dwordx4 v[250:253], v19, s[16:19], s61 offen sc1\n"
    "v_mfma_f32_16x16x32_bf16 v[44:47], a[252:255], v[92:95], v[44:47]\n"
    "s_waitcnt vmcnt(21)\n"
    "v_max3_u32 v24, v148, v152, v156\n"
    "s_waitcnt vmcnt(19)\n"
    "v_max3_u32 v24, v24, v160, v164\n"
    "s_waitcnt vmcnt(17)\n"
    "v_max3_u32 v24, v24, v120, v122\n"
    "v_cmp_eq_u32 vcc, -1, v24\n"
    "s_nop 1\n"
    "s_cbranch_vccnz .Lr9_%=\n"
    ".Ld9_%=:\n"
    "v_mfma_f32_16x16x32_bf16 v[48:51], a[0:3], v[92:95], v[48:51]\n"
    "s_waitcnt lgkmcnt(2)\n"
    "v_add_f32 v120, v120, v122\n"
    "v_add_f32 v121, v121, v123\n"
    "v_mfma_f32_16x16x32_bf16 v[40:43], a[4:7], v[96:99], v[40:43]\n"
    "ds_read_b128 v[88:91], v2 offset:12288\n"
    "buffer_load_dwordx4 v[228:231], v2, s[16:19], s54 offen offset:1024 sc1\n"
    "v_mfma_f32_16x16x32_bf16 v[44:47], a[8:11], v[96:99], v[44:47]\n"
    "ds_write_b64 v6, v[120:121]\n"
    "v_mfma_f32_16x16x32_bf16 v[48:51], a[12:15], v[96:99], v[48:51]\n"
    "ds_write_b128 v4, v[148:151] offset:20480\n"
    "v_mfma_f32_16x16x32_bf16 v[40:43], a[16:19], v[100:103], v[40:43]\n"
    "ds_read_b128 v[92:95], v2 offset:13312\n"
    "v_mfma_f32_16x16x32_bf16 v[44:47], a[20:23], v[100:103], v[44:47]\n"
    "buffer_load_dwordx4 v[232:235], v2, s[16:19], s54 offen offset:2048 sc1\n"
    "v_mfma_f32_16x16x32_bf16 v[48:51], a[24:27], v[100:103], v[48:51]\n"
    "s_waitcnt lgkmcnt(4)\n"
    "v_mfma_f32_16x16x32_bf16 v[40:43], a[28:31], v[104:107], v[40:43]\n"
    "ds_read_b128 v[96:99], v2 offset:14336\n"
    "v_mfma_f32_16x16x32_bf16 v[44:47], a[32:35], v[104:107], v[44:47]\n"
    "ds_write_b128 v4, v[152:155] offset:21504\n"
    "v_mfma_f32_16x16x32_bf16 v[48:51], a[36:39], v[104:107], v[48:51]\n"
    "buffer_store_dwordx4 v[250:253], v18, s[16:19], s62 offen sc1\n"
    "v_mfma_f32_16x16x32_bf16 v[40:43], a[40:43], v[108:111], v[40:43]\n"
    "ds_read_b128 v[100:103], v2 offset:15360\n"
    "buffer_load_dwordx4 v[236:239], v2, s[16:19], s54 offen offset:3072 sc1\n"
    "v_mfma_f32_16x16x32_bf16 v[44:47], a[44:47], v[108:111], v[44:47]\n"
    "ds_write_b128 v4, v[156:159] offset:22528\n"
    "v_mfma_f32_16x16x32_bf16 v[48:51], a[48:51], v[108:111], v[48:51]\n"
    "s_waitcnt lgkmcnt(4)\n"
    "v_mfma_f32_16x16x32_bf16 v[40:43], a[52:55], v[88:91], v[40:43]\n"
    "ds_read_b128 v[104:107], v2 offset:16384\n"
    "s_add_u32 s54, s54, 4096\n"
    "v_mfma_f32_16x16x32_bf16 v[44:47], a[56:59], v[88:91], v[44:47]\n"
    "buffer_load_dwordx4 a[148:151], v2, s[16:19], s54 offen sc1\n"
    "v_mfma_f32_16x16x32_bf16 v[48:51], a[60:63], v[88:91], v[48:51]\n"
    "ds_write_b128 v4, v[160:163] offset:23552\n"
    "v_mfma_f32_16x16x32_bf16 v[40:43], a[64:67], v[92:95], v[40:43]\n"
    "ds_read_b128 v[108:111], v2 offset:17408\n"
    "v_mfma_f32_16x16x32_bf16 v[44:47], a[68:71], v[92:95], v[44:47]\n"
    "ds_write_b128 v4, v[164:167] offset:24576\n"
    "v_mfma_f32_16x16x32_bf16 v[48:51], a[72:75], v[92:95], v[48:51]\n"
    "s_waitcnt lgkmcnt(5)\n"
    "buffer_load_dwordx4 a[152:155], v2, s[16:19], s54 offen offset:1024 sc1\n"
    "v_mfma_f32_16x16x32_bf16 v[40:43], a[76:79], v[96:99], v[40:43]\n"
    "ds_read_b128 v[88:91], v2 offset:18432\n"
    "v_mfma_f32_16x16x32_bf16 v[44:47], a[80:83], v[96:99], v[44:47]\n"
    "buffer_store_dwordx4 v[250:253], v19, s[16:19], s62 offen sc1\n"
    "v_mfma_f32_16x16x32_bf16 v[48:51], a[84:87], v[96:99], v[48:51]\n"
    "buffer_load_dwordx4 a[156:159], v2, s[16:19], s54 offen offset:2048 sc1\n"
    "v_mfma_f32_16x16x32_bf16 v[40:43], a[88:91], v[100:103], v[40:43]\n"
    "ds_read_b128 v[92:95], v2 offset:19456\n"
    "v_mfma_f32_16x16x32_bf16 v[44:47], a[92:95], v[100:103], v[44:47]\n"
    "v_mfma_f32_16x16x32_bf16 v[48:51], a[96:99], v[100:103], v[48:51]\n"
    "s_waitcnt lgkmcnt(0)\n"
    "s_barrier\n"
    "ds_read_b64 v[24:25], v7\n"
    "ds_read_b64 v[26:27], v7 offset:512\n"
    "ds_read_b64 v[28:29], v7 offset:1024\n"
    "ds_read_b64 v[30:31], v7 offset:1536\n"
    "v_mfma_f32_16x16x32_bf16 v[40:43], a[100:103], v[104:107], v[40:43]\n"
    "ds_read_b128 v[96:99], v2 offset:20480\n"
    "buffer_load_dwordx4 a[160:163], v2, s[16:19], s54 offen offset:3072 sc1\n"
    "v_mfma_f32_16x16x32_bf16 v[44:47], a[104:107], v[104:107], v[44:47]\n"
    "v_mfma_f32_16x16x32_bf16 v[48:51], a[108:111], v[104:107], v[48:51]\n"
    "v_mfma_f32_16x16x32_bf16 v[40:43], a[112:115], v[108:111], v[40:43]\n"
    "ds_read_b128 v[100:103], v2 offset:21504\n"
    "v_mfma_f32_16x16x32_bf16 v[44:47], a[116:119], v[108:111], v[44:47]\n"
    "s_add_u32 s54, s54, 4096\n"
    "buffer_load_dwordx4 v[128:131], v2, s[16:19], s54 offen sc1\n"
    "v_mfma_f32_16x16x32_bf16 v[48:51], a[120:123], v[108:111], v[48:51]\n"
    "s_waitcnt lgkmcnt(4)\n"
    "v_add_f32 v24, v24, v26\n"
    "v_add_f32 v25, v25, v27\n"
    "v_mfma_f32_16x16x32_bf16 v[40:43], a[124:127], v[88:91], v[40:43]\n"
    "ds_read_b128 v[104:107], v2 offset:22528\n"
    "buffer_store_dwordx4 v[250:253], v18, s[16:19], s63 offen sc1\n"
    "v_mfma_f32_16x16x32_bf16 v[44:47], a[128:131], v[88:91], v[44:47]\n"
    "s_waitcnt lgkmcnt(3)\n"
    "v_add_f32 v28, v28, v30\n"
    "v_add_f32 v29, v29, v31\n"
    "v_add_f32 v24, v24, v28\n"
    "v_mfma_f32_16x16x32_bf16 v[48:51], a[132:135], v[88:91], v[48:51]\n"
    "v_add_f32 v25, v25, v29\n"
    "v_mul_f32 v24, 0x3acccccd, v24\n"
    "v_mul_f32 v25, 0x3acccccd, v25\n"
    "v_mfma_f32_16x16x32_bf16 v[40:43], a[136:139], v[92:95], v[40:43]\n"
    "ds_read_b128 v[108:111], v2 offset:23552\n"
    "buffer_load_dwordx4 v[132:135], v2, s[16:19], s54 offen offset:1024 sc1\n"
    "v_mfma_f32_16x16x32_bf16 v[44:47], a[140:143], v[92:95], v[44:47]\n"
    "v_fma_f32 v25, -v24, v24, v25\n"
    "v_max_f32 v25, 0, v25\n"
    "v_add_f32 v25, 0x3727c5ac, v25\n"
    "v_mfma_f32_16x16x32_bf16 v[48:51], a[144:147], v[92:95], v[48:51]\n"
    "s_waitcnt lgkmcnt(2)\n"
    "v_rsq_f32 v25, v25\n"
    "v_mfma_f32_16x16x32_bf16 v[52:55], a[164:167], v[96:99], 0\n"
    "ds_read_b128 v[88:91], v2 offset:24576\n"
    "v_mul_f32 v112, s76, v25\n"
    "v_mul_f32 v116, -v24, v112\n"
    "v_mfma_f32_16x16x32_bf16 v[56:59], a[168:171], v[96:99], 0\n"
    "v_mov_b32 v113, v112\n"
    "v_mov_b32 v117, v116\n"
    "v_mfma_f32_16x16x32_bf16 v[60:63], a[172:175], v[96:99], 0\n"
    "s_waitcnt vmcnt(26)\n"
    "v_max3_u32 v120, v168, v172, v176\n"
    "s_waitcnt vmcnt(24)\n"
    "v_max3_u32 v120, v120, v180, v184\n"
    "v_cmp_eq_u32 vcc, -1, v120\n"
    "s_nop 1\n"
    "s_cbranch_vccnz .Lr10_%=\n"
    ".Ld10_%=:\n"
    "v_mfma_f32_16x16x32_bf16 v[52:55], a[176:179], v[100:103], v[52:55]\n"
    "ds_read_b128 v[92:95], v2 offset:25600\n"
    "v_permlane16_swap_b32 v112, v113\n"
    "v_mfma_f32_16x16x32_bf16 v[56:59], a[180:183], v[100:103], v[56:59]\n"
    "v_permlane16_swap_b32 v116, v117\n"
    "v_mov_b32 v114, v112\n"
    "v_mfma_f32_16x16x32_bf16 v[60:63], a[184:187], v[100:103], v[60:63]\n"
    "s_waitcnt lgkmcnt(2)\n"
    "buffer_store_dwordx4 v[250:253], v19, s[16:19], s63 offen sc1\n"
    "v_mov_b32 v115, v113\n"
    "v_mov_b32 v118, v116\n"
    "v_mov_b32 v119, v117\n"
    "v_permlane32_swap_b32 v112, v114\n"
    "v_permlane32_swap_b32 v113, v115\n"
    "v_permlane32_swap_b32 v116, v118\n"
    "v_permlane32_swap_b32 v117, v119\n"
    "v_mfma_f32_16x16x32_bf16 v[52:55], a[188:191], v[104:107], v[52:55]\n"
    "ds_read_b128 v[96:99], v2 offset:26624\n"
    "buffer_load_dwordx4 v[136:139], v2, s[16:19], s54 offen offset:2048 sc1\n"
    "v_mfma_f32_16x16x32_bf16 v[56:59], a[192:195], v[104:107], v[56:59]\n"
    "buffer_load_dwordx4 v[140:143], v2, s[16:19], s54 offen offset:3072 sc1\n"
    "v_fma_f32 v24, v116, v64, v76\n"
    "v_mfma_f32_16x16x32_bf16 v[60:63], a[196:199], v[104:107], v[60:63]\n"
    "ds_write_b128 v4, v[168:171] offset:40960\n"
    "v_mfma_f32_16x16x32_bf16 v[52:55], a[200:203], v[108:111], v[52:55]\n"
    "ds_read_b128 v[100:103], v2 offset:27648\n"
    "v_fma_f32 v25, v116, v65, v77\n"
    "v_fma_f32 v26, v116, v66, v78\n"
    "v_mfma_f32_16x16x32_bf16 v[56:59], a[204:207], v[108:111], v[56:59]\n"
    "s_add_u32 s54, s54, 4096\n"
    "buffer_load_dwordx4 v[144:147], v2, s[16:19], s54 offen sc1\n"
    "v_mfma_f32_16x16x32_bf16 v[60:63], a[208:211], v[108:111], v[60:63]\n"
    "s_waitcnt lgkmcnt(3)\n"
    "v_fma_f32 v27, v116, v67, v79\n"
    "v_fma_f32 v40, v40, v112, v24\n"
    "v_mfma_f32_16x16x32_bf16 v[52:55], a[212:215], v[88:91], v[52:55]\n"
    "ds_read_b128 v[104:107], v2 offset:28672\n"
    "v_fma_f32 v41, v41, v112, v25\n"
    "v_fma_f32 v42, v42, v112, v26\n"
    "v_mfma_f32_16x16x32_bf16 v[56:59], a[216:219], v[88:91], v[56:59]\n"
    "v_fma_f32 v43, v43, v112, v27\n"
    "v_exp_f32 v24, v40\n"
    "v_mfma_f32_16x16x32_bf16 v[60:63], a[220:223], v[88:91], v[60:63]\n"
    "ds_write_b128 v4, v[172:175] offset:41984\n"
    "v_mfma_f32_16x16x32_bf16 v[52:55], a[224:227], v[92:95], v[52:55]\n"
    "ds_read_b128 v[108:111], v2 offset:29696\n"
    "v_exp_f32 v25, v41\n"
    "v_mfma_f32_16x16x32_bf16 v[56:59], a[228:231], v[92:95], v[56:59]\n"
    "buffer_load_dwordx4 v[148:151], v2, s[16:19], s54 offen offset:1024 sc1\n"
    "v_mfma_f32_16x16x32_bf16 v[60:63], a[232:235], v[92:95], v[60:63]\n"
    "s_waitcnt lgkmcnt(3)\n"
    "v_exp_f32 v26, v42\n"
    "v_mfma_f32_16x16x32_bf16 v[52:55], a[236:239], v[96:99], v[52:55]\n"
    "ds_read_b128 v[88:91], v2 offset:30720\n"
    "v_exp_f32 v27, v43\n"
    "v_mfma_f32_16x16x32_bf16 v[56:59], a[240:243], v[96:99], v[56:59]\n"
    "v_fma_f32 v24, v24, s76, s76\n"
    "v_fma_f32 v25, v25, s76, s76\n"
    "v_fma_f32 v26, v26, s76, s76\n"
    "v_mfma_f32_16x16x32_bf16 v[60:63], a[244:247], v[96:99], v[60:63]\n"
    "v_fma_f32 v27, v27, s76, s76\n"
    "buffer_load_dwordx4 v[152:155], v2, s[16:19], s54 offen offset:2048 sc1\n"
    "v_mfma_f32_16x16x32_bf16 v[52:55], a[248:251], v[100:103], v[52:55]\n"
    "ds_read_b128 v[92:95], v2 offset:31744\n"
    "v_rcp_f32 v24, v24\n"
    "v_mfma_f32_16x16x32_bf16 v[56:59], a[252:255], v[100:103], v[56:59]\n"
    "v_rcp_f32 v25, v25\n"
    "v_mfma_f32_16x16x32_bf16 v[60:63], a[0:3], v[100:103], v[60:63]\n"
    "s_waitcnt lgkmcnt(2)\n"
    "v_rcp_f32 v26, v26\n"
    "v_mfma_f32_16x16x32_bf16 v[52:55], a[4:7], v[104:107], v[52:55]\n"
    "ds_read_b128 v[96:99], v2 offset:32768\n"
    "v_rcp_f32 v27, v27\n"
    "v_mfma_f32_16x16x32_bf16 v[56:59], a[8:11], v[104:107], v[56:59]\n"
    "ds_write_b128 v4, v[176:179] offset:43008\n"
    "v_mfma_f32_16x16x32_bf16 v[60:63], a[12:15], v[104:107], v[60:63]\n"
    "buffer_load_dwordx4 v[156:159], v2, s[16:19], s54 offen offset:3072 sc1\n"
    "v_mul_f32 v40, v40, v24\n"
    "v_mfma_f32_16x16x32_bf16 v[52:55], a[16:19], v[108:111], v[52:55]\n"
    "ds_read_b128 v[100:103], v2 offset:33792\n"
    "v_mul_f32 v41, v41, v25\n"
    "v_mul_f32 v42, v42, v26\n"
    "v_mfma_f32_16x16x32_bf16 v[56:59], a[20:23], v[108:111], v[56:59]\n"
    "v_mul_f32 v43, v43, v27\n"
    "v_cvt_pk_bf16_f32 v32, v40, v41\n"
    "v_cvt_pk_bf16_f32 v33, v42, v43\n"
    "v_mfma_f32_16x16x32_bf16 v[60:63], a[24:27], v[108:111], v[60:63]\n"
    "s_waitcnt lgkmcnt(3)\n"
    "v_fma_f32 v28, v116, v68, v80\n"
    "v_fma_f32 v29, v116, v69, v81\n"
    "v_mfma_f32_16x16x32_bf16 v[52:55], a[28:31], v[88:91], v[52:55]\n"
    "ds_read_b128 v[104:107], v2 offset:34816\n"
    "v_fma_f32 v30, v116, v70, v82\n"
    "s_add_u32 s54, s54, 4096\n"
    "buffer_load_dwordx4 v[160:163], v2, s[16:19], s54 offen sc1\n"
    "v_mfma_f32_16x16x32_bf16 v[56:59], a[32:35], v[88:91], v[56:59]\n"
    "v_fma_f32 v31, v116, v71, v83\n"
    "v_fma_f32 v44, v44, v112, v28\n"
    "v_fma_f32 v45, v45, v112, v29\n"
    "v_mfma_f32_16x16x32_bf16 v[60:63], a[36:39], v[88:91], v[60:63]\n"
    "ds_write_b128 v4, v[180:183] offset:44032\n"
    "v_fma_f32 v46, v46, v112, v30\n"
    "v_mfma_f32_16x16x32_bf16 v[52:55], a[40:43], v[92:95], v[52:55]\n"
    "ds_read_b128 v[108:111], v2 offset:35840\n"
    "v_fma_f32 v47, v47, v112, v31\n"
    "v_exp_f32 v28, v44\n"
    "v_mfma_f32_16x16x32_bf16 v[56:59], a[44:47], v[92:95], v[56:59]\n"
    "v_exp_f32 v29, v45\n"
    "buffer_load_dwordx4 v[164:167], v2, s[16:19], s54 offen offset:1024 sc1\n"
    "v_mfma_f32_16x16x32_bf16 v[60:63], a[48:51], v[92:95], v[60:63]\n"
    "s_waitcnt lgkmcnt(3)\n"
    "v_exp_f32 v30, v46\n"
    "v_mfma_f32_16x16x32_bf16 v[52:55], a[52:55], v[96:99], v[52:55]\n"
    "ds_read_b128 v[88:91], v2 offset:36864\n"
    "v_exp_f32 v31, v47\n"
    "v_mfma_f32_16x16x32_bf16 v[56:59], a[56:59], v[96:99], v[56:59]\n"
    "v_fma_f32 v28, v28, s76, s76\n"
    "v_fma_f32 v29, v29, s76, s76\n"
    "v_fma_f32 v30, v30, s76, s76\n"
    "v_mfma_f32_16x16x32_bf16 v[60:63], a[60:63], v[96:99], v[60:63]\n"
    "v_fma_f32 v31, v31, s76, s76\n"
    "v_rcp_f32 v28, v28\n"
    "v_mfma_f32_16x16x32_bf16 v[52:55], a[64:67], v[100:103], v[52:55]\n"
    "ds_read_b128 v[92:95], v2 offset:37888\n"
    "ds_write_b128 v4, v[184:187] offset:45056\n"
    "v_mfma_f32_16x16x32_bf16 v[56:59], a[68:71], v[100:103], v[56:59]\n"
    "v_rcp_f32 v29, v29\n"
    "v_mfma_f32_16x16x32_bf16 v[60:63], a[72:75], v[100:103], v[60:63]\n"
    "s_waitcnt lgkmcnt(3)\n"
    "v_rcp_f32 v30, v30\n"
    "v_mfma_f32_16x16x32_bf16 v[52:55], a[76:79], v[104:107], v[52:55]\n"
    "ds_read_b128 v[96:99], v2 offset:38912\n"
    "v_rcp_f32 v31, v31\n"
    "v_mfma_f32_16x16x32_bf16 v[56:59], a[80:83], v[104:107], v[56:59]\n"
    "v_mul_f32 v44, v44, v28\n"
    "v_mul_f32 v45, v45, v29\n"
    "v_mul_f32 v46, v46, v30\n"
    "v_mfma_f32_16x16x32_bf16 v[60:63], a[84:87], v[104:107], v[60:63]\n"
    "v_mul_f32 v47, v47, v31\n"
    "v_cvt_pk_bf16_f32 v34, v44, v45\n"
    "v_cvt_pk_bf16_f32 v35, v46, v47\n"
    "v_mfma_f32_16x16x32_bf16 v[52:55], a[88:91], v[108:111], v[52:55]\n"
    "ds_read_b128 v[100:103], v2 offset:39936\n"
    "v_min_u32 v32, -2, v32\n"
    "s_waitcnt vmcnt(8)\n"
    "v_mfma_f32_16x16x32_bf16 v[56:59], a[92:95], v[108:111], v[56:59]\n"
    "buffer_store_dwordx4 v[32:35], v18, s[16:19], s60 offen sc1\n"
    "v_fma_f32 v24, v116, v72, v84\n"
    "v_mfma_f32_16x16x32_bf16 v[60:63], a[96:99], v[108:111], v[60:63]\n"
    "s_waitcnt lgkmcnt(0)\n"
    "s_barrier\n"
    "v_mfma_f32_16x16x32_bf16 v[52:55], a[100:103], v[88:91], v[52:55]\n"
    "ds_read_b128 v[104:107], v5\n"
    "v_fma_f32 v25, v116, v73, v85\n"
    "v_fma_f32 v26, v116, v74, v86\n"
    "buffer_load_dwordx4 v[168:171], v2, s[16:19], s54 offen offset:2048 sc1\n"
    "v_mfma_f32_16x16x32_bf16 v[56:59], a[104:107], v[88:91], v[56:59]\n"
    "v_fma_f32 v27, v116, v75, v87\n"
    "v_fma_f32 v48, v48, v112, v24\n"
    "v_fma_f32 v49, v49, v112, v25\n"
    "v_mfma_f32_16x16x32_bf16 v[60:63], a[108:111], v[88:91], v[60:63]\n"
    "v_fma_f32 v50, v50, v112, v26\n"
    "v_fma_f32 v51, v51, v112, v27\n"
    "v_mfma_f32_16x16x32_bf16 v[52:55], a[112:115], v[92:95], v[52:55]\n"
    "ds_read_b128 v[108:111], v5 offset:1024\n"
    "v_exp_f32 v24, v48\n"
    "v_mfma_f32_16x16x32_bf16 v[56:59], a[116:119], v[92:95], v[56:59]\n"
    "v_exp_f32 v25, v49\n"
    "buffer_load_dwordx4 v[172:175], v2, s[16:19], s54 offen offset:3072 sc1\n"
    "v_mfma_f32_16x16x32_bf16 v[60:63], a[120:123], v[92:95], v[60:63]\n"
    "v_exp_f32 v26, v50\n"
    "v_mfma_f32_16x16x32_bf16 v[52:55], a[124:127], v[96:99], v[52:55]\n"
    "ds_read_b128 v[88:91], v5 offset:2048\n"
    "v_exp_f32 v27, v51\n"
    "v_mfma_f32_16x16x32_bf16 v[56:59], a[128:131], v[96:99], v[56:59]\n"
    "v_fma_f32 v24, v24, s76, s76\n"
    "v_fma_f32 v25, v25, s76, s76\n"
    "v_fma_f32 v26, v26, s76, s76\n"
    "v_mfma_f32_16x16x32_bf16 v[60:63], a[132:135], v[96:99], v[60:63]\n"
    "v_fma_f32 v27, v27, s76, s76\n"
    "s_add_u32 s54, s54, 4096\n"
    "buffer_load_dwordx4 v[176:179], v2, s[16:19], s54 offen sc1\n"
    "v_mfma_f32_16x16x32_bf16 v[52:55], a[136:139], v[100:103], v[52:55]\n"
    "ds_read_b128 v[92:95], v5 offset:3072\n"
    "v_rcp_f32 v24, v24\n"
    "v_mfma_f32_16x16x32_bf16 v[56:59], a[140:143], v[100:103], v[56:59]\n"
    "v_rcp_f32 v25, v25\n"
    "v_mfma_f32_16x16x32_bf16 v[60:63], a[144:147], v[100:103], v[60:63]\n"
    "s_waitcnt lgkmcnt(2)\n"
    "v_rcp_f32 v26, v26\n"
    "v_rcp_f32 v27, v27\n"
    "v_mul_f32 v48, v48, v24\n"
    "v_mul_f32 v49, v49, v25\n"
    "v_mul_f32 v50, v50, v26\n"
    "v_mul_f32 v51, v51, v27\n"
    "v_cvt_pk_bf16_f32 v36, v48, v49\n"
    "v_cvt_pk_bf16_f32 v37, v50, v51\n"
    "s_nop 0\n"
    "v_permlane32_swap_b32 v36, v38\n"
    "v_permlane32_swap_b32 v37, v39\n"
    "v_min_u32 v36, -2, v36\n"
    "buffer_store_dwordx4 v[36:39], v19, s[16:19], s60 offen sc1\n"
    "v_mfma_f32_16x16x32_bf16 v[40:43], a[164:167], v[104:107], 0\n"
    "ds_read_b128 v[96:99], v5 offset:4096\n"
    "buffer_load_dwordx4 v[180:183], v2, s[16:19], s54 offen offset:1024 sc1\n"
    "v_mfma_f32_16x16x32_bf16 v[44:47], a[168:171], v[104:107], 0\n"
    "buffer_load_dwordx4 v[184:187], v2, s[16:19], s54 offen offset:2048 sc1\n"
    "v_mfma_f32_16x16x32_bf16 v[48:51], a[172:175], v[104:107], 0\n"
    "v_max3_u32 v120, v188, v192, v196\n"
    "v_max3_u32 v120, v120, v200, v204\n"
    "v_cmp_eq_u32 vcc, -1, v120\n"
    "s_nop 1\n"
    "s_cbranch_vccnz .Lr11_%=\n"
    ".Ld11_%=:\n"
    "v_mfma_f32_16x16x32_bf16 v[40:43], a[176:179], v[108:111], v[40:43]\n"
    "ds_read_b128 v[100:103], v5 offset:5120\n"
    "v_fma_f32 v24, v117, v64, v76\n"
    "v_fma_f32 v25, v117, v65, v77\n"
    "v_mfma_f32_16x16x32_bf16 v[44:47], a[180:183], v[108:111], v[44:47]\n"
    "ds_write_b128 v4, v[188:191] offset:61440\n"
    "v_mfma_f32_16x16x32_bf16 v[48:51], a[184:187], v[108:111], v[48:51]\n"
    "s_waitcnt lgkmcnt(3)\n"
    "v_fma_f32 v26, v117, v66, v78\n"
    "v_fma_f32 v27, v117, v67, v79\n"
    "v_mfma_f32_16x16x32_bf16 v[40:43], a[188:191], v[88:91], v[40:43]\n"
    "ds_read_b128 v[104:107], v5 offset:6144\n"
    "v_fma_f32 v52, v52, v113, v24\n"
    "v_fma_f32 v53, v53, v113, v25\n"
    "v_mfma_f32_16x16x32_bf16 v[44:47], a[192:195], v[88:91], v[44:47]\n"
    "v_fma_f32 v54, v54, v113, v26\n"
    "v_fma_f32 v55, v55, v113, v27\n"
    "v_mfma_f32_16x16x32_bf16 v[48:51], a[196:199], v[88:91], v[48:51]\n"
    "v_exp_f32 v24, v52\n"
    "v_mfma_f32_16x16x32_bf16 v[40:43], a[200:203], v[92:95], v[40:43]\n"
    "ds_read_b128 v[108:111], v5 offset:7168\n"
    "v_exp_f32 v25, v53\n"
    "v_mfma_f32_16x16x32_bf16 v[44:47], a[204:207], v[92:95], v[44:47]\n"
    "ds_write_b128 v4, v[192:195] offset:62464\n"
    "v_mfma_f32_16x16x32_bf16 v[48:51], a[208:211], v[92:95], v[48:51]\n"
    "s_waitcnt lgkmcnt(4)\n"
    "v_exp_f32 v26, v54\n"
    "v_mfma_f32_16x16x32_bf16 v[40:43], a[212:215], v[96:99], v[40:43]\n"
    "ds_read_b128 v[88:91], v5 offset:8192\n"
    "v_exp_f32 v27, v55\n"
    "v_mfma_f32_16x16x32_bf16 v[44:47], a[216:219], v[96:99], v[44:47]\n"
    "v_fma_f32 v24, v24, s76, s76\n"
    "v_fma_f32 v25, v25, s76, s76\n"
    "v_fma_f32 v26, v26, s76, s76\n"
    "v_mfma_f32_16x16x32_bf16 v[48:51], a[220:223], v[96:99], v[48:51]\n"
    "v_fma_f32 v27, v27, s76, s76\n"
    "v_rcp_f32 v24, v24\n"
    "v_mfma_f32_16x16x32_bf16 v[40:43], a[224:227], v[100:103], v[40:43]\n"
    "ds_read_b128 v[92:95], v5 offset:9216\n"
    "v_rcp_f32 v25, v25\n"
    "v_mfma_f32_16x16x32_bf16 v[44:47], a[228:231], v[100:103], v[44:47]\n"
    "v_rcp_f32 v26, v26\n"
    "v_mfma_f32_16x16x32_bf16 v[48:51], a[232:235], v[100:103], v[48:51]\n"
    "s_waitcnt lgkmcnt(3)\n"
    "v_rcp_f32 v27, v27\n"
    "v_mfma_f32_16x16x32_bf16 v[40:43], a[236:239], v[104:107], v[40:43]\n"
    "ds_read_b128 v[96:99], v5 offset:10240\n"
    "v_mul_f32 v52, v52, v24\n"
    "v_mul_f32 v53, v53, v25\n"
    "v_mfma_f32_16x16x32_bf16 v[44:47], a[240:243], v[104:107], v[44:47]\n"
    "ds_write_b128 v4, v[196:199] offset:63488\n"
    "v_mfma_f32_16x16x32_bf16 v[48:51], a[244:247], v[104:107], v[48:51]\n"
    "v_mul_f32 v54, v54, v26\n"
    "v_mul_f32 v55, v55, v27\n"
    "v_cvt_pk_bf16_f32 v32, v52, v53\n"
    "v_mfma_f32_16x16x32_bf16 v[40:43], a[248:251], v[108:111], v[40:43]\n"
    "ds_read_b128 v[100:103], v5 offset:11264\n"
    "v_cvt_pk_bf16_f32 v33, v54, v55\n"
    "v_fma_f32 v28, v117, v68, v80\n"
    "v_mfma_f32_16x16x32_bf16 v[44:47], a[252:255], v[108:111], v[44:47]\n"
    "v_fma_f32 v29, v117, v69, v81\n"
    "v_fma_f32 v30, v117, v70, v82\n"
    "v_fma_f32 v31, v117, v71, v83\n"
    "v_mfma_f32_16x16x32_bf16 v[48:51], a[0:3], v[108:111], v[48:51]\n"
    "s_waitcnt lgkmcnt(3)\n"
    "v_fma_f32 v56, v56, v113, v28\n"
    "v_fma_f32 v57, v57, v113, v29\n"
    "v_mfma_f32_16x16x32_bf16 v[40:43], a[4:7], v[88:91], v[40:43]\n"
    "ds_read_b128 v[104:107], v5 offset:12288\n"
    "v_fma_f32 v58, v58, v113, v30\n"
    "v_fma_f32 v59, v59, v113, v31\n"
    "v_mfma_f32_16x16x32_bf16 v[44:47], a[8:11], v[88:91], v[44:47]\n"
    "ds_write_b128 v4, v[200:203] offset:64512\n"
    "v_mfma_f32_16x16x32_bf16 v[48:51], a[12:15], v[88:91], v[48:51]\n"
    "v_exp_f32 v28, v56\n"
    "v_mfma_f32_16x16x32_bf16 v[40:43], a[16:19], v[92:95], v[40:43]\n"
    "ds_read_b128 v[108:111], v5 offset:13312\n"
    "v_exp_f32 v29, v57\n"
    "v_mfma_f32_16x16x32_bf16 v[44:47], a[20:23], v[92:95], v[44:47]\n"
    "v_exp_f32 v30, v58\n"
    "v_mfma_f32_16x16x32_bf16 v[48:51], a[24:27], v[92:95], v[48:51]\n"
    "s_waitcnt lgkmcnt(3)\n"
    "v_exp_f32 v31, v59\n"
    "v_mfma_f32_16x16x32_bf16 v[40:43], a[28:31], v[96:99], v[40:43]\n"
    "ds_read_b128 v[88:91], v5 offset:14336\n"
    "v_fma_f32 v28, v28, s76, s76\n"
    "v_fma_f32 v29, v29, s76, s76\n"
    "v_mfma_f32_16x16x32_bf16 v[44:47], a[32:35], v[96:99], v[44:47]\n"
    "v_add_u32 v124, 0x1000, v4\n"
    "ds_write_b128 v124, v[204:207] offset:61440\n"
    "v_mfma_f32_16x16x32_bf16 v[48:51], a[36:39], v[96:99], v[48:51]\n"
    "v_fma_f32 v30, v30, s76, s76\n"
    "v_fma_f32 v31, v31, s76, s76\n"
    "v_mfma_f32_16x16x32_bf16 v[40:43], a[40:43], v[100:103], v[40:43]\n"
    "ds_read_b128 v[92:95], v5 offset:15360\n"
    "v_rcp_f32 v28, v28\n"
    "v_mfma_f32_16x16x32_bf16 v[44:47], a[44:47], v[100:103], v[44:47]\n"
    "v_rcp_f32 v29, v29\n"
    "v_mfma_f32_16x16x32_bf16 v[48:51], a[48:51], v[100:103], v[48:51]\n"
    "s_waitcnt lgkmcnt(3)\n"
    "v_rcp_f32 v30, v30\n"
    "v_mfma_f32_16x16x32_bf16 v[40:43], a[52:55], v[104:107], v[40:43]\n"
    "ds_read_b128 v[96:99], v5 offset:16384\n"
    "v_rcp_f32 v31, v31\n"
    "v_mfma_f32_16x16x32_bf16 v[44:47], a[56:59], v[104:107], v[44:47]\n"
    "v_mul_f32 v56, v56, v28\n"
    "v_mul_f32 v57, v57, v29\n"
    "v_mul_f32 v58, v58, v30\n"
    "v_mfma_f32_16x16x32_bf16 v[48:51], a[60:63], v[104:107], v[48:51]\n"
    "v_mul_f32 v59, v59, v31\n"
    "v_cvt_pk_bf16_f32 v34, v56, v57\n"
    "v_cvt_pk_bf16_f32 v35, v58, v59\n"
    "v_mfma_f32_16x16x32_bf16 v[40:43], a[64:67], v[108:111], v[40:43]\n"
    "ds_read_b128 v[100:103], v5 offset:17408\n"
    "v_min_u32 v32, -2, v32\n"
    "v_mfma_f32_16x16x32_bf16 v[44:47], a[68:71], v[108:111], v[44:47]\n"
    "buffer_store_dwordx4 v[32:35], v18, s[16:19], s61 offen sc1\n"
    "v_fma_f32 v24, v117, v72, v84\n"
    "v_mfma_f32_16x16x32_bf16 v[48:51], a[72:75], v[108:111], v[48:51]\n"
    "s_waitcnt lgkmcnt(2)\n"
    "v_fma_f32 v25, v117, v73, v85\n"
    "v_fma_f32 v26, v117, v74, v86\n"
    "v_mfma_f32_16x16x32_bf16 v[40:43], a[76:79], v[88:91], v[40:43]\n"
    "ds_read_b128 v[104:107], v5 offset:18432\n"
    "v_fma_f32 v27, v117, v75, v87\n"
    "v_fma_f32 v60, v60, v113, v24\n"
    "v_mfma_f32_16x16x32_bf16 v[44:47], a[80:83], v[88:91], v[44:47]\n"
    "buffer_load_dwordx4 v[188:191], v2, s[16:19], s54 offen offset:3072 sc1\n"
    "v_fma_f32 v61, v61, v113, v25\n"
    "v_mfma_f32_16x16x32_bf16 v[48:51], a[84:87], v[88:91], v[48:51]\n"
    "v_fma_f32 v62, v62, v113, v26\n"
    "v_fma_f32 v63, v63, v113, v27\n"
    "v_mfma_f32_16x16x32_bf16 v[40:43], a[88:91], v[92:95], v[40:43]\n"
    "ds_read_b128 v[108:111], v5 offset:19456\n"
    "v_exp_f32 v24, v60\n"
    "v_mfma_f32_16x16x32_bf16 v[44:47], a[92:95], v[92:95], v[44:47]\n"
    "v_exp_f32 v25, v61\n"
    "s_add_u32 s54, s54, 4096\n"
    "v_mfma_f32_16x16x32_bf16 v[48:51], a[96:99], v[92:95], v[48:51]\n"
    "s_waitcnt lgkmcnt(0)\n"
    "s_barrier\n"
    "v_mfma_f32_16x16x32_bf16 v[40:43], a[100:103], v[96:99], v[40:43]\n"
    "ds_read_b128 v[88:91], v5 offset:20480\n"
    "v_exp_f32 v26, v62\n"
    "v_mfma_f32_16x16x32_bf16 v[44:47], a[104:107], v[96:99], v[44:47]\n"
    "buffer_load_dwordx4 v[192:195], v2, s[16:19], s54 offen sc1\n"
    "v_mfma_f32_16x16x32_bf16 v[48:51], a[108:111], v[96:99], v[48:51]\n"
    "v_exp_f32 v27, v63\n"
    "v_fma_f32 v24, v24, s76, s76\n"
    "v_mfma_f32_16x16x32_bf16 v[40:43], a[112:115], v[100:103], v[40:43]\n"
    "ds_read_b128 v[92:95], v5 offset:21504\n"
    "v_fma_f32 v25, v25, s76, s76\n"
    "v_fma_f32 v26, v26, s76, s76\n"
    "v_mfma_f32_16x16x32_bf16 v[44:47], a[116:119], v[100:103], v[44:47]\n"
    "v_fma_f32 v27, v27, s76, s76\n"
    "v_rcp_f32 v24, v24\n"
    "v_mfma_f32_16x16x32_bf16 v[48:51], a[120:123], v[100:103], v[48:51]\n"
    "buffer_load_dwordx4 v[196:199], v2, s[16:19], s54 offen offset:1024 sc1\n"
    "v_mfma_f32_16x16x32_bf16 v[40:43], a[124:127], v[104:107], v[40:43]\n"
    "ds_read_b128 v[96:99], v5 offset:22528\n"
    "v_rcp_f32 v25, v25\n"
    "v_mfma_f32_16x16x32_bf16 v[44:47], a[128:131], v[104:107], v[44:47]\n"
    "v_rcp_f32 v26, v26\n"
    "v_mfma_f32_16x16x32_bf16 v[48:51], a[132:135], v[104:107], v[48:51]\n"
    "v_rcp_f32 v27, v27\n"
    "v_mul_f32 v60, v60, v24\n"
    "v_mfma_f32_16x16x32_bf16 v[40:43], a[136:139], v[108:111], v[40:43]\n"
    "ds_read_b128 v[100:103], v5 offset:23552\n"
    "v_mul_f32 v61, v61, v25\n"
    "v_mul_f32 v62, v62, v26\n"
    "v_mfma_f32_16x16x32_bf16 v[44:47], a[140:143], v[108:111], v[44:47]\n"
    "v_mul_f32 v63, v63, v27\n"
    "v_cvt_pk_bf16_f32 v36, v60, v61\n"
    "v_cvt_pk_bf16_f32 v37, v62, v63\n"
    "v_mfma_f32_16x16x32_bf16 v[48:51], a[144:147], v[108:111], v[48:51]\n"
    "s_waitcnt lgkmcnt(2)\n"
    "v_permlane32_swap_b32 v36, v38\n"
    "v_permlane32_swap_b32 v37, v39\n"
    "v_min_u32 v36, -2, v36\n"
    "buffer_store_dwordx4 v[36:39], v19, s[16:19], s61 offen sc1\n"
    "v_mfma_f32_16x16x32_bf16 v[52:55], a[164:167], v[88:91], 0\n"
    "ds_read_b128 v[104:107], v5 offset:24576\n"
    "buffer_load_dwordx4 v[200:203], v2, s[16:19], s54 offen offset:2048 sc1\n"
    "v_mfma_f32_16x16x32_bf16 v[56:59], a[168:171], v[88:91], 0\n"
    "buffer_load_dwordx4 v[204:207], v2, s[16:19], s54 offen offset:3072 sc1\n"
    "v_mfma_f32_16x16x32_bf16 v[60:63], a[172:175], v[88:91], 0\n"
    "buffer_load_dwordx4 a[164:167], v2, s[16:19], s72 offen sc1\n"
    "v_mfma_f32_16x16x32_bf16 v[52:55], a[176:179], v[92:95], v[52:55]\n"
    "ds_read_b128 v[108:111], v5 offset:25600\n"
    "v_fma_f32 v24, v118, v64, v76\n"
    "v_fma_f32 v25, v118, v65, v77\n"
    "v_mfma_f32_16x16x32_bf16 v[56:59], a[180:183], v[92:95], v[56:59]\n"
    "v_fma_f32 v26, v118, v66, v78\n"
    "v_fma_f32 v27, v118, v67, v79\n"
    "v_mfma_f32_16x16x32_bf16 v[60:63], a[184:187], v[92:95], v[60:63]\n"
    "s_waitcnt lgkmcnt(2)\n"
    "buffer_load_dwordx4 a[168:171], v2, s[16:19], s72 offen offset:1024 sc1\n"
    "v_mfma_f32_16x16x32_bf16 v[52:55], a[188:191], v[96:99], v[52:55]\n"
    "ds_read_b128 v[88:91], v5 offset:26624\n"
    "v_fma_f32 v40, v40, v114, v24\n"
    "v_fma_f32 v41, v41, v114, v25\n"
    "v_mfma_f32_16x16x32_bf16 v[56:59], a[192:195], v[96:99], v[56:59]\n"
    "v_fma_f32 v42, v42, v114, v26\n"
    "v_fma_f32 v43, v43, v114, v27\n"
    "v_mfma_f32_16x16x32_bf16 v[60:63], a[196:199], v[96:99], v[60:63]\n"
    "buffer_load_dwordx4 a[172:175], v2, s[16:19], s72 offen offset:2048 sc1\n"
    "v_mfma_f32_16x16x32_bf16 v[52:55], a[200:203], v[100:103], v[52:55]\n"
    "ds_read_b128 v[92:95], v5 offset:27648\n"
    "v_exp_f32 v24, v40\n"
    "v_mfma_f32_16x16x32_bf16 v[56:59], a[204:207], v[100:103], v[56:59]\n"
    "v_exp_f32 v25, v41\n"
    "v_mfma_f32_16x16x32_bf16 v[60:63], a[208:211], v[100:103], v[60:63]\n"
    "s_waitcnt lgkmcnt(2)\n"
    "v_exp_f32 v26, v42\n"
    "v_mfma_f32_16x16x32_bf16 v[52:55], a[212:215], v[104:107], v[52:55]\n"
    "ds_read_b128 v[96:99], v5 offset:28672\n"
    "buffer_load_dwordx4 a[176:179], v2, s[16:19], s72 offen offset:3072 sc1\n"
    "v_mfma_f32_16x16x32_bf16 v[56:59], a[216:219], v[104:107], v[56:59]\n"
    "v_exp_f32 v27, v43\n"
    "v_fma_f32 v24, v24, s76, s76\n"
    "v_mfma_f32_16x16x32_bf16 v[60:63], a[220:223], v[104:107], v[60:63]\n"
    "v_fma_f32 v25, v25, s76, s76\n"
    "v_fma_f32 v26, v26, s76, s76\n"
    "v_fma_f32 v27, v27, s76, s76\n"
    "v_mfma_f32_16x16x32_bf16 v[52:55], a[224:227], v[108:111], v[52:55]\n"
    "ds_read_b128 v[100:103], v5 offset:29696\n"
    "buffer_load_dwordx4 a[180:183], v10, s[16:19], s72 offen sc1\n"
    "v_mfma_f32_16x16x32_bf16 v[56:59], a[228:231], v[108:111], v[56:59]\n"
    "v_rcp_f32 v24, v24\n"
    "v_mfma_f32_16x16x32_bf16 v[60:63], a[232:235], v[108:111], v[60:63]\n"
    "s_waitcnt lgkmcnt(2)\n"
    "v_rcp_f32 v25, v25\n"
    "v_mfma_f32_16x16x32_bf16 v[52:55], a[236:239], v[88:91], v[52:55]\n"
    "ds_read_b128 v[104:107], v5 offset:30720\n"
    "v_rcp_f32 v26, v26\n"
    "v_mfma_f32_16x16x32_bf16 v[56:59], a[240:243], v[88:91], v[56:59]\n"
    "buffer_load_dwordx4 a[184:187], v10, s[16:19], s72 offen offset:1024 sc1\n"
    "v_mfma_f32_16x16x32_bf16 v[60:63], a[244:247], v[88:91], v[60:63]\n"
    "v_rcp_f32 v27, v27\n"
    "v_mul_f32 v40, v40, v24\n"
    "v_mfma_f32_16x16x32_bf16 v[52:55], a[248:251], v[92:95], v[52:55]\n"
    "ds_read_b128 v[108:111], v5 offset:31744\n"
    "v_mul_f32 v41, v41, v25\n"
    "v_mul_f32 v42, v42, v26\n"
    "v_mfma_f32_16x16x32_bf16 v[56:59], a[252:255], v[92:95], v[56:59]\n"
    "v_mul_f32 v43, v43, v27\n"
    "buffer_load_dwordx4 a[188:191], v10, s[16:19], s72 offen offset:2048 sc1\n"
    "v_mfma_f32_16x16x32_bf16 v[60:63], a[0:3], v[92:95], v[60:63]\n"
    "s_waitcnt lgkmcnt(2)\n"
    "v_cvt_pk_bf16_f32 v32, v40, v41\n"
    "v_cvt_pk_bf16_f32 v33, v42, v43\n"
    "v_mfma_f32_16x16x32_bf16 v[52:55], a[4:7], v[96:99], v[52:55]\n"
    "ds_read_b128 v[88:91], v5 offset:32768\n"
    "v_fma_f32 v28, v118, v68, v80\n"
    "v_fma_f32 v29, v118, v69, v81\n"
    "v_mfma_f32_16x16x32_bf16 v[56:59], a[8:11], v[96:99], v[56:59]\n"
    "v_fma_f32 v30, v118, v70, v82\n"
    "v_fma_f32 v31, v118, v71, v83\n"
    "v_fma_f32 v44, v44, v114, v28\n"
    "v_mfma_f32_16x16x32_bf16 v[60:63], a[12:15], v[96:99], v[60:63]\n"
    "buffer_load_dwordx4 a[192:195], v10, s[16:19], s72 offen offset:3072 sc1\n"
    "v_fma_f32 v45, v45, v114, v29\n"
    "v_mfma_f32_16x16x32_bf16 v[52:55], a[16:19], v[100:103], v[52:55]\n"
    "ds_read_b128 v[92:95], v5 offset:33792\n"
    "v_fma_f32 v46, v46, v114, v30\n"
    "v_fma_f32 v47, v47, v114, v31\n"
    "v_mfma_f32_16x16x32_bf16 v[56:59], a[20:23], v[100:103], v[56:59]\n"
    "v_exp_f32 v28, v44\n"
    "v_mfma_f32_16x16x32_bf16 v[60:63], a[24:27], v[100:103], v[60:63]\n"
    "s_waitcnt lgkmcnt(2)\n"
    "buffer_load_dwordx4 a[196:199], v9, s[16:19], s72 offen sc1\n"
    "v_mfma_f32_16x16x32_bf16 v[52:55], a[28:31], v[104:107], v[52:55]\n"
    "ds_read_b128 v[96:99], v5 offset:34816\n"
    "v_exp_f32 v29, v45\n"
    "v_mfma_f32_16x16x32_bf16 v[56:59], a[32:35], v[104:107], v[56:59]\n"
    "v_exp_f32 v30, v46\n"
    "v_mfma_f32_16x16x32_bf16 v[60:63], a[36:39], v[104:107], v[60:63]\n"
    "v_exp_f32 v31, v47\n"
    "v_fma_f32 v28, v28, s76, s76\n"
    "v_mfma_f32_16x16x32_bf16 v[52:55], a[40:43], v[108:111], v[52:55]\n"
    "ds_read_b128 v[100:103], v5 offset:35840\n"
    "buffer_load_dwordx4 a[200:203], v9, s[16:19], s72 offen offset:1024 sc1\n"
    "v_mfma_f32_16x16x32_bf16 v[56:59], a[44:47], v[108:111], v[56:59]\n"
    "v_fma_f32 v29, v29, s76, s76\n"
    "v_fma_f32 v30, v30, s76, s76\n"
    "v_fma_f32 v31, v31, s76, s76\n"
    "v_mfma_f32_16x16x32_bf16 v[60:63], a[48:51], v[108:111], v[60:63]\n"
    "s_waitcnt lgkmcnt(2)\n"
    "v_rcp_f32 v28, v28\n"
    "v_mfma_f32_16x16x32_bf16 v[52:55], a[52:55], v[88:91], v[52:55]\n"
    "ds_read_b128 v[104:107], v5 offset:36864\n"
    "buffer_load_dwordx4 a[204:207], v9, s[16:19], s72 offen offset:2048 sc1\n"
    "v_mfma_f32_16x16x32_bf16 v[56:59], a[56:59], v[88:91], v[56:59]\n"
    "v_rcp_f32 v29, v29\n"
    "v_mfma_f32_16x16x32_bf16 v[60:63], a[60:63], v[88:91], v[60:63]\n"
    "v_rcp_f32 v30, v30\n"
    "v_mfma_f32_16x16x32_bf16 v[52:55], a[64:67], v[92:95], v[52:55]\n"
    "ds_read_b128 v[108:111], v5 offset:37888\n"
    "v_rcp_f32 v31, v31\n"
    "v_mfma_f32_16x16x32_bf16 v[56:59], a[68:71], v[92:95], v[56:59]\n"
    "buffer_load_dwordx4 a[208:211], v9, s[16:19], s72 offen offset:3072 sc1\n"
    "v_mul_f32 v44, v44, v28\n"
    "v_mfma_f32_16x16x32_bf16 v[60:63], a[72:75], v[92:95], v[60:63]\n"
    "s_waitcnt lgkmcnt(2)\n"
    "v_mul_f32 v45, v45, v29\n"
    "v_mul_f32 v46, v46, v30\n"
    "v_mfma_f32_16x16x32_bf16 v[52:55], a[76:79], v[96:99], v[52:55]\n"
    "ds_read_b128 v[88:91], v5 offset:38912\n"
    "v_mul_f32 v47, v47, v31\n"
    "v_cvt_pk_bf16_f32 v34, v44, v45\n"
    "v_mfma_f32_16x16x32_bf16 v[56:59], a[80:83], v[96:99], v[56:59]\n"
    "v_cvt_pk_bf16_f32 v35, v46, v47\n"
    "s_add_u32 s54, s54, 4096\n"
    "v_min_u32 v32, -2, v32\n"
    "v_mfma_f32_16x16x32_bf16 v[60:63], a[84:87], v[96:99], v[60:63]\n"
    "buffer_load_dwordx4 a[212:215], v2, s[16:19], s54 offen sc1\n"
    "v_mfma_f32_16x16x32_bf16 v[52:55], a[88:91], v[100:103], v[52:55]\n"
    "ds_read_b128 v[92:95], v5 offset:39936\n"
    "buffer_store_dwordx4 v[32:35], v18, s[16:19], s62 offen sc1\n"
    "v_mfma_f32_16x16x32_bf16 v[56:59], a[92:95], v[100:103], v[56:59]\n"
    "v_fma_f32 v24, v118, v72, v84\n"
    "v_fma_f32 v25, v118, v73, v85\n"
    "v_fma_f32 v26, v118, v74, v86\n"
    "v_mfma_f32_16x16x32_bf16 v[60:63], a[96:99], v[100:103], v[60:63]\n"
    "s_waitcnt lgkmcnt(2)\n"
    "buffer_load_dwordx4 a[216:219], v2, s[16:19], s54 offen offset:1024 sc1\n"
    "v_mfma_f32_16x16x32_bf16 v[52:55], a[100:103], v[104:107], v[52:55]\n"
    "v_fma_f32 v27, v118, v75, v87\n"
    "v_fma_f32 v48, v48, v114, v24\n"
    "v_fma_f32 v49, v49, v114, v25\n"
    "v_mfma_f32_16x16x32_bf16 v[56:59], a[104:107], v[104:107], v[56:59]\n"
    "v_fma_f32 v50, v50, v114, v26\n"
    "v_fma_f32 v51, v51, v114, v27\n"
    "v_mfma_f32_16x16x32_bf16 v[60:63], a[108:111], v[104:107], v[60:63]\n"
    "v_exp_f32 v24, v48\n"
    "buffer_load_dwordx4 a[220:223], v2, s[16:19], s54 offen offset:2048 sc1\n"
    "v_mfma_f32_16x16x32_bf16 v[52:55], a[112:115], v[108:111], v[52:55]\n"
    "v_exp_f32 v25, v49\n"
    "v_mfma_f32_16x16x32_bf16 v[56:59], a[116:119], v[108:111], v[56:59]\n"
    "v_exp_f32 v26, v50\n"
    "v_mfma_f32_16x16x32_bf16 v[60:63], a[120:123], v[108:111], v[60:63]\n"
    "s_waitcnt lgkmcnt(0)\n"
    "v_exp_f32 v27, v51\n"
    "buffer_load_dwordx4 a[224:227], v2, s[16:19], s54 offen offset:3072 sc1\n"
    "v_mfma_f32_16x16x32_bf16 v[52:55], a[124:127], v[88:91], v[52:55]\n"
    "v_fma_f32 v24, v24, s76, s76\n"
    "v_fma_f32 v25, v25, s76, s76\n"
    "v_fma_f32 v26, v26, s76, s76\n"
    "v_mfma_f32_16x16x32_bf16 v[56:59], a[128:131], v[88:91], v[56:59]\n"
    "v_fma_f32 v27, v27, s76, s76\n"
    "v_rcp_f32 v24, v24\n"
    "v_mfma_f32_16x16x32_bf16 v[60:63], a[132:135], v[88:91], v[60:63]\n"
    "s_add_u32 s54, s54, 4096\n"
    "buffer_load_dwordx4 a[228:231], v2, s[16:19], s54 offen sc1\n"
    "v_mfma_f32_16x16x32_bf16 v[52:55], a[136:139], v[92:95], v[52:55]\n"
    "v_rcp_f32 v25, v25\n"
    "v_mfma_f32_16x16x32_bf16 v[56:59], a[140:143], v[92:95], v[56:59]\n"
    "v_rcp_f32 v26, v26\n"
    "v_mfma_f32_16x16x32_bf16 v[60:63], a[144:147], v[92:95], v[60:63]\n"
    "v_rcp_f32 v27, v27\n"
    "v_mul_f32 v48, v48, v24\n"
    "v_mul_f32 v49, v49, v25\n"
    "v_mul_f32 v50, v50, v26\n"
    "v_mul_f32 v51, v51, v27\n"
    "v_cvt_pk_bf16_f32 v36, v48, v49\n"
    "v_cvt_pk_bf16_f32 v37, v50, v51\n"
    "s_nop 0\n"
    "v_permlane32_swap_b32 v36, v38\n"
    "v_permlane32_swap_b32 v37, v39\n"
    "v_min_u32 v36, -2, v36\n"
    "buffer_store_dwordx4 v[36:39], v19, s[16:19], s62 offen sc1\n"
    "buffer_load_dwordx4 a[232:235], v2, s[16:19], s54 offen offset:1024 sc1\n"
    "buffer_load_dwordx4 a[236:239], v2, s[16:19], s54 offen offset:2048 sc1\n"
    "buffer_load_dwordx4 a[240:243], v2, s[16:19], s54 offen offset:3072 sc1\n"
    "v_fma_f32 v24, v119, v64, v76\n"
    "v_fma_f32 v25, v119, v65, v77\n"
    "v_fma_f32 v26, v119, v66, v78\n"
    "s_add_u32 s54, s54, 4096\n"
    "buffer_load_dwordx4 a[244:247], v2, s[16:19], s54 offen sc1\n"
    "v_fma_f32 v27, v119, v67, v79\n"
    "v_fma_f32 v52, v52, v115, v24\n"
    "v_fma_f32 v53, v53, v115, v25\n"
    "v_fma_f32 v54, v54, v115, v26\n"
    "v_fma_f32 v55, v55, v115, v27\n"
    "v_exp_f32 v24, v52\n"
    "buffer_load_dwordx4 a[248:251], v2, s[16:19], s54 offen offset:1024 sc1\n"
    "v_exp_f32 v25, v53\n"
    "v_exp_f32 v26, v54\n"
    "v_exp_f32 v27, v55\n"
    "v_fma_f32 v24, v24, s76, s76\n"
    "v_fma_f32 v25, v25, s76, s76\n"
    "v_fma_f32 v26, v26, s76, s76\n"
    "buffer_load_dwordx4 a[252:255], v2, s[16:19], s54 offen offset:2048 sc1\n"
    "v_fma_f32 v27, v27, s76, s76\n"
    "v_rcp_f32 v24, v24\n"
    "v_rcp_f32 v25, v25\n"
    "v_rcp_f32 v26, v26\n"
    "v_rcp_f32 v27, v27\n"
    "v_mul_f32 v52, v52, v24\n"
    "buffer_load_dwordx4 a[0:3], v2, s[16:19], s54 offen offset:3072 sc1\n"
    "v_mul_f32 v53, v53, v25\n"
    "v_mul_f32 v54, v54, v26\n"
    "v_mul_f32 v55, v55, v27\n"
    "v_cvt_pk_bf16_f32 v32, v52, v53\n"
    "v_cvt_pk_bf16_f32 v33, v54, v55\n"
    "v_fma_f32 v28, v119, v68, v80\n"
    "s_add_u32 s54, s54, 4096\n"
    "buffer_load_dwordx4 a[4:7], v2, s[16:19], s54 offen sc1\n"
    "v_fma_f32 v29, v119, v69, v81\n"
    "v_fma_f32 v30, v119, v70, v82\n"
    "v_fma_f32 v31, v119, v71, v83\n"
    "v_fma_f32 v56, v56, v115, v28\n"
    "v_fma_f32 v57, v57, v115, v29\n"
    "v_fma_f32 v58, v58, v115, v30\n"
    "buffer_load_dwordx4 a[8:11], v2, s[16:19], s54 offen offset:1024 sc1\n"
    "v_fma_f32 v59, v59, v115, v31\n"
    "v_exp_f32 v28, v56\n"
    "v_exp_f32 v29, v57\n"
    "v_exp_f32 v30, v58\n"
    "v_exp_f32 v31, v59\n"
    "v_fma_f32 v28, v28, s76, s76\n"
    "buffer_load_dwordx4 a[12:15], v2, s[16:19], s54 offen offset:2048 sc1\n"
    "v_fma_f32 v29, v29, s76, s76\n"
    "v_fma_f32 v30, v30, s76, s76\n"
    "v_fma_f32 v31, v31, s76, s76\n"
    "v_rcp_f32 v28, v28\n"
    "v_rcp_f32 v29, v29\n"
    "v_rcp_f32 v30, v30\n"
    "buffer_load_dwordx4 a[16:19], v2, s[16:19], s54 offen offset:3072 sc1\n"
    "v_rcp_f32 v31, v31\n"
    "v_mul_f32 v56, v56, v28\n"
    "v_mul_f32 v57, v57, v29\n"
    "v_mul_f32 v58, v58, v30\n"
    "v_mul_f32 v59, v59, v31\n"
    "v_cvt_pk_bf16_f32 v34, v56, v57\n"
    "s_add_u32 s54, s54, 4096\n"
    "buffer_load_dwordx4 a[20:23], v2, s[16:19], s54 offen sc1\n"
    "v_cvt_pk_bf16_f32 v35, v58, v59\n"
    "v_min_u32 v32, -2, v32\n"
    "buffer_store_dwordx4 v[32:35], v18, s[16:19], s63 offen sc1\n"
    "v_fma_f32 v24, v119, v72, v84\n"
    "v_fma_f32 v25, v119, v73, v85\n"
    "v_fma_f32 v26, v119, v74, v86\n"
    "buffer_load_dwordx4 a[24:27], v2, s[16:19], s54 offen offset:1024 sc1\n"
    "v_fma_f32 v27, v119, v75, v87\n"
    "v_fma_f32 v60, v60, v115, v24\n"
    "v_fma_f32 v61, v61, v115, v25\n"
    "v_fma_f32 v62, v62, v115, v26\n"
    "v_fma_f32 v63, v63, v115, v27\n"
    "v_exp_f32 v24, v60\n"
    "buffer_load_dwordx4 a[28:31], v2, s[16:19], s54 offen offset:2048 sc1\n"
    "v_exp_f32 v25, v61\n"
    "v_exp_f32 v26, v62\n"
    "v_exp_f32 v27, v63\n"
    "v_fma_f32 v24, v24, s76, s76\n"
    "v_fma_f32 v25, v25, s76, s76\n"
    "v_fma_f32 v26, v26, s76, s76\n"
    "buffer_load_dwordx4 a[32:35], v2, s[16:19], s54 offen offset:3072 sc1\n"
    "v_fma_f32 v27, v27, s76, s76\n"
    "v_rcp_f32 v24, v24\n"
    "v_rcp_f32 v25, v25\n"
    "v_rcp_f32 v26, v26\n"
    "v_rcp_f32 v27, v27\n"
    "v_mul_f32 v60, v60, v24\n"
    "s_add_u32 s54, s54, 4096\n"
    "buffer_load_dwordx4 a[36:39], v2, s[16:19], s54 offen sc1\n"
    "v_mul_f32 v61, v61, v25\n"
    "v_mul_f32 v62, v62, v26\n"
    "v_mul_f32 v63, v63, v27\n"
    "v_cvt_pk_bf16_f32 v36, v60, v61\n"
    "v_cvt_pk_bf16_f32 v37, v62, v63\n"
    "s_nop 0\n"
    "v_permlane32_swap_b32 v36, v38\n"
    "buffer_load_dwordx4 a[40:43], v2, s[16:19], s54 offen offset:1024 sc1\n"
    "v_permlane32_swap_b32 v37, v39\n"
    "v_min_u32 v36, -2, v36\n"
    "buffer_store_dwordx4 v[36:39], v19, s[16:19], s63 offen sc1\n"
    "s_waitcnt vmcnt(37)\n"
    "v_accvgpr_read_b32 v24, a164\n"
    "s_waitcnt vmcnt(36)\n"
    "v_accvgpr_read_b32 v25, a168\n"
    "s_waitcnt vmcnt(35)\n"
    "v_accvgpr_read_b32 v26, a172\n"
    "v_max3_u32 v24, v24, v25, v26\n"
    "s_waitcnt vmcnt(34)\n"
    "v_accvgpr_read_b32 v25, a176\n"
    "s_waitcnt vmcnt(33)\n"
    "v_accvgpr_read_b32 v26, a180\n"
    "v_max3_u32 v24, v24, v25, v26\n"
    "s_waitcnt vmcnt(32)\n"
    "v_accvgpr_read_b32 v25, a184\n"
    "s_waitcnt vmcnt(31)\n"
    "v_accvgpr_read_b32 v26, a188\n"
    "v_max3_u32 v24, v24, v25, v26\n"
    "s_waitcnt vmcnt(30)\n"
    "v_accvgpr_read_b32 v25, a192\n"
    "s_waitcnt vmcnt(29)\n"
    "v_accvgpr_read_b32 v26, a196\n"
    "v_max3_u32 v24, v24, v25, v26\n"
    "s_waitcnt vmcnt(28)\n"
    "v_accvgpr_read_b32 v25, a200\n"
    "s_waitcnt vmcnt(27)\n"
    "v_accvgpr_read_b32 v26, a204\n"
    "v_max3_u32 v24, v24, v25, v26\n"
    "s_waitcnt vmcnt(26)\n"
    "v_accvgpr_read_b32 v25, a208\n"
    "v_max_u32 v24, v24, v25\n"
    "v_cmp_eq_u32 vcc, -1, v24\n"
    "s_nop 1\n"
    "s_cbranch_vccnz .Lr15_%=\n"
    ".Ld15_%=:\n"
    "v_mfma_f32_16x16x32_bf16 v[40:43], v[208:211], a[164:167], 0\n"
    "v_mfma_f32_16x16x32_bf16 v[44:47], v[212:215], a[164:167], 0\n"
    "v_mfma_f32_16x16x32_bf16 v[48:51], v[216:219], a[164:167], 0\n"
    "buffer_load_dwordx4 a[44:47], v2, s[16:19], s54 offen offset:2048 sc1\n"
    "v_mfma_f32_16x16x32_bf16 v[52:55], v[220:223], a[164:167], 0\n"
    "v_mfma_f32_16x16x32_bf16 v[56:59], v[224:227], a[164:167], 0\n"
    "v_mfma_f32_16x16x32_bf16 v[40:43], v[228:231], a[168:171], v[40:43]\n"
    "buffer_load_dwordx4 a[48:51], v2, s[16:19], s54 offen offset:3072 sc1\n"
    "v_mfma_f32_16x16x32_bf16 v[44:47], v[232:235], a[168:171], v[44:47]\n"
    "s_add_u32 s54, s54, 4096\n"
    "v_mfma_f32_16x16x32_bf16 v[48:51], v[236:239], a[168:171], v[48:51]\n"
    "buffer_load_dwordx4 a[52:55], v2, s[16:19], s54 offen sc1\n"
    "v_mfma_f32_16x16x32_bf16 v[52:55], a[148:151], a[168:171], v[52:55]\n"
    "buffer_load_dwordx4 a[56:59], v2, s[16:19], s54 offen offset:1024 sc1\n"
    "v_mfma_f32_16x16x32_bf16 v[56:59], a[152:155], a[168:171], v[56:59]\n"
    "v_mfma_f32_16x16x32_bf16 v[40:43], a[156:159], a[172:175], v[40:43]\n"
    "buffer_load_dwordx4 a[60:63], v2, s[16:19], s54 offen offset:2048 sc1\n"
    "v_mfma_f32_16x16x32_bf16 v[44:47], a[160:163], a[172:175], v[44:47]\n"
    "v_mfma_f32_16x16x32_bf16 v[48:51], v[128:131], a[172:175], v[48:51]\n"
    "buffer_load_dwordx4 a[64:67], v2, s[16:19], s54 offen offset:3072 sc1\n"
    "v_mfma_f32_16x16x32_bf16 v[52:55], v[132:135], a[172:175], v[52:55]\n"
    "v_mfma_f32_16x16x32_bf16 v[56:59], v[136:139], a[172:175], v[56:59]\n"
    "v_mfma_f32_16x16x32_bf16 v[40:43], v[140:143], a[176:179], v[40:43]\n"
    "buffer_load_dwordx4 a[68:71], v2, s[16:19], s73 offen sc1\n"
    "v_mfma_f32_16x16x32_bf16 v[44:47], v[144:147], a[176:179], v[44:47]\n"
    "buffer_load_dwordx4 a[72:75], v2, s[16:19], s73 offen offset:1024 sc1\n"
    "v_mfma_f32_16x16x32_bf16 v[48:51], v[148:151], a[176:179], v[48:51]\n"
    "v_mfma_f32_16x16x32_bf16 v[52:55], v[152:155], a[176:179], v[52:55]\n"
    "buffer_load_dwordx4 a[76:79], v2, s[16:19], s73 offen offset:2048 sc1\n"
    "v_mfma_f32_16x16x32_bf16 v[56:59], v[156:159], a[176:179], v[56:59]\n"
    "v_mfma_f32_16x16x32_bf16 v[40:43], v[160:163], a[180:183], v[40:43]\n"
    "buffer_load_dwordx4 a[80:83], v2, s[16:19], s73 offen offset:3072 sc1\n"
    "v_mfma_f32_16x16x32_bf16 v[44:47], v[164:167], a[180:183], v[44:47]\n"
    "v_mfma_f32_16x16x32_bf16 v[48:51], v[168:171], a[180:183], v[48:51]\n"
    "buffer_load_dwordx4 a[84:87], v10, s[16:19], s73 offen sc1\n"
    "v_mfma_f32_16x16x32_bf16 v[52:55], v[172:175], a[180:183], v[52:55]\n"
    "v_mfma_f32_16x16x32_bf16 v[56:59], v[176:179], a[180:183], v[56:59]\n"
    "v_mfma_f32_16x16x32_bf16 v[40:43], v[180:183], a[184:187], v[40:43]\n"
    "buffer_load_dwordx4 a[88:91], v10, s[16:19], s73 offen offset:1024 sc1\n"
    "v_mfma_f32_16x16x32_bf16 v[44:47], v[184:187], a[184:187], v[44:47]\n"
    "buffer_load_dwordx4 a[92:95], v10, s[16:19], s73 offen offset:2048 sc1\n"
    "v_mfma_f32_16x16x32_bf16 v[48:51], v[188:191], a[184:187], v[48:51]\n"
    "v_mfma_f32_16x16x32_bf16 v[52:55], v[192:195], a[184:187], v[52:55]\n"
    "buffer_load_dwordx4 a[96:99], v10, s[16:19], s73 offen offset:3072 sc1\n"
    "v_mfma_f32_16x16x32_bf16 v[56:59], v[196:199], a[184:187], v[56:59]\n"
    "s_waitcnt vmcnt(36)\n"
    "v_mfma_f32_16x16x32_bf16 v[40:43], v[200:203], a[188:191], v[40:43]\n"
    "buffer_load_dwordx4 a[100:103], v9, s[16:19], s73 offen sc1\n"
    "v_mfma_f32_16x16x32_bf16 v[44:47], v[204:207], a[188:191], v[44:47]\n"
    "v_mfma_f32_16x16x32_bf16 v[48:51], a[212:215], a[188:191], v[48:51]\n"
    "buffer_load_dwordx4 a[104:107], v9, s[16:19], s73 offen offset:1024 sc1\n"
    "v_mfma_f32_16x16x32_bf16 v[52:55], a[216:219], a[188:191], v[52:55]\n"
    "v_mfma_f32_16x16x32_bf16 v[56:59], a[220:223], a[188:191], v[56:59]\n"
    "s_waitcnt vmcnt(32)\n"
    "v_mfma_f32_16x16x32_bf16 v[40:43], a[224:227], a[192:195], v[40:43]\n"
    "buffer_load_dwordx4 a[108:111], v9, s[16:19], s73 offen offset:2048 sc1\n"
    "v_mfma_f32_16x16x32_bf16 v[44:47], a[228:231], a[192:195], v[44:47]\n"
    "buffer_load_dwordx4 a[112:115], v9, s[16:19], s73 offen offset:3072 sc1\n"
    "v_mfma_f32_16x16x32_bf16 v[48:51], a[232:235], a[192:195], v[48:51]\n"
    "v_mfma_f32_16x16x32_bf16 v[52:55], a[236:239], a[192:195], v[52:55]\n"
    "buffer_load_dwordx4 a[116:119], v2, s[16:19], s74 offen sc1\n"
    "v_mfma_f32_16x16x32_bf16 v[56:59], a[240:243], a[192:195], v[56:59]\n"
    "s_waitcnt vmcnt(30)\n"
    "v_mfma_f32_16x16x32_bf16 v[40:43], a[244:247], a[196:199], v[40:43]\n"
    "buffer_load_dwordx4 a[120:123], v2, s[16:19], s74 offen offset:1024 sc1\n"
    "v_mfma_f32_16x16x32_bf16 v[44:47], a[248:251], a[196:199], v[44:47]\n"
    "v_mfma_f32_16x16x32_bf16 v[48:51], a[252:255], a[196:199], v[48:51]\n"
    "buffer_load_dwordx4 a[124:127], v2, s[16:19], s74 offen offset:2048 sc1\n"
    "v_mfma_f32_16x16x32_bf16 v[52:55], a[0:3], a[196:199], v[52:55]\n"
    "v_mfma_f32_16x16x32_bf16 v[56:59], a[4:7], a[196:199], v[56:59]\n"
    "s_waitcnt vmcnt(26)\n"
    "v_mfma_f32_16x16x32_bf16 v[40:43], a[8:11], a[200:203], v[40:43]\n"
    "buffer_load_dwordx4 a[128:131], v2, s[16:19], s74 offen offset:3072 sc1\n"
    "v_mfma_f32_16x16x32_bf16 v[44:47], a[12:15], a[200:203], v[44:47]\n"
    "buffer_load_dwordx4 a[132:135], v10, s[16:19], s74 offen sc1\n"
    "v_mfma_f32_16x16x32_bf16 v[48:51], a[16:19], a[200:203], v[48:51]\n"
    "s_waitcnt vmcnt(5)\n"
    "v_mfma_f32_16x16x32_bf16 v[52:55], a[20:23], a[200:203], v[52:55]\n"
    "buffer_load_dwordx4 a[136:139], v10, s[16:19], s74 offen offset:1024 sc1\n"
    "v_mfma_f32_16x16x32_bf16 v[56:59], a[24:27], a[200:203], v[56:59]\n"
    "v_accvgpr_read_b32 v24, a68\n"
    "v_mfma_f32_16x16x32_bf16 v[40:43], a[28:31], a[204:207], v[40:43]\n"
    "buffer_load_dwordx4 a[140:143], v10, s[16:19], s74 offen offset:2048 sc1\n"
    "v_mfma_f32_16x16x32_bf16 v[44:47], a[32:35], a[204:207], v[44:47]\n"
    "v_accvgpr_read_b32 v25, a72\n"
    "v_accvgpr_read_b32 v26, a76\n"
    "v_mfma_f32_16x16x32_bf16 v[48:51], a[36:39], a[204:207], v[48:51]\n"
    "v_max3_u32 v24, v24, v25, v26\n"
    "v_accvgpr_read_b32 v25, a80\n"
    "v_mfma_f32_16x16x32_bf16 v[52:55], a[40:43], a[204:207], v[52:55]\n"
    "buffer_load_dwordx4 a[144:147], v10, s[16:19], s74 offen offset:3072 sc1\n"
    "v_mfma_f32_16x16x32_bf16 v[56:59], a[44:47], a[204:207], v[56:59]\n"
    "v_accvgpr_read_b32 v26, a84\n"
    "v_mfma_f32_16x16x32_bf16 v[40:43], a[48:51], a[208:211], v[40:43]\n"
    "v_max3_u32 v24, v24, v25, v26\n"
    "v_accvgpr_read_b32 v25, a88\n"
    "v_mfma_f32_16x16x32_bf16 v[44:47], a[52:55], a[208:211], v[44:47]\n"
    "v_accvgpr_read_b32 v26, a92\n"
    "v_max3_u32 v24, v24, v25, v26\n"
    "v_mfma_f32_16x16x32_bf16 v[48:51], a[56:59], a[208:211], v[48:51]\n"
    "v_accvgpr_read_b32 v25, a96\n"
    "v_accvgpr_read_b32 v26, a100\n"
    "v_mfma_f32_16x16x32_bf16 v[52:55], a[60:63], a[208:211], v[52:55]\n"
    "v_max3_u32 v24, v24, v25, v26\n"
    "v_accvgpr_read_b32 v25, a104\n"
    "v_accvgpr_read_b32 v26, a108\n"
    "v_max3_u32 v24, v24, v25, v26\n"
    "v_accvgpr_read_b32 v25, a112\n"
    "v_max_u32 v24, v24, v25\n"
    "v_mfma_f32_16x16x32_bf16 v[56:59], a[64:67], a[208:211], v[56:59]\n"
    "v_cmp_eq_u32 vcc, -1, v24\n"
    "s_nop 1\n"
    "s_cbranch_vccnz .Lr12_%=\n"
    ".Ld12_%=:\n"
    "v_mfma_f32_16x16x32_bf16 v[60:63], v[208:211], a[68:71], 0\n"
    "buffer_load_dwordx4 a[164:167], v9, s[16:19], s74 offen sc1\n"
    "v_mfma_f32_16x16x32_bf16 v[64:67], v[212:215], a[68:71], 0\n"
    "buffer_load_dwordx4 a[168:171], v9, s[16:19], s74 offen offset:1024 sc1\n"
    "v_mfma_f32_16x16x32_bf16 v[68:71], v[216:219], a[68:71], 0\n"
    "buffer_load_dwordx4 a[172:175], v9, s[16:19], s74 offen offset:2048 sc1\n"
    "v_mfma_f32_16x16x32_bf16 v[72:75], v[220:223], a[68:71], 0\n"
    "buffer_load_dwordx4 a[176:179], v9, s[16:19], s74 offen offset:3072 sc1\n"
    "v_mfma_f32_16x16x32_bf16 v[76:79], v[224:227], a[68:71], 0\n"
    "ds_write_b128 v20, v[40:43]\n"
    "v_mfma_f32_16x16x32_bf16 v[60:63], v[228:231], a[72:75], v[60:63]\n"
    "v_mfma_f32_16x16x32_bf16 v[64:67], v[232:235], a[72:75], v[64:67]\n"
    "v_mfma_f32_16x16x32_bf16 v[68:71], v[236:239], a[72:75], v[68:71]\n"
    "v_mfma_f32_16x16x32_bf16 v[72:75], a[148:151], a[72:75], v[72:75]\n"
    "v_mfma_f32_16x16x32_bf16 v[76:79], a[152:155], a[72:75], v[76:79]\n"
    "v_mfma_f32_16x16x32_bf16 v[60:63], a[156:159], a[76:79], v[60:63]\n"
    "v_mfma_f32_16x16x32_bf16 v[64:67], a[160:163], a[76:79], v[64:67]\n"
    "v_mfma_f32_16x16x32_bf16 v[68:71], v[128:131], a[76:79], v[68:71]\n"
    "v_mfma_f32_16x16x32_bf16 v[72:75], v[132:135], a[76:79], v[72:75]\n"
    "v_mfma_f32_16x16x32_bf16 v[76:79], v[136:139], a[76:79], v[76:79]\n"
    "ds_write_b128 v20, v[44:47] offset:1024\n"
    "v_mfma_f32_16x16x32_bf16 v[60:63], v[140:143], a[80:83], v[60:63]\n"
    "v_mfma_f32_16x16x32_bf16 v[64:67], v[144:147], a[80:83], v[64:67]\n"
    "v_mfma_f32_16x16x32_bf16 v[68:71], v[148:151], a[80:83], v[68:71]\n"
    "v_mfma_f32_16x16x32_bf16 v[72:75], v[152:155], a[80:83], v[72:75]\n"
    "v_mfma_f32_16x16x32_bf16 v[76:79], v[156:159], a[80:83], v[76:79]\n"
    "v_mfma_f32_16x16x32_bf16 v[60:63], v[160:163], a[84:87], v[60:63]\n"
    "buffer_load_dwordx4 a[180:183], v2, s[16:19], s75 offen sc1\n"
    "v_mfma_f32_16x16x32_bf16 v[64:67], v[164:167], a[84:87], v[64:67]\n"
    "v_mfma_f32_16x16x32_bf16 v[68:71], v[168:171], a[84:87], v[68:71]\n"
    "buffer_load_dwordx4 a[184:187], v2, s[16:19], s75 offen offset:1024 sc1\n"
    "v_mfma_f32_16x16x32_bf16 v[72:75], v[172:175], a[84:87], v[72:75]\n"
    "v_mfma_f32_16x16x32_bf16 v[76:79], v[176:179], a[84:87], v[76:79]\n"
    "ds_write_b128 v20, v[48:51] offset:2048\n"
    "v_mfma_f32_16x16x32_bf16 v[60:63], v[180:183], a[88:91], v[60:63]\n"
    "buffer_load_dwordx4 a[188:191], v2, s[16:19], s75 offen offset:2048 sc1\n"
    "v_mfma_f32_16x16x32_bf16 v[64:67], v[184:187], a[88:91], v[64:67]\n"
    "buffer_load_dwordx4 a[192:195], v2, s[16:19], s75 offen offset:3072 sc1\n"
    "v_mfma_f32_16x16x32_bf16 v[68:71], v[188:191], a[88:91], v[68:71]\n"
    "v_mfma_f32_16x16x32_bf16 v[72:75], v[192:195], a[88:91], v[72:75]\n"
    "buffer_load_dwordx4 a[196:199], v10, s[16:19], s75 offen sc1\n"
    "v_mfma_f32_16x16x32_bf16 v[76:79], v[196:199], a[88:91], v[76:79]\n"
    "v_mfma_f32_16x16x32_bf16 v[60:63], v[200:203], a[92:95], v[60:63]\n"
    "buffer_load_dwordx4 a[200:203], v10, s[16:19], s75 offen offset:1024 sc1\n"
    "v_mfma_f32_16x16x32_bf16 v[64:67], v[204:207], a[92:95], v[64:67]\n"
    "v_mfma_f32_16x16x32_bf16 v[68:71], a[212:215], a[92:95], v[68:71]\n"
    "buffer_load_dwordx4 a[204:207], v10, s[16:19], s75 offen offset:2048 sc1\n"
    "v_mfma_f32_16x16x32_bf16 v[72:75], a[216:219], a[92:95], v[72:75]\n"
    "v_mfma_f32_16x16x32_bf16 v[76:79], a[220:223], a[92:95], v[76:79]\n"
    "buffer_load_dwordx4 a[208:211], v10, s[16:19], s75 offen offset:3072 sc1\n"
    "v_mfma_f32_16x16x32_bf16 v[60:63], a[224:227], a[96:99], v[60:63]\n"
    "ds_write_b128 v20, v[52:55] offset:3072\n"
    "v_mfma_f32_16x16x32_bf16 v[64:67], a[228:231], a[96:99], v[64:67]\n"
    "buffer_load_dwordx4 a[68:71], v9, s[16:19], s75 offen sc1\n"
    "v_mfma_f32_16x16x32_bf16 v[68:71], a[232:235], a[96:99], v[68:71]\n"
    "v_mfma_f32_16x16x32_bf16 v[72:75], a[236:239], a[96:99], v[72:75]\n"
    "buffer_load_dwordx4 a[72:75], v9, s[16:19], s75 offen offset:1024 sc1\n"
    "v_mfma_f32_16x16x32_bf16 v[76:79], a[240:243], a[96:99], v[76:79]\n"
    "v_mfma_f32_16x16x32_bf16 v[60:63], a[244:247], a[100:103], v[60:63]\n"
    "buffer_load_dwordx4 a[76:79], v9, s[16:19], s75 offen offset:2048 sc1\n"
    "v_mfma_f32_16x16x32_bf16 v[64:67], a[248:251], a[100:103], v[64:67]\n"
    "v_mfma_f32_16x16x32_bf16 v[68:71], a[252:255], a[100:103], v[68:71]\n"
    "buffer_load_dwordx4 a[80:83], v9, s[16:19], s75 offen offset:3072 sc1\n"
    "v_mfma_f32_16x16x32_bf16 v[72:75], a[0:3], a[100:103], v[72:75]\n"
    "v_mfma_f32_16x16x32_bf16 v[76:79], a[4:7], a[100:103], v[76:79]\n"
    "v_mfma_f32_16x16x32_bf16 v[60:63], a[8:11], a[104:107], v[60:63]\n"
    "ds_write_b128 v20, v[56:59] offset:4096\n"
    "v_mfma_f32_16x16x32_bf16 v[64:67], a[12:15], a[104:107], v[64:67]\n"
    "v_mfma_f32_16x16x32_bf16 v[68:71], a[16:19], a[104:107], v[68:71]\n"
    "s_waitcnt vmcnt(12)\n"
    "v_mfma_f32_16x16x32_bf16 v[72:75], a[20:23], a[104:107], v[72:75]\n"
    "v_accvgpr_read_b32 v24, a116\n"
    "v_accvgpr_read_b32 v25, a120\n"
    "v_mfma_f32_16x16x32_bf16 v[76:79], a[24:27], a[104:107], v[76:79]\n"
    "v_accvgpr_read_b32 v26, a124\n"
    "v_max3_u32 v24, v24, v25, v26\n"
    "v_mfma_f32_16x16x32_bf16 v[60:63], a[28:31], a[108:111], v[60:63]\n"
    "v_accvgpr_read_b32 v25, a128\n"
    "v_accvgpr_read_b32 v26, a132\n"
    "v_mfma_f32_16x16x32_bf16 v[64:67], a[32:35], a[108:111], v[64:67]\n"
    "v_max3_u32 v24, v24, v25, v26\n"
    "v_accvgpr_read_b32 v25, a136\n"
    "v_mfma_f32_16x16x32_bf16 v[68:71], a[36:39], a[108:111], v[68:71]\n"
    "v_accvgpr_read_b32 v26, a140\n"
    "v_mfma_f32_16x16x32_bf16 v[72:75], a[40:43], a[108:111], v[72:75]\n"
    "v_max3_u32 v24, v24, v25, v26\n"
    "v_accvgpr_read_b32 v25, a144\n"
    "v_mfma_f32_16x16x32_bf16 v[76:79], a[44:47], a[108:111], v[76:79]\n"
    "v_accvgpr_read_b32 v26, a164\n"
    "v_max3_u32 v24, v24, v25, v26\n"
    "v_mfma_f32_16x16x32_bf16 v[60:63], a[48:51], a[112:115], v[60:63]\n"
    "v_accvgpr_read_b32 v25, a168\n"
    "v_accvgpr_read_b32 v26, a172\n"
    "v_mfma_f32_16x16x32_bf16 v[64:67], a[52:55], a[112:115], v[64:67]\n"
    "v_max3_u32 v24, v24, v25, v26\n"
    "v_accvgpr_read_b32 v25, a176\n"
    "v_mfma_f32_16x16x32_bf16 v[68:71], a[56:59], a[112:115], v[68:71]\n"
    "v_max_u32 v24, v24, v25\n"
    "v_mfma_f32_16x16x32_bf16 v[72:75], a[60:63], a[112:115], v[72:75]\n"
    "v_mfma_f32_16x16x32_bf16 v[76:79], a[64:67], a[112:115], v[76:79]\n"
    "v_cmp_eq_u32 vcc, -1, v24\n"
    "s_nop 1\n"
    "s_cbranch_vccnz .Lr13_%=\n"
    ".Ld13_%=:\n"
    "v_mfma_f32_16x16x32_bf16 v[40:43], v[208:211], a[116:119], 0\n"
    "v_mfma_f32_16x16x32_bf16 v[44:47], v[212:215], a[116:119], 0\n"
    "v_mfma_f32_16x16x32_bf16 v[48:51], v[216:219], a[116:119], 0\n"
    "v_mfma_f32_16x16x32_bf16 v[52:55], v[220:223], a[116:119], 0\n"
    "v_mfma_f32_16x16x32_bf16 v[56:59], v[224:227], a[116:119], 0\n"
    "ds_write_b128 v21, v[60:63]\n"
    "v_mfma_f32_16x16x32_bf16 v[40:43], v[228:231], a[120:123], v[40:43]\n"
    "v_mfma_f32_16x16x32_bf16 v[44:47], v[232:235], a[120:123], v[44:47]\n"
    "v_mfma_f32_16x16x32_bf16 v[48:51], v[236:239], a[120:123], v[48:51]\n"
    "v_mfma_f32_16x16x32_bf16 v[52:55], a[148:151], a[120:123], v[52:55]\n"
    "v_mfma_f32_16x16x32_bf16 v[56:59], a[152:155], a[120:123], v[56:59]\n"
    "v_mfma_f32_16x16x32_bf16 v[40:43], a[156:159], a[124:127], v[40:43]\n"
    "v_mfma_f32_16x16x32_bf16 v[44:47], a[160:163], a[124:127], v[44:47]\n"
    "v_mfma_f32_16x16x32_bf16 v[48:51], v[128:131], a[124:127], v[48:51]\n"
    "v_mfma_f32_16x16x32_bf16 v[52:55], v[132:135], a[124:127], v[52:55]\n"
    "v_mfma_f32_16x16x32_bf16 v[56:59], v[136:139], a[124:127], v[56:59]\n"
    "ds_write_b128 v21, v[64:67] offset:1024\n"
    "v_mfma_f32_16x16x32_bf16 v[40:43], v[140:143], a[128:131], v[40:43]\n"
    "v_mfma_f32_16x16x32_bf16 v[44:47], v[144:147], a[128:131], v[44:47]\n"
    "v_mfma_f32_16x16x32_bf16 v[48:51], v[148:151], a[128:131], v[48:51]\n"
    "v_mfma_f32_16x16x32_bf16 v[52:55], v[152:155], a[128:131], v[52:55]\n"
    "v_mfma_f32_16x16x32_bf16 v[56:59], v[156:159], a[128:131], v[56:59]\n"
    "v_mfma_f32_16x16x32_bf16 v[40:43], v[160:163], a[132:135], v[40:43]\n"
    "v_mfma_f32_16x16x32_bf16 v[44:47], v[164:167], a[132:135], v[44:47]\n"
    "v_mfma_f32_16x16x32_bf16 v[48:51], v[168:171], a[132:135], v[48:51]\n"
    "v_mfma_f32_16x16x32_bf16 v[52:55], v[172:175], a[132:135], v[52:55]\n"
    "v_mfma_f32_16x16x32_bf16 v[56:59], v[176:179], a[132:135], v[56:59]\n"
    "ds_write_b128 v21, v[68:71] offset:2048\n"
    "v_mfma_f32_16x16x32_bf16 v[40:43], v[180:183], a[136:139], v[40:43]\n"
    "v_mfma_f32_16x16x32_bf16 v[44:47], v[184:187], a[136:139], v[44:47]\n"
    "v_mfma_f32_16x16x32_bf16 v[48:51], v[188:191], a[136:139], v[48:51]\n"
    "v_mfma_f32_16x16x32_bf16 v[52:55], v[192:195], a[136:139], v[52:55]\n"
    "v_mfma_f32_16x16x32_bf16 v[56:59], v[196:199], a[136:139], v[56:59]\n"
    "v_mfma_f32_16x16x32_bf16 v[40:43], v[200:203], a[140:143], v[40:43]\n"
    "v_mfma_f32_16x16x32_bf16 v[44:47], v[204:207], a[140:143], v[44:47]\n"
    "v_mfma_f32_16x16x32_bf16 v[48:51], a[212:215], a[140:143], v[48:51]\n"
    "v_mfma_f32_16x16x32_bf16 v[52:55], a[216:219], a[140:143], v[52:55]\n"
    "v_mfma_f32_16x16x32_bf16 v[56:59], a[220:223], a[140:143], v[56:59]\n"
    "v_mfma_f32_16x16x32_bf16 v[40:43], a[224:227], a[144:147], v[40:43]\n"
    "ds_write_b128 v21, v[72:75] offset:3072\n"
    "v_mfma_f32_16x16x32_bf16 v[44:47], a[228:231], a[144:147], v[44:47]\n"
    "v_mfma_f32_16x16x32_bf16 v[48:51], a[232:235], a[144:147], v[48:51]\n"
    "v_mfma_f32_16x16x32_bf16 v[52:55], a[236:239], a[144:147], v[52:55]\n"
    "v_mfma_f32_16x16x32_bf16 v[56:59], a[240:243], a[144:147], v[56:59]\n"
    "v_mfma_f32_16x16x32_bf16 v[40:43], a[244:247], a[164:167], v[40:43]\n"
    "v_mfma_f32_16x16x32_bf16 v[44:47], a[248:251], a[164:167], v[44:47]\n"
    "v_mfma_f32_16x16x32_bf16 v[48:51], a[252:255], a[164:167], v[48:51]\n"
    "v_mfma_f32_16x16x32_bf16 v[52:55], a[0:3], a[164:167], v[52:55]\n"
    "v_mfma_f32_16x16x32_bf16 v[56:59], a[4:7], a[164:167], v[56:59]\n"
    "v_mfma_f32_16x16x32_bf16 v[40:43], a[8:11], a[168:171], v[40:43]\n"
    "ds_write_b128 v21, v[76:79] offset:4096\n"
    "v_mfma_f32_16x16x32_bf16 v[44:47], a[12:15], a[168:171], v[44:47]\n"
    "v_mfma_f32_16x16x32_bf16 v[48:51], a[16:19], a[168:171], v[48:51]\n"
    "s_waitcnt vmcnt(0)\n"
    "v_mfma_f32_16x16x32_bf16 v[52:55], a[20:23], a[168:171], v[52:55]\n"
    "v_accvgpr_read_b32 v24, a180\n"
    "v_accvgpr_read_b32 v25, a184\n"
    "v_mfma_f32_16x16x32_bf16 v[56:59], a[24:27], a[168:171], v[56:59]\n"
    "v_accvgpr_read_b32 v26, a188\n"
    "v_max3_u32 v24, v24, v25, v26\n"
    "v_mfma_f32_16x16x32_bf16 v[40:43], a[28:31], a[172:175], v[40:43]\n"
    "v_accvgpr_read_b32 v25, a192\n"
    "v_accvgpr_read_b32 v26, a196\n"
    "v_mfma_f32_16x16x32_bf16 v[44:47], a[32:35], a[172:175], v[44:47]\n"
    "v_max3_u32 v24, v24, v25, v26\n"
    "v_accvgpr_read_b32 v25, a200\n"
    "v_mfma_f32_16x16x32_bf16 v[48:51], a[36:39], a[172:175], v[48:51]\n"
    "v_accvgpr_read_b32 v26, a204\n"
    "v_mfma_f32_16x16x32_bf16 v[52:55], a[40:43], a[172:175], v[52:55]\n"
    "v_max3_u32 v24, v24, v25, v26\n"
    "v_accvgpr_read_b32 v25, a208\n"
    "v_mfma_f32_16x16x32_bf16 v[56:59], a[44:47], a[172:175], v[56:59]\n"
    "v_accvgpr_read_b32 v26, a68\n"
    "v_max3_u32 v24, v24, v25, v26\n"
    "v_mfma_f32_16x16x32_bf16 v[40:43], a[48:51], a[176:179], v[40:43]\n"
    "v_accvgpr_read_b32 v25, a72\n"
    "v_accvgpr_read_b32 v26, a76\n"
    "v_mfma_f32_16x16x32_bf16 v[44:47], a[52:55], a[176:179], v[44:47]\n"
    "v_max3_u32 v24, v24, v25, v26\n"
    "v_accvgpr_read_b32 v25, a80\n"
    "v_mfma_f32_16x16x32_bf16 v[48:51], a[56:59], a[176:179], v[48:51]\n"
    "v_max_u32 v24, v24, v25\n"
    "v_mfma_f32_16x16x32_bf16 v[52:55], a[60:63], a[176:179], v[52:55]\n"
    "v_mfma_f32_16x16x32_bf16 v[56:59], a[64:67], a[176:179], v[56:59]\n"
    "v_cmp_eq_u32 vcc, -1, v24\n"
    "s_nop 1\n"
    "s_cbranch_vccnz .Lr14_%=\n"
    ".Ld14_%=:\n"
    "v_mfma_f32_16x16x32_bf16 v[60:63], v[208:211], a[180:183], 0\n"
    "v_mfma_f32_16x16x32_bf16 v[64:67], v[212:215], a[180:183], 0\n"
    "v_mfma_f32_16x16x32_bf16 v[68:71], v[216:219], a[180:183], 0\n"
    "v_mfma_f32_16x16x32_bf16 v[72:75], v[220:223], a[180:183], 0\n"
    "v_mfma_f32_16x16x32_bf16 v[76:79], v[224:227], a[180:183], 0\n"
    "ds_write_b128 v22, v[40:43]\n"
    "v_mfma_f32_16x16x32_bf16 v[60:63], v[228:231], a[184:187], v[60:63]\n"
    "ds_read_b128 v[28:31], v16 offset:6400\n"
    "ds_read_b128 v[32:35], v16 offset:6464\n"
    "v_mfma_f32_16x16x32_bf16 v[64:67], v[232:235], a[184:187], v[64:67]\n"
    "ds_read_b64 v[36:37], v17 offset:6400\n"
    "v_mfma_f32_16x16x32_bf16 v[68:71], v[236:239], a[184:187], v[68:71]\n"
    "v_lshlrev_b32 v80, 16, v240\n"
    "v_mfma_f32_16x16x32_bf16 v[72:75], a[148:151], a[184:187], v[72:75]\n"
    "ds_write_b128 v22, v[44:47] offset:1024\n"
    "v_mfma_f32_16x16x32_bf16 v[76:79], a[152:155], a[184:187], v[76:79]\n"
    "v_and_b32 v81, 0xffff0000, v240\n"
    "v_lshlrev_b32 v82, 16, v241\n"
    "v_mfma_f32_16x16x32_bf16 v[60:63], a[156:159], a[188:191], v[60:63]\n"
    "v_and_b32 v83, 0xffff0000, v241\n"
    "v_mfma_f32_16x16x32_bf16 v[64:67], a[160:163], a[188:191], v[64:67]\n"
    "v_lshlrev_b32 v84, 16, v242\n"
    "v_mfma_f32_16x16x32_bf16 v[68:71], v[128:131], a[188:191], v[68:71]\n"
    "ds_write_b128 v22, v[48:51] offset:2048\n"
    "v_mfma_f32_16x16x32_bf16 v[72:75], v[132:135], a[188:191], v[72:75]\n"
    "v_and_b32 v85, 0xffff0000, v242\n"
    "v_lshlrev_b32 v86, 16, v243\n"
    "v_mfma_f32_16x16x32_bf16 v[76:79], v[136:139], a[188:191], v[76:79]\n"
    "v_and_b32 v87, 0xffff0000, v243\n"
    "v_mfma_f32_16x16x32_bf16 v[60:63], v[140:143], a[192:195], v[60:63]\n"
    "v_lshlrev_b32 v88, 16, v244\n"
    "v_and_b32 v89, 0xffff0000, v244\n"
    "v_mfma_f32_16x16x32_bf16 v[64:67], v[144:147], a[192:195], v[64:67]\n"
    "ds_write_b128 v22, v[52:55] offset:3072\n"
    "v_mfma_f32_16x16x32_bf16 v[68:71], v[148:151], a[192:195], v[68:71]\n"
    "v_lshlrev_b32 v90, 16, v245\n"
    "v_and_b32 v91, 0xffff0000, v245\n"
    "v_mfma_f32_16x16x32_bf16 v[72:75], v[152:155], a[192:195], v[72:75]\n"
    "v_lshlrev_b32 v92, 16, v246\n"
    "v_mfma_f32_16x16x32_bf16 v[76:79], v[156:159], a[192:195], v[76:79]\n"
    "v_and_b32 v93, 0xffff0000, v246\n"
    "v_mfma_f32_16x16x32_bf16 v[60:63], v[160:163], a[196:199], v[60:63]\n"
    "ds_write_b128 v22, v[56:59] offset:4096\n"
    "v_mfma_f32_16x16x32_bf16 v[64:67], v[164:167], a[196:199], v[64:67]\n"
    "v_lshlrev_b32 v94, 16, v247\n"
    "v_and_b32 v95, 0xffff0000, v247\n"
    "v_mfma_f32_16x16x32_bf16 v[68:71], v[168:171], a[196:199], v[68:71]\n"
    "v_lshlrev_b32 v96, 16, v248\n"
    "v_mfma_f32_16x16x32_bf16 v[72:75], v[172:175], a[196:199], v[72:75]\n"
    "v_and_b32 v97, 0xffff0000, v248\n"
    "v_mfma_f32_16x16x32_bf16 v[76:79], v[176:179], a[196:199], v[76:79]\n"
    "v_lshlrev_b32 v98, 16, v249\n"
    "v_mfma_f32_16x16x32_bf16 v[60:63], v[180:183], a[200:203], v[60:63]\n"
    "v_and_b32 v99, 0xffff0000, v249\n"
    "s_waitcnt lgkmcnt(4)\n"
    "v_mfma_f32_16x16x32_bf16 v[64:67], v[184:187], a[200:203], v[64:67]\n"
    "v_lshlrev_b32 v100, 16, v28\n"
    "v_mfma_f32_16x16x32_bf16 v[68:71], v[188:191], a[200:203], v[68:71]\n"
    "v_and_b32 v101, 0xffff0000, v28\n"
    "v_mfma_f32_16x16x32_bf16 v[72:75], v[192:195], a[200:203], v[72:75]\n"
    "v_lshlrev_b32 v102, 16, v29\n"
    "v_mfma_f32_16x16x32_bf16 v[76:79], v[196:199], a[200:203], v[76:79]\n"
    "v_and_b32 v103, 0xffff0000, v29\n"
    "v_mfma_f32_16x16x32_bf16 v[60:63], v[200:203], a[204:207], v[60:63]\n"
    "v_lshlrev_b32 v104, 16, v30\n"
    "v_mfma_f32_16x16x32_bf16 v[64:67], v[204:207], a[204:207], v[64:67]\n"
    "v_and_b32 v105, 0xffff0000, v30\n"
    "v_mfma_f32_16x16x32_bf16 v[68:71], a[212:215], a[204:207], v[68:71]\n"
    "v_lshlrev_b32 v106, 16, v31\n"
    "v_mfma_f32_16x16x32_bf16 v[72:75], a[216:219], a[204:207], v[72:75]\n"
    "v_and_b32 v107, 0xffff0000, v31\n"
    "v_mfma_f32_16x16x32_bf16 v[76:79], a[220:223], a[204:207], v[76:79]\n"
    "v_lshlrev_b32 v108, 16, v32\n"
    "v_mfma_f32_16x16x32_bf16 v[60:63], a[224:227], a[208:211], v[60:63]\n"
    "v_and_b32 v109, 0xffff0000, v32\n"
    "v_lshlrev_b32 v110, 16, v33\n"
    "v_mfma_f32_16x16x32_bf16 v[64:67], a[228:231], a[208:211], v[64:67]\n"
    "v_and_b32 v111, 0xffff0000, v33\n"
    "v_mfma_f32_16x16x32_bf16 v[68:71], a[232:235], a[208:211], v[68:71]\n"
    "v_lshlrev_b32 v112, 16, v34\n"
    "v_mfma_f32_16x16x32_bf16 v[72:75], a[236:239], a[208:211], v[72:75]\n"
    "v_and_b32 v113, 0xffff0000, v34\n"
    "v_mfma_f32_16x16x32_bf16 v[76:79], a[240:243], a[208:211], v[76:79]\n"
    "v_lshlrev_b32 v114, 16, v35\n"
    "v_mfma_f32_16x16x32_bf16 v[60:63], a[244:247], a[68:71], v[60:63]\n"
    "v_and_b32 v115, 0xffff0000, v35\n"
    "v_mfma_f32_16x16x32_bf16 v[64:67], a[248:251], a[68:71], v[64:67]\n"
    "v_lshlrev_b32 v116, 16, v36\n"
    "v_mfma_f32_16x16x32_bf16 v[68:71], a[252:255], a[68:71], v[68:71]\n"
    "s_waitcnt lgkmcnt(0)\n"
    "s_barrier\n"
    "ds_read_b128 v[128:131], v23\n"
    "ds_read_b128 v[148:151], v23 offset:5120\n"
    "v_mfma_f32_16x16x32_bf16 v[72:75], a[0:3], a[68:71], v[72:75]\n"
    "ds_read_b128 v[168:171], v23 offset:10240\n"
    "v_and_b32 v117, 0xffff0000, v36\n"
    "v_mfma_f32_16x16x32_bf16 v[76:79], a[4:7], a[68:71], v[76:79]\n"
    "ds_read_b128 v[132:135], v23 offset:1024\n"
    "v_lshlrev_b32 v118, 16, v37\n"
    "v_mfma_f32_16x16x32_bf16 v[60:63], a[8:11], a[72:75], v[60:63]\n"
    "ds_read_b128 v[152:155], v23 offset:6144\n"
    "v_and_b32 v119, 0xffff0000, v37\n"
    "v_mfma_f32_16x16x32_bf16 v[64:67], a[12:15], a[72:75], v[64:67]\n"
    "ds_read_b128 v[172:175], v23 offset:11264\n"
    "ds_read_b128 v[136:139], v23 offset:2048\n"
    "v_mfma_f32_16x16x32_bf16 v[68:71], a[16:19], a[72:75], v[68:71]\n"
    "ds_read_b128 v[156:159], v23 offset:7168\n"
    "v_mfma_f32_16x16x32_bf16 v[72:75], a[20:23], a[72:75], v[72:75]\n"
    "ds_read_b128 v[176:179], v23 offset:12288\n"
    "v_mfma_f32_16x16x32_bf16 v[76:79], a[24:27], a[72:75], v[76:79]\n"
    "ds_read_b128 v[140:143], v23 offset:3072\n"
    "v_mfma_f32_16x16x32_bf16 v[60:63], a[28:31], a[76:79], v[60:63]\n"
    "ds_read_b128 v[160:163], v23 offset:8192\n"
    "ds_read_b128 v[180:183], v23 offset:13312\n"
    "v_mfma_f32_16x16x32_bf16 v[64:67], a[32:35], a[76:79], v[64:67]\n"
    "ds_read_b128 v[144:147], v23 offset:4096\n"
    "v_mfma_f32_16x16x32_bf16 v[68:71], a[36:39], a[76:79], v[68:71]\n"
    "ds_read_b128 v[164:167], v23 offset:9216\n"
    "v_mfma_f32_16x16x32_bf16 v[72:75], a[40:43], a[76:79], v[72:75]\n"
    "ds_read_b128 v[184:187], v23 offset:14336\n"
    "v_mfma_f32_16x16x32_bf16 v[76:79], a[44:47], a[76:79], v[76:79]\n"
    "v_mfma_f32_16x16x32_bf16 v[60:63], a[48:51], a[80:83], v[60:63]\n"
    "v_mfma_f32_16x16x32_bf16 v[64:67], a[52:55], a[80:83], v[64:67]\n"
    "v_mfma_f32_16x16x32_bf16 v[68:71], a[56:59], a[80:83], v[68:71]\n"
    "v_mfma_f32_16x16x32_bf16 v[72:75], a[60:63], a[80:83], v[72:75]\n"
    "v_mfma_f32_16x16x32_bf16 v[76:79], a[64:67], a[80:83], v[76:79]\n"
    "s_waitcnt lgkmcnt(12)\n"
    "v_pk_add_f32 v[148:149], v[148:149], v[168:169]\n"
    "v_pk_add_f32 v[150:151], v[150:151], v[170:171]\n"
    "v_pk_add_f32 v[128:129], v[128:129], v[148:149]\n"
    "v_pk_add_f32 v[130:131], v[130:131], v[150:151]\n"
    "v_pk_add_f32 v[60:61], v[60:61], v[128:129]\n"
    "v_pk_add_f32 v[62:63], v[62:63], v[130:131]\n"
    "v_pk_fma_f32 v[80:81], v[60:61], v[100:101], v[80:81]\n"
    "v_pk_fma_f32 v[82:83], v[62:63], v[102:103], v[82:83]\n"
    "s_waitcnt lgkmcnt(9)\n"
    "v_pk_add_f32 v[152:153], v[152:153], v[172:173]\n"
    "v_pk_add_f32 v[154:155], v[154:155], v[174:175]\n"
    "v_pk_add_f32 v[132:133], v[132:133], v[152:153]\n"
    "v_pk_add_f32 v[134:135], v[134:135], v[154:155]\n"
    "v_pk_add_f32 v[64:65], v[64:65], v[132:133]\n"
    "v_pk_add_f32 v[66:67], v[66:67], v[134:135]\n"
    "v_pk_fma_f32 v[84:85], v[64:65], v[104:105], v[84:85]\n"
    "v_pk_fma_f32 v[86:87], v[66:67], v[106:107], v[86:87]\n"
    "v_cvt_pk_bf16_f32 v40, v80, v81\n"
    "v_cvt_pk_bf16_f32 v41, v82, v83\n"
    "v_cvt_pk_bf16_f32 v42, v84, v85\n"
    "v_cvt_pk_bf16_f32 v43, v86, v87\n"
    "buffer_store_dwordx4 v[40:43], v12, s[24:27], 0 offen sc1\n"
    "s_waitcnt lgkmcnt(6)\n"
    "v_pk_add_f32 v[156:157], v[156:157], v[176:177]\n"
    "v_pk_add_f32 v[158:159], v[158:159], v[178:179]\n"
    "v_pk_add_f32 v[136:137], v[136:137], v[156:157]\n"
    "v_pk_add_f32 v[138:139], v[138:139], v[158:159]\n"
    "v_pk_add_f32 v[68:69], v[68:69], v[136:137]\n"
    "v_pk_add_f32 v[70:71], v[70:71], v[138:139]\n"
    "v_pk_fma_f32 v[88:89], v[68:69], v[108:109], v[88:89]\n"
    "v_pk_fma_f32 v[90:91], v[70:71], v[110:111], v[90:91]\n"
    "s_waitcnt lgkmcnt(3)\n"
    "v_pk_add_f32 v[160:161], v[160:161], v[180:181]\n"
    "v_pk_add_f32 v[162:163], v[162:163], v[182:183]\n"
    "v_pk_add_f32 v[140:141], v[140:141], v[160:161]\n"
    "v_pk_add_f32 v[142:143], v[142:143], v[162:163]\n"
    "v_pk_add_f32 v[72:73], v[72:73], v[140:141]\n"
    "v_pk_add_f32 v[74:75], v[74:75], v[142:143]\n"
    "v_pk_fma_f32 v[92:93], v[72:73], v[112:113], v[92:93]\n"
    "v_pk_fma_f32 v[94:95], v[74:75], v[114:115], v[94:95]\n"
    "v_cvt_pk_bf16_f32 v44, v88, v89\n"
    "v_cvt_pk_bf16_f32 v45, v90, v91\n"
    "v_cvt_pk_bf16_f32 v46, v92, v93\n"
    "v_cvt_pk_bf16_f32 v47, v94, v95\n"
    "buffer_store_dwordx4 v[44:47], v12, s[24:27], 0 offen offset:64 sc1\n"
    "s_waitcnt lgkmcnt(0)\n"
    "v_pk_add_f32 v[164:165], v[164:165], v[184:185]\n"
    "v_pk_add_f32 v[166:167], v[166:167], v[186:187]\n"
    "v_pk_add_f32 v[144:145], v[144:145], v[164:165]\n"
    "v_pk_add_f32 v[146:147], v[146:147], v[166:167]\n"
    "v_pk_add_f32 v[76:77], v[76:77], v[144:145]\n"
    "v_pk_add_f32 v[78:79], v[78:79], v[146:147]\n"
    "v_pk_fma_f32 v[96:97], v[76:77], v[116:117], v[96:97]\n"
    "v_pk_fma_f32 v[98:99], v[78:79], v[118:119], v[98:99]\n"
    "v_cvt_pk_bf16_f32 v48, v96, v97\n"
    "v_cvt_pk_bf16_f32 v49, v98, v99\n"
    "buffer_store_dwordx2 v[48:49], v13, s[24:27], 0 offen sc1\n"
    "s_endpgm\n"
    ".Lgp_%=:\n"
    "s_lshr_b32 s92, s2, 4\n"
    "s_lshl_b32 s92, s92, 3\n"
    "s_add_u32 s70, s92, s41\n"
    "s_and_b32 s62, s37, 1\n"
    "s_lshl_b32 s92, s70, 1\n"
    "s_add_u32 s71, s92, s62\n"
    "s_lshl_b32 s92, s71, 6\n"
    "s_cmp_lt_u32 s70, 60\n"
    "s_cselect_b32 s72, s92, 0x40000000\n"
    "v_and_b32 v4, 0xfc, v0\n"
    "v_and_b32 v24, 3, v0\n"
    "v_lshrrev_b32 v25, 2, v0\n"
    "v_mul_u32_u24 v25, 0x3c00, v25\n"
    "v_lshl_add_u32 v5, v24, 4, v25\n"
    "v_add_u32 v6, 0x1e00, v5\n"
    "s_lshl_b32 s51, s62, 9\n"
    "s_mul_i32 s92, s51, 0xf00\n"
    "s_add_u32 s56, s92, s72\n"
    "s_add_u32 s52, s51, 0x100\n"
    "s_mul_i32 s92, s52, 0xf00\n"
    "s_add_u32 s57, s92, s72\n"
    "s_add_u32 s53, s52, 0x100\n"
    "s_mul_i32 s92, s53, 0xf00\n"
    "s_add_u32 s58, s92, s72\n"
    "s_add_u32 s54, s53, 0x100\n"
    "s_cmp_eq_u32 s54, 1280\n"
    "s_cselect_b32 s54, 0, s54\n"
    "s_mul_i32 s92, s54, 0xf00\n"
    "s_add_u32 s59, s92, s72\n"
    "s_add_u32 s55, s54, 0x100\n"
    "s_mul_i32 s92, s55, 0xf00\n"
    "s_add_u32 s72, s92, s72\n"
    "s_waitcnt lgkmcnt(0)\n"
    "buffer_load_dword v240, v4, s[12:15], s51 offen\n"
    "buffer_load_dword v241, v4, s[12:15], s52 offen\n"
    "buffer_load_dword v242, v4, s[12:15], s53 offen\n"
    "buffer_load_dword v243, v4, s[12:15], s54 offen\n"
    "buffer_load_dword v244, v4, s[12:15], s55 offen\n"
    "buffer_load_dword v253, v33, s[16:19], 0 offen sc1\n"
    "buffer_load_dwordx4 v[200:203], v5, s[8:11], s56 offen\n"
    "buffer_load_dwordx4 v[204:207], v6, s[8:11], s56 offen\n"
    "buffer_load_dwordx4 v[208:211], v5, s[8:11], s57 offen\n"
    "buffer_load_dwordx4 v[212:215], v6, s[8:11], s57 offen\n"
    "buffer_load_dwordx4 v[216:219], v5, s[8:11], s58 offen\n"
    "buffer_load_dwordx4 v[220:223], v6, s[8:11], s58 offen\n"
    "buffer_load_dwordx4 v[224:227], v5, s[8:11], s59 offen\n"
    "buffer_load_dwordx4 v[228:231], v6, s[8:11], s59 offen\n"
    "buffer_load_dwordx4 v[232:235], v5, s[8:11], s72 offen\n"
    "buffer_load_dwordx4 v[236:239], v6, s[8:11], s72 offen\n"
    "v_and_b32 v24, 3, v1\n"
    "s_max_i32 s91, s90, 0\n"
    "buffer_load_dwordx4 v[46:49], v2, s[4:7], s91 offen\n"
    "v_lshrrev_b32 v25, 4, v1\n"
    "v_lshl_add_u32 v24, v24, 2, v25\n"
    "s_lshl_b32 s92, s36, 7\n"
    "s_add_u32 s92, s92, 0x19100\n"
    "v_lshl_add_u32 v19, v24, 3, s92\n"
    "v_and_b32 v24, 7, v1\n"
    "v_lshlrev_b32 v24, 4, v24\n"
    "v_add_u32 v18, 0x19100, v24\n"
    "s_lshl_b32 s92, s71, 7\n"
    "s_add_u32 s92, s92, 0x20000\n"
    "s_cmp_lt_u32 s70, 60\n"
    "s_cselect_b32 s88, s92, 0x40000000\n"
    "v_and_b32 v24, 7, v1\n"
    "v_lshrrev_b32 v25, 3, v1\n"
    "v_mul_u32_u24 v25, 0x3c00, v25\n"
    "v_lshl_add_u32 v24, v24, 4, v25\n"
    "v_cmp_gt_u32 vcc, 64, v0\n"
    "s_nop 1\n"
    "v_cndmask_b32 v17, 0x40000000, v24, vcc\n"
    "s_mul_i32 s92, s71, 0xcd\n"
    "s_lshr_b32 s89, s92, 12\n"
    "s_cmp_eq_u32 s89, 4\n"
    "s_cselect_b32 s92, 0x3f800000, 0\n"
    "buffer_load_dword v45, v34, s[4:7], s91 offen offset:1024\n"
    "s_cmp_eq_u32 s89, 1\n"
    "s_cselect_b32 s89, 0x3f800000, s92\n"
    "s_mul_i32 s92, s36, 0x3c00\n"
    "s_add_u32 s78, s92, 0x17100\n"
    "s_add_u32 s92, s39, -8\n"
    "s_cmp_lt_u32 s39, 8\n"
    "s_cselect_b32 s73, 0, s92\n"
    "s_mul_i32 s92, s73, 43\n"
    "s_lshr_b32 s74, s92, 8\n"
    "s_mul_i32 s92, s74, 6\n"
    "s_sub_u32 s75, s73, s92\n"
    "s_mul_i32 s92, s74, 52\n"
    "s_lshr_b32 s76, s92, 8\n"
    "s_mul_i32 s92, s76, 5\n"
    "s_sub_u32 s77, s74, s92\n"
    "s_mul_i32 s92, s76, 0xa0\n"
    "s_lshr_b32 s93, s77, 1\n"
    "s_mul_i32 s93, s93, 0x50\n"
    "s_and_b32 s94, s77, 1\n"
    "buffer_load_dwordx4 v[56:59], v2, s[4:7], s91 offen offset:2560\n"
    "s_lshl_b32 s94, s94, 5\n"
    "s_add_u32 s93, s93, s94\n"
    "s_cmp_eq_u32 s77, 4\n"
    "s_cselect_b32 s93, 64, s93\n"
    "s_add_u32 s92, s92, s93\n"
    "s_mul_i32 s92, s92, 0xc00\n"
    "s_mul_i32 s93, s37, 0x180\n"
    "s_add_u32 s92, s92, s93\n"
    "s_lshl_b32 s93, s75, 6\n"
    "s_add_u32 s76, s92, s93\n"
    "s_mov_b32 s92, 0x3c000\n"
    "s_cmp_eq_u32 s77, 4\n"
    "s_cselect_b32 s92, s92, 0xc000\n"
    "s_add_u32 s77, s92, s76\n"
    "v_and_b32 v24, 3, v1\n"
    "v_lshrrev_b32 v25, 2, v1\n"
    "v_mul_u32_u24 v25, 0xc00, v25\n"
    "v_lshl_add_u32 v11, v24, 4, v25\n"
    "s_mov_b32 s42, 0x40000\n"
    "s_mov_b32 s43, 0x3f803f80\n"
    "buffer_load_dword v55, v34, s[4:7], s91 offen offset:3584\n"
    "s_lshl_b32 s92, s37, 9\n"
    "s_lshl_b32 s93, s39, 2\n"
    "s_add_u32 s92, s92, s93\n"
    "s_add_u32 s92, s92, 0x1000\n"
    "v_lshl_add_u32 v24, v1, 12, s92\n"
    "s_lshl_b32 s92, s38, 7\n"
    "s_lshl_b32 s93, s37, 4\n"
    "s_add_u32 s92, s92, s93\n"
    "s_lshl_b32 s93, s36, 2\n"
    "s_add_u32 s92, s92, s93\n"
    "s_add_u32 s92, s92, 0xa0000\n"
    "v_mov_b32 v25, s92\n"
    "v_cmp_eq_u32 vcc, 8, v1\n"
    "s_nop 1\n"
    "v_cndmask_b32 v25, 0x40000000, v25, vcc\n"
    "v_cmp_gt_u32 vcc, 8, v1\n"
    "s_nop 1\n"
    "v_cndmask_b32 v9, v25, v24, vcc\n"
    "s_lshl_b32 s92, s38, 2\n"
    "s_lshr_b32 s93, s37, 1\n"
    "s_add_u32 s92, s92, s93\n"
    "s_mul_i32 s92, s92, 0x5000\n"
    "s_add_u32 s90, s90, 5120\n"
    "s_max_i32 s91, s90, 0\n"
    "buffer_load_dwordx4 v[66:69], v2, s[4:7], s91 offen\n"
    "s_add_u32 s49, s92, 0x800000\n"
    "v_lshrrev_b32 v24, 5, v1\n"
    "v_mul_u32_u24 v24, 0x1400, v24\n"
    "v_and_b32 v25, 31, v1\n"
    "s_and_b32 s92, s37, 1\n"
    "s_lshl_b32 s92, s92, 11\n"
    "s_add_u32 s92, s49, s92\n"
    "s_lshl_b32 s93, s36, 9\n"
    "s_add_u32 s92, s92, s93\n"
    "v_lshl_add_u32 v25, v25, 4, s92\n"
    "v_add_u32 v7, v24, v25\n"
    "v_lshrrev_b32 v24, 4, v1\n"
    "v_mul_u32_u24 v24, 0x1400, v24\n"
    "v_and_b32 v25, 15, v1\n"
    "s_and_b32 s92, s37, 1\n"
    "s_lshl_b32 s92, s92, 9\n"
    "s_add_u32 s92, s49, s92\n"
    "s_lshl_b32 s93, s36, 7\n"
    "s_add_u32 s92, s92, s93\n"
    "s_add_u32 s92, s92, 0x1000\n"
    "v_lshl_add_u32 v25, v25, 3, s92\n"
    "buffer_load_dword v65, v34, s[4:7], s91 offen offset:1024\n"
    "v_add_u32 v8, v24, v25\n"
    "s_mov_b32 s50, 0x2800\n"
    "v_accvgpr_write_b32 a244, -1\n"
    "v_accvgpr_write_b32 a245, -1\n"
    "v_accvgpr_write_b32 a246, -1\n"
    "v_accvgpr_write_b32 a247, -1\n"
    "v_and_b32 v24, 3, v1\n"
    "v_lshrrev_b32 v25, 2, v1\n"
    "v_mul_u32_u24 v25, 0x50, v25\n"
    "v_add_u32 v25, s78, v25\n"
    "v_lshl_add_u32 v22, v24, 4, v25\n"
    "v_and_b32 v24, 1, v1\n"
    "v_bfe_u32 v25, v1, 1, 1\n"
    "s_cmp_lt_u32 s75, 4\n"
    "s_cselect_b32 s92, 32, 8\n"
    "v_lshrrev_b32 v26, 4, v1\n"
    "v_bfe_u32 v27, v1, 2, 2\n"
    "v_lshl_add_u32 v26, v26, 3, v27\n"
    "v_mul_u32_u24 v26, 0x50, v26\n"
    "v_add_u32 v26, s78, v26\n"
    "buffer_load_dwordx4 v[76:79], v2, s[4:7], s91 offen offset:2560\n"
    "v_mad_u32_u24 v25, v25, s92, v26\n"
    "v_lshl_add_u32 v21, v24, 4, v25\n"
    "s_cmp_lt_u32 s75, 4\n"
    "s_cselect_b32 s92, 8, 32\n"
    "v_add_u32 v20, s92, v21\n"
    "s_mul_i32 s92, s37, 0x50\n"
    "s_add_u32 s92, s92, s74\n"
    "s_mul_i32 s92, s92, 0xc00\n"
    "s_mul_i32 s93, s75, 0xf000\n"
    "s_add_u32 s93, s93, 0x610000\n"
    "s_mul_i32 s94, s75, 0x1e000\n"
    "s_add_u32 s94, s94, 0x598800\n"
    "s_cmp_lt_u32 s75, 4\n"
    "s_cselect_b32 s93, s93, s94\n"
    "s_add_u32 s92, s92, s93\n"
    "s_cmp_lt_u32 s39, 8\n"
    "s_cselect_b32 s12, 0x40000000, s92\n"
    "s_mov_b32 s92, 0x400\n"
    "s_cmp_lt_u32 s75, 4\n"
    "s_cselect_b32 s92, s92, 0xf000\n"
    "buffer_load_dword v75, v34, s[4:7], s91 offen offset:3584\n"
    "s_add_u32 s13, s12, s92\n"
    "s_mul_i32 s92, s41, 0x3c00\n"
    "s_add_u32 s45, s92, 0x20000\n"
    "s_add_u32 s46, s45, 0x1000\n"
    "v_lshlrev_b32 v6, 4, v0\n"
    "v_cmp_gt_u32 vcc, 60, v1\n"
    "s_nop 1\n"
    "v_cndmask_b32 v24, 59, v1, vcc\n"
    "s_lshl_b32 s92, s41, 12\n"
    "s_lshl_b32 s93, s37, 9\n"
    "s_add_u32 s92, s92, s93\n"
    "s_add_u32 s92, s92, 0x1000\n"
    "v_lshl_add_u32 v10, v24, 2, s92\n"
    "s_lshl_b32 s92, s38, 3\n"
    "s_add_u32 s92, s92, s37\n"
    "s_lshl_b32 s92, s92, 2\n"
    "s_add_u32 s87, s92, s36\n"
    "s_mul_i32 s92, s87, 0xa00\n"
    "s_add_u32 s32, s92, 0x800000\n"
    "s_add_u32 s33, s32, 0x280000\n"
    "s_waitcnt vmcnt(18)\n"
    "v_readfirstlane_b32 s40, v253\n"
    "s_add_u32 s90, s90, 5120\n"
    "s_max_i32 s91, s90, 0\n"
    "buffer_load_dwordx4 v[86:89], v2, s[4:7], s91 offen\n"
    "s_mul_i32 s40, s40, 0x9E3779B1\n"
    "s_add_u32 s40, s40, 0x7F4A7C15\n"
    "v_mov_b32 v3, s40\n"
    "v_and_b32 v190, 0xffff0000, v240\n"
    "v_lshlrev_b32 v240, 16, v240\n"
    "v_and_b32 v191, 0xffff0000, v241\n"
    "v_lshlrev_b32 v241, 16, v241\n"
    "v_and_b32 v192, 0xffff0000, v242\n"
    "v_lshlrev_b32 v242, 16, v242\n"
    "v_and_b32 v193, 0xffff0000, v243\n"
    "v_lshlrev_b32 v243, 16, v243\n"
    "v_and_b32 v194, 0xffff0000, v244\n"
    "v_lshlrev_b32 v244, 16, v244\n"
    "buffer_load_dword v85, v34, s[4:7], s91 offen offset:1024\n"
    "v_mul_f32 v35, 0xbfb8aa3b, v240\n"
    "v_mul_f32 v195, 0xbfb8aa3b, v190\n"
    "v_mul_f32 v36, 0xbfb8aa3b, v241\n"
    "v_mul_f32 v196, 0xbfb8aa3b, v191\n"
    "v_mul_f32 v37, 0xbfb8aa3b, v242\n"
    "v_mul_f32 v197, 0xbfb8aa3b, v192\n"
    "v_mul_f32 v38, 0xbfb8aa3b, v243\n"
    "v_mul_f32 v198, 0xbfb8aa3b, v193\n"
    "v_mul_f32 v39, 0xbfb8aa3b, v244\n"
    "v_mul_f32 v199, 0xbfb8aa3b, v194\n"
    "v_exp_f32 v35, v35\n"
    "v_exp_f32 v195, v195\n"
    "v_exp_f32 v36, v36\n"
    "buffer_load_dwordx4 v[96:99], v2, s[4:7], s91 offen offset:2560\n"
    "v_exp_f32 v196, v196\n"
    "v_exp_f32 v37, v37\n"
    "v_exp_f32 v197, v197\n"
    "v_exp_f32 v38, v38\n"
    "v_exp_f32 v198, v198\n"
    "v_exp_f32 v39, v39\n"
    "v_exp_f32 v199, v199\n"
    "v_add_f32 v35, 1.0, v35\n"
    "v_add_f32 v195, 1.0, v195\n"
    "v_add_f32 v36, 1.0, v36\n"
    "v_add_f32 v196, 1.0, v196\n"
    "v_add_f32 v37, 1.0, v37\n"
    "v_add_f32 v197, 1.0, v197\n"
    "buffer_load_dword v95, v34, s[4:7], s91 offen offset:3584\n"
    "v_add_f32 v38, 1.0, v38\n"
    "v_add_f32 v198, 1.0, v198\n"
    "v_add_f32 v39, 1.0, v39\n"
    "v_add_f32 v199, 1.0, v199\n"
    "v_rcp_f32 v35, v35\n"
    "v_rcp_f32 v195, v195\n"
    "v_rcp_f32 v36, v36\n"
    "v_rcp_f32 v196, v196\n"
    "v_rcp_f32 v37, v37\n"
    "v_rcp_f32 v197, v197\n"
    "v_rcp_f32 v38, v38\n"
    "v_rcp_f32 v198, v198\n"
    "v_rcp_f32 v39, v39\n"
    "s_add_u32 s90, s90, 5120\n"
    "s_max_i32 s91, s90, 0\n"
    "buffer_load_dwordx4 v[106:109], v2, s[4:7], s91 offen\n"
    "v_rcp_f32 v199, v199\n"
    "v_mul_f32 v240, v240, v35\n"
    "v_mul_f32 v190, v190, v195\n"
    "v_mul_f32 v241, v241, v36\n"
    "v_mul_f32 v191, v191, v196\n"
    "v_mul_f32 v242, v242, v37\n"
    "v_mul_f32 v192, v192, v197\n"
    "v_mul_f32 v243, v243, v38\n"
    "v_mul_f32 v193, v193, v198\n"
    "v_mul_f32 v244, v244, v39\n"
    "v_mul_f32 v194, v194, v199\n"
    "v_cvt_pk_bf16_f32 v240, v240, 0\n"
    "v_cvt_pk_bf16_f32 v190, v190, 0\n"
    "buffer_load_dword v105, v34, s[4:7], s91 offen offset:1024\n"
    "v_cvt_pk_bf16_f32 v241, v241, 0\n"
    "v_cvt_pk_bf16_f32 v191, v191, 0\n"
    "v_cvt_pk_bf16_f32 v242, v242, 0\n"
    "v_cvt_pk_bf16_f32 v192, v192, 0\n"
    "v_cvt_pk_bf16_f32 v243, v243, 0\n"
    "v_cvt_pk_bf16_f32 v193, v193, 0\n"
    "v_cvt_pk_bf16_f32 v244, v244, 0\n"
    "v_cvt_pk_bf16_f32 v194, v194, 0\n"
    "v_lshlrev_b32 v35, 16, v240\n"
    "v_lshlrev_b32 v195, 16, v190\n"
    "v_lshlrev_b32 v36, 16, v241\n"
    "v_lshlrev_b32 v196, 16, v191\n"
    "v_lshlrev_b32 v37, 16, v242\n"
    "buffer_load_dwordx4 v[116:119], v2, s[4:7], s91 offen offset:2560\n"
    "v_lshlrev_b32 v197, 16, v192\n"
    "v_lshlrev_b32 v38, 16, v243\n"
    "v_lshlrev_b32 v198, 16, v193\n"
    "v_lshlrev_b32 v39, 16, v244\n"
    "v_lshlrev_b32 v199, 16, v194\n"
    "s_waitcnt vmcnt(24)\n"
    "v_dot2_f32_bf16 v245, v200, v240, 0\n"
    "v_dot2_f32_bf16 v246, v200, v35, 0\n"
    "v_dot2_f32_bf16 v247, v201, v240, 0\n"
    "v_dot2_f32_bf16 v248, v201, v35, 0\n"
    "v_dot2_f32_bf16 v249, v202, v240, 0\n"
    "v_dot2_f32_bf16 v250, v202, v35, 0\n"
    "v_dot2_f32_bf16 v251, v203, v240, 0\n"
    "v_dot2_f32_bf16 v252, v203, v35, 0\n"
    "buffer_load_dword v115, v34, s[4:7], s91 offen offset:3584\n"
    "s_waitcnt vmcnt(24)\n"
    "v_dot2_f32_bf16 v245, v204, v190, v245\n"
    "v_dot2_f32_bf16 v246, v204, v195, v246\n"
    "v_dot2_f32_bf16 v247, v205, v190, v247\n"
    "v_dot2_f32_bf16 v248, v205, v195, v248\n"
    "v_dot2_f32_bf16 v249, v206, v190, v249\n"
    "v_dot2_f32_bf16 v250, v206, v195, v250\n"
    "v_dot2_f32_bf16 v251, v207, v190, v251\n"
    "v_dot2_f32_bf16 v252, v207, v195, v252\n"
    "s_waitcnt vmcnt(23)\n"
    "v_dot2_f32_bf16 v245, v208, v241, v245\n"
    "v_dot2_f32_bf16 v246, v208, v36, v246\n"
    "v_dot2_f32_bf16 v247, v209, v241, v247\n"
    "v_dot2_f32_bf16 v248, v209, v36, v248\n"
    "v_dot2_f32_bf16 v249, v210, v241, v249\n"
    "s_add_u32 s90, s90, 5120\n"
    "buffer_load_dwordx4 v[126:129], v2, s[4:7], s90 offen\n"
    "v_dot2_f32_bf16 v250, v210, v36, v250\n"
    "v_dot2_f32_bf16 v251, v211, v241, v251\n"
    "v_dot2_f32_bf16 v252, v211, v36, v252\n"
    "s_waitcnt vmcnt(23)\n"
    "v_dot2_f32_bf16 v245, v212, v191, v245\n"
    "v_dot2_f32_bf16 v246, v212, v196, v246\n"
    "v_dot2_f32_bf16 v247, v213, v191, v247\n"
    "v_dot2_f32_bf16 v248, v213, v196, v248\n"
    "v_dot2_f32_bf16 v249, v214, v191, v249\n"
    "v_dot2_f32_bf16 v250, v214, v196, v250\n"
    "v_dot2_f32_bf16 v251, v215, v191, v251\n"
    "v_dot2_f32_bf16 v252, v215, v196, v252\n"
    "s_waitcnt vmcnt(22)\n"
    "v_dot2_f32_bf16 v245, v216, v242, v245\n"
    "v_dot2_f32_bf16 v246, v216, v37, v246\n"
    "buffer_load_dword v125, v34, s[4:7], s90 offen offset:1024\n"
    "v_dot2_f32_bf16 v247, v217, v242, v247\n"
    "v_dot2_f32_bf16 v248, v217, v37, v248\n"
    "v_dot2_f32_bf16 v249, v218, v242, v249\n"
    "v_dot2_f32_bf16 v250, v218, v37, v250\n"
    "v_dot2_f32_bf16 v251, v219, v242, v251\n"
    "v_dot2_f32_bf16 v252, v219, v37, v252\n"
    "s_waitcnt vmcnt(22)\n"
    "v_dot2_f32_bf16 v245, v220, v192, v245\n"
    "v_dot2_f32_bf16 v246, v220, v197, v246\n"
    "v_dot2_f32_bf16 v247, v221, v192, v247\n"
    "v_dot2_f32_bf16 v248, v221, v197, v248\n"
    "v_dot2_f32_bf16 v249, v222, v192, v249\n"
    "v_dot2_f32_bf16 v250, v222, v197, v250\n"
    "v_dot2_f32_bf16 v251, v223, v192, v251\n"
    "buffer_load_dwordx4 v[136:139], v2, s[4:7], s90 offen offset:2560\n"
    "v_dot2_f32_bf16 v252, v223, v197, v252\n"
    "s_waitcnt vmcnt(22)\n"
    "v_dot2_f32_bf16 v245, v224, v243, v245\n"
    "v_dot2_f32_bf16 v246, v224, v38, v246\n"
    "v_dot2_f32_bf16 v247, v225, v243, v247\n"
    "v_dot2_f32_bf16 v248, v225, v38, v248\n"
    "v_dot2_f32_bf16 v249, v226, v243, v249\n"
    "v_dot2_f32_bf16 v250, v226, v38, v250\n"
    "v_dot2_f32_bf16 v251, v227, v243, v251\n"
    "v_dot2_f32_bf16 v252, v227, v38, v252\n"
    "s_waitcnt vmcnt(21)\n"
    "v_dot2_f32_bf16 v245, v228, v193, v245\n"
    "v_dot2_f32_bf16 v246, v228, v198, v246\n"
    "v_dot2_f32_bf16 v247, v229, v193, v247\n"
    "v_dot2_f32_bf16 v248, v229, v198, v248\n"
    "buffer_load_dword v135, v34, s[4:7], s90 offen offset:3584\n"
    "v_dot2_f32_bf16 v249, v230, v193, v249\n"
    "v_dot2_f32_bf16 v250, v230, v198, v250\n"
    "v_dot2_f32_bf16 v251, v231, v193, v251\n"
    "v_dot2_f32_bf16 v252, v231, v198, v252\n"
    "s_waitcnt vmcnt(21)\n"
    "v_dot2_f32_bf16 v245, v232, v244, v245\n"
    "v_dot2_f32_bf16 v246, v232, v39, v246\n"
    "v_dot2_f32_bf16 v247, v233, v244, v247\n"
    "v_dot2_f32_bf16 v248, v233, v39, v248\n"
    "v_dot2_f32_bf16 v249, v234, v244, v249\n"
    "v_dot2_f32_bf16 v250, v234, v39, v250\n"
    "v_dot2_f32_bf16 v251, v235, v244, v251\n"
    "v_dot2_f32_bf16 v252, v235, v39, v252\n"
    "s_waitcnt vmcnt(20)\n"
    "v_dot2_f32_bf16 v245, v236, v194, v245\n"
    "s_add_u32 s90, s90, 5120\n"
    "buffer_load_dwordx4 v[146:149], v2, s[4:7], s90 offen\n"
    "v_dot2_f32_bf16 v246, v236, v199, v246\n"
    "v_dot2_f32_bf16 v247, v237, v194, v247\n"
    "v_dot2_f32_bf16 v248, v237, v199, v248\n"
    "v_dot2_f32_bf16 v249, v238, v194, v249\n"
    "v_dot2_f32_bf16 v250, v238, v199, v250\n"
    "v_dot2_f32_bf16 v251, v239, v194, v251\n"
    "v_dot2_f32_bf16 v252, v239, v199, v252\n"
    "v_permlane32_swap_b32 v245, v249\n"
    "v_permlane32_swap_b32 v246, v250\n"
    "v_permlane32_swap_b32 v247, v251\n"
    "v_permlane32_swap_b32 v248, v252\n"
    "v_add_f32 v245, v245, v249\n"
    "v_add_f32 v246, v246, v250\n"
    "buffer_load_dword v145, v34, s[4:7], s90 offen offset:1024\n"
    "v_add_f32 v247, v247, v251\n"
    "v_add_f32 v248, v248, v252\n"
    "s_nop 0\n"
    "v_permlane16_swap_b32 v245, v247\n"
    "v_permlane16_swap_b32 v246, v248\n"
    "v_add_f32 v250, v245, v247\n"
    "v_add_f32 v251, v246, v248\n"
    "s_nop 0\n"
    "v_add_f32_dpp v250, v250, v250 row_ror:8 row_mask:0xf bank_mask:0xf\n"
    "v_add_f32_dpp v251, v251, v251 row_ror:8 row_mask:0xf bank_mask:0xf\n"
    "s_nop 0\n"
    "v_add_f32_dpp v250, v250, v250 row_ror:4 row_mask:0xf bank_mask:0xf\n"
    "v_add_f32_dpp v251, v251, v251 row_ror:4 row_mask:0xf bank_mask:0xf\n"
    "ds_write_b64 v19, v[250:251]\n"
    "s_waitcnt lgkmcnt(0)\n"
    "s_barrier\n"
    "ds_read_b128 v[220:223], v18\n"
    "ds_read_b128 v[224:227], v18 offset:128\n"
    "ds_read_b128 v[228:231], v18 offset:256\n"
    "ds_read_b128 v[232:235], v18 offset:384\n"
    "s_waitcnt lgkmcnt(2)\n"
    "v_pk_add_f32 v[220:221], v[220:221], v[224:225]\n"
    "v_pk_add_f32 v[222:223], v[222:223], v[226:227]\n"
    "s_waitcnt lgkmcnt(0)\n"
    "v_pk_add_f32 v[228:229], v[228:229], v[232:233]\n"
    "v_pk_add_f32 v[230:231], v[230:231], v[234:235]\n"
    "v_pk_add_f32 v[220:221], v[220:221], v[228:229]\n"
    "v_pk_add_f32 v[222:223], v[222:223], v[230:231]\n"
    "v_cvt_pk_bf16_f32 v224, v220, v221\n"
    "v_cvt_pk_bf16_f32 v225, v222, v223\n"
    "v_lshlrev_b32 v220, 16, v224\n"
    "v_and_b32 v221, 0xffff0000, v224\n"
    "v_lshlrev_b32 v222, 16, v225\n"
    "v_and_b32 v223, 0xffff0000, v225\n"
    "v_add_f32 v220, s89, v220\n"
    "v_add_f32 v221, s89, v221\n"
    "v_add_f32 v222, s89, v222\n"
    "v_add_f32 v223, s89, v223\n"
    "v_cvt_pk_bf16_f32 v224, v220, v221\n"
    "v_cvt_pk_bf16_f32 v226, v222, v223\n"
    "v_mov_b32 v225, s40\n"
    "v_mov_b32 v227, s40\n"
    "buffer_store_dwordx4 v[224:227], v17, s[16:19], s88 offen sc1\n"
    "buffer_load_dwordx4 v[156:159], v2, s[4:7], s90 offen offset:2560\n"
    "buffer_load_dword v155, v34, s[4:7], s90 offen offset:3584\n"
    "s_add_u32 s90, s90, 5120\n"
    "buffer_load_dwordx4 v[166:169], v2, s[4:7], s90 offen\n"
    "buffer_load_dword v165, v34, s[4:7], s90 offen offset:1024\n"
    "buffer_load_dwordx4 v[176:179], v2, s[4:7], s90 offen offset:2560\n"
    "buffer_load_dword v175, v34, s[4:7], s90 offen offset:3584\n"
    "s_add_u32 s90, s90, 5120\n"
    "buffer_load_dwordx4 v[186:189], v2, s[4:7], s90 offen\n"
    "buffer_load_dword v185, v34, s[4:7], s90 offen offset:1024\n"
    "buffer_load_dwordx4 v[196:199], v2, s[4:7], s90 offen offset:2560\n"
    "buffer_load_dword v195, v34, s[4:7], s90 offen offset:3584\n"
    "s_add_u32 s90, s90, 5120\n"
    "buffer_load_dwordx4 v[206:209], v2, s[4:7], s90 offen\n"
    "buffer_load_dword v205, v34, s[4:7], s90 offen offset:1024\n"
    "buffer_load_dwordx4 v[216:219], v2, s[4:7], s90 offen offset:2560\n"
    "buffer_load_dword v215, v34, s[4:7], s90 offen offset:3584\n"
    "buffer_load_dwordx4 a[40:43], v11, s[20:23], s76 offen\n"
    "buffer_load_dwordx4 a[44:47], v11, s[20:23], s77 offen\n"
    "s_waitcnt vmcnt(37)\n"
    "v_dot2_f32_bf16 v40, v45, s43, 0\n"
    "v_dot2_f32_bf16 v41, v45, v45, 0\n"
    "s_waitcnt vmcnt(35)\n"
    "v_dot2_f32_bf16 v50, v55, s43, 0\n"
    "v_dot2_f32_bf16 v51, v55, v55, 0\n"
    "v_dot2_f32_bf16 v40, v46, s43, v40\n"
    "v_dot2_f32_bf16 v41, v46, v46, v41\n"
    "v_dot2_f32_bf16 v50, v56, s43, v50\n"
    "v_dot2_f32_bf16 v51, v56, v56, v51\n"
    "v_dot2_f32_bf16 v40, v47, s43, v40\n"
    "v_dot2_f32_bf16 v41, v47, v47, v41\n"
    "v_dot2_f32_bf16 v50, v57, s43, v50\n"
    "v_dot2_f32_bf16 v51, v57, v57, v51\n"
    "v_dot2_f32_bf16 v40, v48, s43, v40\n"
    "v_dot2_f32_bf16 v41, v48, v48, v41\n"
    "v_dot2_f32_bf16 v50, v58, s43, v50\n"
    "v_dot2_f32_bf16 v51, v58, v58, v51\n"
    "v_dot2_f32_bf16 v40, v49, s43, v40\n"
    "v_dot2_f32_bf16 v41, v49, v49, v41\n"
    "v_dot2_f32_bf16 v50, v59, s43, v50\n"
    "v_dot2_f32_bf16 v51, v59, v59, v51\n"
    "v_lshlrev_b32 v42, 16, v46\n"
    "v_and_b32 v43, 0xffff0000, v46\n"
    "v_lshlrev_b32 v52, 16, v56\n"
    "v_and_b32 v53, 0xffff0000, v56\n"
    "v_permlane32_swap_b32 v40, v41\n"
    "v_permlane32_swap_b32 v50, v51\n"
    "v_add_f32 v40, v40, v41\n"
    "v_add_f32 v50, v50, v51\n"
    "v_lshlrev_b32 v44, 16, v47\n"
    "v_lshlrev_b32 v54, 16, v57\n"
    "v_permlane16_swap_b32 v40, v50\n"
    "v_add_f32 v24, v40, v50\n"
    "v_lshlrev_b32 v40, 16, v45\n"
    "v_and_b32 v41, 0xffff0000, v45\n"
    "v_and_b32 v45, 0xffff0000, v47\n"
    "v_lshlrev_b32 v46, 16, v48\n"
    "v_and_b32 v47, 0xffff0000, v48\n"
    "v_lshlrev_b32 v48, 16, v49\n"
    "v_and_b32 v49, 0xffff0000, v49\n"
    "v_lshlrev_b32 v50, 16, v55\n"
    "v_and_b32 v51, 0xffff0000, v55\n"
    "v_and_b32 v55, 0xffff0000, v57\n"
    "v_lshlrev_b32 v56, 16, v58\n"
    "v_and_b32 v57, 0xffff0000, v58\n"
    "v_lshlrev_b32 v58, 16, v59\n"
    "v_and_b32 v59, 0xffff0000, v59\n"
    "s_waitcnt vmcnt(33)\n"
    "v_dot2_f32_bf16 v60, v65, s43, 0\n"
    "v_dot2_f32_bf16 v61, v65, v65, 0\n"
    "s_waitcnt vmcnt(31)\n"
    "v_dot2_f32_bf16 v70, v75, s43, 0\n"
    "v_dot2_f32_bf16 v71, v75, v75, 0\n"
    "v_dot2_f32_bf16 v60, v66, s43, v60\n"
    "v_dot2_f32_bf16 v61, v66, v66, v61\n"
    "v_dot2_f32_bf16 v70, v76, s43, v70\n"
    "v_dot2_f32_bf16 v71, v76, v76, v71\n"
    "v_dot2_f32_bf16 v60, v67, s43, v60\n"
    "v_dot2_f32_bf16 v61, v67, v67, v61\n"
    "v_dot2_f32_bf16 v70, v77, s43, v70\n"
    "v_dot2_f32_bf16 v71, v77, v77, v71\n"
    "v_dot2_f32_bf16 v60, v68, s43, v60\n"
    "v_dot2_f32_bf16 v61, v68, v68, v61\n"
    "v_dot2_f32_bf16 v70, v78, s43, v70\n"
    "v_dot2_f32_bf16 v71, v78, v78, v71\n"
    "v_dot2_f32_bf16 v60, v69, s43, v60\n"
    "v_dot2_f32_bf16 v61, v69, v69, v61\n"
    "v_dot2_f32_bf16 v70, v79, s43, v70\n"
    "v_dot2_f32_bf16 v71, v79, v79, v71\n"
    "v_lshlrev_b32 v62, 16, v66\n"
    "v_and_b32 v63, 0xffff0000, v66\n"
    "v_lshlrev_b32 v72, 16, v76\n"
    "v_and_b32 v73, 0xffff0000, v76\n"
    "v_permlane32_swap_b32 v60, v61\n"
    "v_permlane32_swap_b32 v70, v71\n"
    "v_add_f32 v60, v60, v61\n"
    "v_add_f32 v70, v70, v71\n"
    "v_lshlrev_b32 v64, 16, v67\n"
    "v_lshlrev_b32 v74, 16, v77\n"
    "v_permlane16_swap_b32 v60, v70\n"
    "v_add_f32 v25, v60, v70\n"
    "v_lshlrev_b32 v60, 16, v65\n"
    "v_and_b32 v61, 0xffff0000, v65\n"
    "v_and_b32 v65, 0xffff0000, v67\n"
    "v_lshlrev_b32 v66, 16, v68\n"
    "v_and_b32 v67, 0xffff0000, v68\n"
    "v_lshlrev_b32 v68, 16, v69\n"
    "v_and_b32 v69, 0xffff0000, v69\n"
    "v_lshlrev_b32 v70, 16, v75\n"
    "v_and_b32 v71, 0xffff0000, v75\n"
    "v_and_b32 v75, 0xffff0000, v77\n"
    "v_lshlrev_b32 v76, 16, v78\n"
    "v_and_b32 v77, 0xffff0000, v78\n"
    "v_lshlrev_b32 v78, 16, v79\n"
    "v_and_b32 v79, 0xffff0000, v79\n"
    "buffer_load_dword v11, v10, s[16:19], 0 offen sc1\n"
    "s_waitcnt vmcnt(30)\n"
    "v_dot2_f32_bf16 v80, v85, s43, 0\n"
    "v_dot2_f32_bf16 v81, v85, v85, 0\n"
    "s_waitcnt vmcnt(28)\n"
    "v_dot2_f32_bf16 v90, v95, s43, 0\n"
    "v_dot2_f32_bf16 v91, v95, v95, 0\n"
    "v_dot2_f32_bf16 v80, v86, s43, v80\n"
    "v_dot2_f32_bf16 v81, v86, v86, v81\n"
    "v_dot2_f32_bf16 v90, v96, s43, v90\n"
    "v_dot2_f32_bf16 v91, v96, v96, v91\n"
    "v_dot2_f32_bf16 v80, v87, s43, v80\n"
    "v_dot2_f32_bf16 v81, v87, v87, v81\n"
    "v_dot2_f32_bf16 v90, v97, s43, v90\n"
    "v_dot2_f32_bf16 v91, v97, v97, v91\n"
    "v_dot2_f32_bf16 v80, v88, s43, v80\n"
    "v_dot2_f32_bf16 v81, v88, v88, v81\n"
    "v_dot2_f32_bf16 v90, v98, s43, v90\n"
    "v_dot2_f32_bf16 v91, v98, v98, v91\n"
    "v_dot2_f32_bf16 v80, v89, s43, v80\n"
    "v_dot2_f32_bf16 v81, v89, v89, v81\n"
    "v_dot2_f32_bf16 v90, v99, s43, v90\n"
    "v_dot2_f32_bf16 v91, v99, v99, v91\n"
    "v_lshlrev_b32 v82, 16, v86\n"
    "v_and_b32 v83, 0xffff0000, v86\n"
    "v_lshlrev_b32 v92, 16, v96\n"
    "v_and_b32 v93, 0xffff0000, v96\n"
    "v_permlane32_swap_b32 v80, v81\n"
    "v_permlane32_swap_b32 v90, v91\n"
    "v_add_f32 v80, v80, v81\n"
    "v_add_f32 v90, v90, v91\n"
    "v_lshlrev_b32 v84, 16, v87\n"
    "v_lshlrev_b32 v94, 16, v97\n"
    "v_permlane16_swap_b32 v80, v90\n"
    "v_add_f32 v26, v80, v90\n"
    "v_lshlrev_b32 v80, 16, v85\n"
    "v_and_b32 v81, 0xffff0000, v85\n"
    "v_and_b32 v85, 0xffff0000, v87\n"
    "v_lshlrev_b32 v86, 16, v88\n"
    "v_and_b32 v87, 0xffff0000, v88\n"
    "v_lshlrev_b32 v88, 16, v89\n"
    "v_and_b32 v89, 0xffff0000, v89\n"
    "v_lshlrev_b32 v90, 16, v95\n"
    "v_and_b32 v91, 0xffff0000, v95\n"
    "v_and_b32 v95, 0xffff0000, v97\n"
    "v_lshlrev_b32 v96, 16, v98\n"
    "v_and_b32 v97, 0xffff0000, v98\n"
    "v_lshlrev_b32 v98, 16, v99\n"
    "v_and_b32 v99, 0xffff0000, v99\n"
    "s_waitcnt vmcnt(26)\n"
    "v_dot2_f32_bf16 v100, v105, s43, 0\n"
    "v_dot2_f32_bf16 v101, v105, v105, 0\n"
    "s_waitcnt vmcnt(24)\n"
    "v_dot2_f32_bf16 v110, v115, s43, 0\n"
    "v_dot2_f32_bf16 v111, v115, v115, 0\n"
    "v_dot2_f32_bf16 v100, v106, s43, v100\n"
    "v_dot2_f32_bf16 v101, v106, v106, v101\n"
    "v_dot2_f32_bf16 v110, v116, s43, v110\n"
    "v_dot2_f32_bf16 v111, v116, v116, v111\n"
    "v_dot2_f32_bf16 v100, v107, s43, v100\n"
    "v_dot2_f32_bf16 v101, v107, v107, v101\n"
    "v_dot2_f32_bf16 v110, v117, s43, v110\n"
    "v_dot2_f32_bf16 v111, v117, v117, v111\n"
    "v_dot2_f32_bf16 v100, v108, s43, v100\n"
    "v_dot2_f32_bf16 v101, v108, v108, v101\n"
    "v_dot2_f32_bf16 v110, v118, s43, v110\n"
    "v_dot2_f32_bf16 v111, v118, v118, v111\n"
    "v_dot2_f32_bf16 v100, v109, s43, v100\n"
    "v_dot2_f32_bf16 v101, v109, v109, v101\n"
    "v_dot2_f32_bf16 v110, v119, s43, v110\n"
    "v_dot2_f32_bf16 v111, v119, v119, v111\n"
    "v_lshlrev_b32 v102, 16, v106\n"
    "v_and_b32 v103, 0xffff0000, v106\n"
    "v_lshlrev_b32 v112, 16, v116\n"
    "v_and_b32 v113, 0xffff0000, v116\n"
    "v_permlane32_swap_b32 v100, v101\n"
    "v_permlane32_swap_b32 v110, v111\n"
    "v_add_f32 v100, v100, v101\n"
    "v_add_f32 v110, v110, v111\n"
    "v_lshlrev_b32 v104, 16, v107\n"
    "v_lshlrev_b32 v114, 16, v117\n"
    "v_permlane16_swap_b32 v100, v110\n"
    "v_add_f32 v27, v100, v110\n"
    "v_lshlrev_b32 v100, 16, v105\n"
    "v_and_b32 v101, 0xffff0000, v105\n"
    "v_and_b32 v105, 0xffff0000, v107\n"
    "v_lshlrev_b32 v106, 16, v108\n"
    "v_and_b32 v107, 0xffff0000, v108\n"
    "v_lshlrev_b32 v108, 16, v109\n"
    "v_and_b32 v109, 0xffff0000, v109\n"
    "v_lshlrev_b32 v110, 16, v115\n"
    "v_and_b32 v111, 0xffff0000, v115\n"
    "v_and_b32 v115, 0xffff0000, v117\n"
    "v_lshlrev_b32 v116, 16, v118\n"
    "v_and_b32 v117, 0xffff0000, v118\n"
    "v_lshlrev_b32 v118, 16, v119\n"
    "v_and_b32 v119, 0xffff0000, v119\n"
    "s_waitcnt vmcnt(22)\n"
    "v_dot2_f32_bf16 v120, v125, s43, 0\n"
    "v_dot2_f32_bf16 v121, v125, v125, 0\n"
    "s_waitcnt vmcnt(20)\n"
    "v_dot2_f32_bf16 v130, v135, s43, 0\n"
    "v_dot2_f32_bf16 v131, v135, v135, 0\n"
    "v_dot2_f32_bf16 v120, v126, s43, v120\n"
    "v_dot2_f32_bf16 v121, v126, v126, v121\n"
    "v_dot2_f32_bf16 v130, v136, s43, v130\n"
    "v_dot2_f32_bf16 v131, v136, v136, v131\n"
    "v_dot2_f32_bf16 v120, v127, s43, v120\n"
    "v_dot2_f32_bf16 v121, v127, v127, v121\n"
    "v_dot2_f32_bf16 v130, v137, s43, v130\n"
    "v_dot2_f32_bf16 v131, v137, v137, v131\n"
    "v_dot2_f32_bf16 v120, v128, s43, v120\n"
    "v_dot2_f32_bf16 v121, v128, v128, v121\n"
    "v_dot2_f32_bf16 v130, v138, s43, v130\n"
    "v_dot2_f32_bf16 v131, v138, v138, v131\n"
    "v_dot2_f32_bf16 v120, v129, s43, v120\n"
    "v_dot2_f32_bf16 v121, v129, v129, v121\n"
    "v_dot2_f32_bf16 v130, v139, s43, v130\n"
    "v_dot2_f32_bf16 v131, v139, v139, v131\n"
    "v_lshlrev_b32 v122, 16, v126\n"
    "v_and_b32 v123, 0xffff0000, v126\n"
    "v_lshlrev_b32 v132, 16, v136\n"
    "v_and_b32 v133, 0xffff0000, v136\n"
    "v_permlane32_swap_b32 v120, v121\n"
    "v_permlane32_swap_b32 v130, v131\n"
    "v_add_f32 v120, v120, v121\n"
    "v_add_f32 v130, v130, v131\n"
    "v_lshlrev_b32 v124, 16, v127\n"
    "v_lshlrev_b32 v134, 16, v137\n"
    "v_permlane16_swap_b32 v120, v130\n"
    "v_add_f32 v28, v120, v130\n"
    "v_lshlrev_b32 v120, 16, v125\n"
    "v_and_b32 v121, 0xffff0000, v125\n"
    "v_and_b32 v125, 0xffff0000, v127\n"
    "v_lshlrev_b32 v126, 16, v128\n"
    "v_and_b32 v127, 0xffff0000, v128\n"
    "v_lshlrev_b32 v128, 16, v129\n"
    "v_and_b32 v129, 0xffff0000, v129\n"
    "v_lshlrev_b32 v130, 16, v135\n"
    "v_and_b32 v131, 0xffff0000, v135\n"
    "v_and_b32 v135, 0xffff0000, v137\n"
    "v_lshlrev_b32 v136, 16, v138\n"
    "v_and_b32 v137, 0xffff0000, v138\n"
    "v_lshlrev_b32 v138, 16, v139\n"
    "v_and_b32 v139, 0xffff0000, v139\n"
    "s_waitcnt vmcnt(18)\n"
    "v_dot2_f32_bf16 v140, v145, s43, 0\n"
    "v_dot2_f32_bf16 v141, v145, v145, 0\n"
    "s_waitcnt vmcnt(15)\n"
    "v_dot2_f32_bf16 v150, v155, s43, 0\n"
    "v_dot2_f32_bf16 v151, v155, v155, 0\n"
    "v_dot2_f32_bf16 v140, v146, s43, v140\n"
    "v_dot2_f32_bf16 v141, v146, v146, v141\n"
    "v_dot2_f32_bf16 v150, v156, s43, v150\n"
    "v_dot2_f32_bf16 v151, v156, v156, v151\n"
    "v_dot2_f32_bf16 v140, v147, s43, v140\n"
    "v_dot2_f32_bf16 v141, v147, v147, v141\n"
    "v_dot2_f32_bf16 v150, v157, s43, v150\n"
    "v_dot2_f32_bf16 v151, v157, v157, v151\n"
    "v_dot2_f32_bf16 v140, v148, s43, v140\n"
    "v_dot2_f32_bf16 v141, v148, v148, v141\n"
    "v_dot2_f32_bf16 v150, v158, s43, v150\n"
    "v_dot2_f32_bf16 v151, v158, v158, v151\n"
    "v_dot2_f32_bf16 v140, v149, s43, v140\n"
    "v_dot2_f32_bf16 v141, v149, v149, v141\n"
    "v_dot2_f32_bf16 v150, v159, s43, v150\n"
    "v_dot2_f32_bf16 v151, v159, v159, v151\n"
    "v_lshlrev_b32 v142, 16, v146\n"
    "v_and_b32 v143, 0xffff0000, v146\n"
    "v_lshlrev_b32 v152, 16, v156\n"
    "v_and_b32 v153, 0xffff0000, v156\n"
    "v_permlane32_swap_b32 v140, v141\n"
    "v_permlane32_swap_b32 v150, v151\n"
    "v_add_f32 v140, v140, v141\n"
    "v_add_f32 v150, v150, v151\n"
    "v_lshlrev_b32 v144, 16, v147\n"
    "v_lshlrev_b32 v154, 16, v157\n"
    "v_permlane16_swap_b32 v140, v150\n"
    "v_add_f32 v29, v140, v150\n"
    "v_lshlrev_b32 v140, 16, v145\n"
    "v_and_b32 v141, 0xffff0000, v145\n"
    "v_and_b32 v145, 0xffff0000, v147\n"
    "v_lshlrev_b32 v146, 16, v148\n"
    "v_and_b32 v147, 0xffff0000, v148\n"
    "v_lshlrev_b32 v148, 16, v149\n"
    "v_and_b32 v149, 0xffff0000, v149\n"
    "v_lshlrev_b32 v150, 16, v155\n"
    "v_and_b32 v151, 0xffff0000, v155\n"
    "v_and_b32 v155, 0xffff0000, v157\n"
    "v_lshlrev_b32 v156, 16, v158\n"
    "v_and_b32 v157, 0xffff0000, v158\n"
    "v_lshlrev_b32 v158, 16, v159\n"
    "v_and_b32 v159, 0xffff0000, v159\n"
    "s_waitcnt vmcnt(13)\n"
    "v_dot2_f32_bf16 v160, v165, s43, 0\n"
    "v_dot2_f32_bf16 v161, v165, v165, 0\n"
    "s_waitcnt vmcnt(11)\n"
    "v_dot2_f32_bf16 v170, v175, s43, 0\n"
    "v_dot2_f32_bf16 v171, v175, v175, 0\n"
    "v_dot2_f32_bf16 v160, v166, s43, v160\n"
    "v_dot2_f32_bf16 v161, v166, v166, v161\n"
    "v_dot2_f32_bf16 v170, v176, s43, v170\n"
    "v_dot2_f32_bf16 v171, v176, v176, v171\n"
    "v_dot2_f32_bf16 v160, v167, s43, v160\n"
    "v_dot2_f32_bf16 v161, v167, v167, v161\n"
    "v_dot2_f32_bf16 v170, v177, s43, v170\n"
    "v_dot2_f32_bf16 v171, v177, v177, v171\n"
    "v_dot2_f32_bf16 v160, v168, s43, v160\n"
    "v_dot2_f32_bf16 v161, v168, v168, v161\n"
    "v_dot2_f32_bf16 v170, v178, s43, v170\n"
    "v_dot2_f32_bf16 v171, v178, v178, v171\n"
    "v_dot2_f32_bf16 v160, v169, s43, v160\n"
    "v_dot2_f32_bf16 v161, v169, v169, v161\n"
    "v_dot2_f32_bf16 v170, v179, s43, v170\n"
    "v_dot2_f32_bf16 v171, v179, v179, v171\n"
    "v_lshlrev_b32 v162, 16, v166\n"
    "v_and_b32 v163, 0xffff0000, v166\n"
    "v_lshlrev_b32 v172, 16, v176\n"
    "v_and_b32 v173, 0xffff0000, v176\n"
    "v_permlane32_swap_b32 v160, v161\n"
    "v_permlane32_swap_b32 v170, v171\n"
    "v_add_f32 v160, v160, v161\n"
    "v_add_f32 v170, v170, v171\n"
    "v_lshlrev_b32 v164, 16, v167\n"
    "v_lshlrev_b32 v174, 16, v177\n"
    "v_permlane16_swap_b32 v160, v170\n"
    "v_add_f32 v30, v160, v170\n"
    "v_lshlrev_b32 v160, 16, v165\n"
    "v_and_b32 v161, 0xffff0000, v165\n"
    "v_and_b32 v165, 0xffff0000, v167\n"
    "v_lshlrev_b32 v166, 16, v168\n"
    "v_and_b32 v167, 0xffff0000, v168\n"
    "v_lshlrev_b32 v168, 16, v169\n"
    "v_and_b32 v169, 0xffff0000, v169\n"
    "v_lshlrev_b32 v170, 16, v175\n"
    "v_and_b32 v171, 0xffff0000, v175\n"
    "v_and_b32 v175, 0xffff0000, v177\n"
    "v_lshlrev_b32 v176, 16, v178\n"
    "v_and_b32 v177, 0xffff0000, v178\n"
    "v_lshlrev_b32 v178, 16, v179\n"
    "v_and_b32 v179, 0xffff0000, v179\n"
    "s_waitcnt vmcnt(2)\n"
    "ds_write_b128 v22, a[40:43] offset:5120\n"
    "s_waitcnt vmcnt(1)\n"
    "ds_write_b128 v22, a[44:47] offset:6400\n"
    "ds_read_b64_tr_b16 a[236:237], v21 offset:5120\n"
    "ds_read_b64_tr_b16 a[238:239], v21 offset:5440\n"
    "ds_read_b64_tr_b16 a[240:241], v20 offset:5120\n"
    "ds_read_b64_tr_b16 a[242:243], v20 offset:5440\n"
    "s_waitcnt vmcnt(0)\n"
    "s_waitcnt lgkmcnt(0)\n"
    "s_nop 1\n"
    "s_branch .Ljoin_%=\n"
    ".Lr3_%=:\n"
    "s_cmp_eq_u32 s42, 0\n"
    "s_cbranch_scc1 .Ld3_%=\n"
    "s_sub_u32 s42, s42, 1\n"
    "buffer_load_dwordx2 v[34:35], v32, s[16:19], 0 offen sc1\n"
    "buffer_load_dword v36, v33, s[16:19], 0 offen sc1\n"
    "buffer_load_dwordx4 v[196:199], v208, s[16:19], s46 offen offset:1024 sc1\n"
    "buffer_load_dwordx4 v[200:203], v208, s[16:19], s47 offen offset:1024 sc1\n"
    "buffer_load_dwordx4 v[204:207], v209, s[16:19], s48 offen offset:1024 sc1\n"
    "s_waitcnt vmcnt(0)\n"
    "v_xor_b32 v37, s40, v34\n"
    "v_bitop3_b32 v37, v37, v35, s40 bitop3:0xf6\n"
    "v_bitop3_b32 v37, v37, v36, s40 bitop3:0xf6\n"
    "v_bitop3_b32 v37, v37, v197, s40 bitop3:0xf6\n"
    "v_bitop3_b32 v37, v37, v199, s40 bitop3:0xf6\n"
    "v_bitop3_b32 v37, v37, v201, s40 bitop3:0xf6\n"
    "v_bitop3_b32 v37, v37, v203, s40 bitop3:0xf6\n"
    "v_bitop3_b32 v37, v37, v205, s40 bitop3:0xf6\n"
    "v_bitop3_b32 v37, v37, v207, s40 bitop3:0xf6\n"
    "v_cmp_ne_u32 vcc, 0, v37\n"
    "s_nop 1\n"
    "s_cbranch_vccnz .Lr3_%=\n"
    "s_branch .Ld3_%=\n"
    ".Lr5_%=:\n"
    "s_cmp_eq_u32 s42, 0\n"
    "s_cbranch_scc1 .Ld5_%=\n"
    "s_sub_u32 s42, s42, 1\n"
    "buffer_load_dwordx4 v[148:151], v2, s[16:19], s65 offen sc1\n"
    "buffer_load_dwordx4 v[152:155], v2, s[16:19], s65 offen offset:1024 sc1\n"
    "buffer_load_dwordx4 v[156:159], v2, s[16:19], s65 offen offset:2048 sc1\n"
    "buffer_load_dwordx4 v[160:163], v2, s[16:19], s65 offen offset:3072 sc1\n"
    "buffer_load_dwordx2 v[164:165], v11, s[16:19], s65 offen sc1\n"
    "buffer_load_dwordx2 v[166:167], v11, s[16:19], s65 offen offset:512 sc1\n"
    "s_waitcnt vmcnt(0)\n"
    "v_max3_u32 v24, v148, v152, v156\n"
    "v_max3_u32 v24, v24, v160, v164\n"
    "v_max_u32 v24, v24, v166\n"
    "v_cmp_eq_u32 vcc, -1, v24\n"
    "s_nop 1\n"
    "s_cbranch_vccnz .Lr5_%=\n"
    "s_branch .Ld5_%=\n"
    ".Lr6_%=:\n"
    "s_cmp_eq_u32 s42, 0\n"
    "s_cbranch_scc1 .Ld6_%=\n"
    "s_sub_u32 s42, s42, 1\n"
    "buffer_load_dwordx4 v[168:171], v2, s[16:19], s66 offen sc1\n"
    "buffer_load_dwordx4 v[172:175], v2, s[16:19], s66 offen offset:1024 sc1\n"
    "buffer_load_dwordx4 v[176:179], v2, s[16:19], s66 offen offset:2048 sc1\n"
    "buffer_load_dwordx4 v[180:183], v2, s[16:19], s66 offen offset:3072 sc1\n"
    "buffer_load_dwordx2 v[184:185], v11, s[16:19], s66 offen sc1\n"
    "buffer_load_dwordx2 v[186:187], v11, s[16:19], s66 offen offset:512 sc1\n"
    "s_waitcnt vmcnt(0)\n"
    "v_max3_u32 v24, v168, v172, v176\n"
    "v_max3_u32 v24, v24, v180, v184\n"
    "v_max_u32 v24, v24, v186\n"
    "v_cmp_eq_u32 vcc, -1, v24\n"
    "s_nop 1\n"
    "s_cbranch_vccnz .Lr6_%=\n"
    "s_branch .Ld6_%=\n"
    ".Lr7_%=:\n"
    "s_cmp_eq_u32 s42, 0\n"
    "s_cbranch_scc1 .Ld7_%=\n"
    "s_sub_u32 s42, s42, 1\n"
    "buffer_load_dwordx4 v[188:191], v2, s[16:19], s67 offen sc1\n"
    "buffer_load_dwordx4 v[192:195], v2, s[16:19], s67 offen offset:1024 sc1\n"
    "buffer_load_dwordx4 v[196:199], v2, s[16:19], s67 offen offset:2048 sc1\n"
    "buffer_load_dwordx4 v[200:203], v2, s[16:19], s67 offen offset:3072 sc1\n"
    "buffer_load_dwordx2 v[204:205], v11, s[16:19], s67 offen sc1\n"
    "buffer_load_dwordx2 v[206:207], v11, s[16:19], s67 offen offset:512 sc1\n"
    "s_waitcnt vmcnt(0)\n"
    "v_max3_u32 v24, v188, v192, v196\n"
    "v_max3_u32 v24, v24, v200, v204\n"
    "v_max_u32 v24, v24, v206\n"
    "v_cmp_eq_u32 vcc, -1, v24\n"
    "s_nop 1\n"
    "s_cbranch_vccnz .Lr7_%=\n"
    "s_branch .Ld7_%=\n"
    ".Lr9_%=:\n"
    "s_cmp_eq_u32 s42, 0\n"
    "s_cbranch_scc1 .Ld9_%=\n"
    "s_sub_u32 s42, s42, 1\n"
    "buffer_load_dwordx4 v[148:151], v2, s[16:19], s69 offen sc1\n"
    "buffer_load_dwordx4 v[152:155], v2, s[16:19], s69 offen offset:1024 sc1\n"
    "buffer_load_dwordx4 v[156:159], v2, s[16:19], s69 offen offset:2048 sc1\n"
    "buffer_load_dwordx4 v[160:163], v2, s[16:19], s69 offen offset:3072 sc1\n"
    "buffer_load_dwordx4 v[164:167], v10, s[16:19], s69 offen sc1\n"
    "buffer_load_dwordx2 v[120:121], v11, s[16:19], s77 offen sc1\n"
    "buffer_load_dwordx2 v[122:123], v11, s[16:19], s77 offen offset:512 sc1\n"
    "s_waitcnt vmcnt(0)\n"
    "v_max3_u32 v24, v148, v152, v156\n"
    "v_max3_u32 v24, v24, v160, v164\n"
    "v_max3_u32 v24, v24, v120, v122\n"
    "v_cmp_eq_u32 vcc, -1, v24\n"
    "s_nop 1\n"
    "s_cbranch_vccnz .Lr9_%=\n"
    "s_branch .Ld9_%=\n"
    ".Lr10_%=:\n"
    "s_cmp_eq_u32 s42, 0\n"
    "s_cbranch_scc1 .Ld10_%=\n"
    "s_sub_u32 s42, s42, 1\n"
    "buffer_load_dwordx4 v[168:171], v2, s[16:19], s70 offen sc1\n"
    "buffer_load_dwordx4 v[172:175], v2, s[16:19], s70 offen offset:1024 sc1\n"
    "buffer_load_dwordx4 v[176:179], v2, s[16:19], s70 offen offset:2048 sc1\n"
    "buffer_load_dwordx4 v[180:183], v2, s[16:19], s70 offen offset:3072 sc1\n"
    "buffer_load_dwordx4 v[184:187], v10, s[16:19], s70 offen sc1\n"
    "s_waitcnt vmcnt(0)\n"
    "v_max3_u32 v120, v168, v172, v176\n"
    "v_max3_u32 v120, v120, v180, v184\n"
    "v_cmp_eq_u32 vcc, -1, v120\n"
    "s_nop 1\n"
    "s_cbranch_vccnz .Lr10_%=\n"
    "s_branch .Ld10_%=\n"
    ".Lr11_%=:\n"
    "s_cmp_eq_u32 s42, 0\n"
    "s_cbranch_scc1 .Ld11_%=\n"
    "s_sub_u32 s42, s42, 1\n"
    "buffer_load_dwordx4 v[188:191], v2, s[16:19], s71 offen sc1\n"
    "buffer_load_dwordx4 v[192:195], v2, s[16:19], s71 offen offset:1024 sc1\n"
    "buffer_load_dwordx4 v[196:199], v2, s[16:19], s71 offen offset:2048 sc1\n"
    "buffer_load_dwordx4 v[200:203], v2, s[16:19], s71 offen offset:3072 sc1\n"
    "buffer_load_dwordx4 v[204:207], v10, s[16:19], s71 offen sc1\n"
    "s_waitcnt vmcnt(0)\n"
    "v_max3_u32 v120, v188, v192, v196\n"
    "v_max3_u32 v120, v120, v200, v204\n"
    "v_cmp_eq_u32 vcc, -1, v120\n"
    "s_nop 1\n"
    "s_cbranch_vccnz .Lr11_%=\n"
    "s_branch .Ld11_%=\n"
    ".Lr12_%=:\n"
    "s_cmp_eq_u32 s42, 0\n"
    "s_cbranch_scc1 .Ld12_%=\n"
    "s_sub_u32 s42, s42, 1\n"
    "buffer_load_dwordx4 a[68:71], v2, s[16:19], s73 offen sc1\n"
    "buffer_load_dwordx4 a[72:75], v2, s[16:19], s73 offen offset:1024 sc1\n"
    "buffer_load_dwordx4 a[76:79], v2, s[16:19], s73 offen offset:2048 sc1\n"
    "buffer_load_dwordx4 a[80:83], v2, s[16:19], s73 offen offset:3072 sc1\n"
    "buffer_load_dwordx4 a[84:87], v10, s[16:19], s73 offen sc1\n"
    "buffer_load_dwordx4 a[88:91], v10, s[16:19], s73 offen offset:1024 sc1\n"
    "buffer_load_dwordx4 a[92:95], v10, s[16:19], s73 offen offset:2048 sc1\n"
    "buffer_load_dwordx4 a[96:99], v10, s[16:19], s73 offen offset:3072 sc1\n"
    "buffer_load_dwordx4 a[100:103], v9, s[16:19], s73 offen sc1\n"
    "buffer_load_dwordx4 a[104:107], v9, s[16:19], s73 offen offset:1024 sc1\n"
    "buffer_load_dwordx4 a[108:111], v9, s[16:19], s73 offen offset:2048 sc1\n"
    "buffer_load_dwordx4 a[112:115], v9, s[16:19], s73 offen offset:3072 sc1\n"
    "s_waitcnt vmcnt(0)\n"
    "v_accvgpr_read_b32 v24, a68\n"
    "v_accvgpr_read_b32 v25, a72\n"
    "v_accvgpr_read_b32 v26, a76\n"
    "v_max3_u32 v24, v24, v25, v26\n"
    "v_accvgpr_read_b32 v25, a80\n"
    "v_accvgpr_read_b32 v26, a84\n"
    "v_max3_u32 v24, v24, v25, v26\n"
    "v_accvgpr_read_b32 v25, a88\n"
    "v_accvgpr_read_b32 v26, a92\n"
    "v_max3_u32 v24, v24, v25, v26\n"
    "v_accvgpr_read_b32 v25, a96\n"
    "v_accvgpr_read_b32 v26, a100\n"
    "v_max3_u32 v24, v24, v25, v26\n"
    "v_accvgpr_read_b32 v25, a104\n"
    "v_accvgpr_read_b32 v26, a108\n"
    "v_max3_u32 v24, v24, v25, v26\n"
    "v_accvgpr_read_b32 v25, a112\n"
    "v_max_u32 v24, v24, v25\n"
    "v_cmp_eq_u32 vcc, -1, v24\n"
    "s_nop 1\n"
    "s_cbranch_vccnz .Lr12_%=\n"
    "s_branch .Ld12_%=\n"
    ".Lr13_%=:\n"
    "s_cmp_eq_u32 s42, 0\n"
    "s_cbranch_scc1 .Ld13_%=\n"
    "s_sub_u32 s42, s42, 1\n"
    "buffer_load_dwordx4 a[116:119], v2, s[16:19], s74 offen sc1\n"
    "buffer_load_dwordx4 a[120:123], v2, s[16:19], s74 offen offset:1024 sc1\n"
    "buffer_load_dwordx4 a[124:127], v2, s[16:19], s74 offen offset:2048 sc1\n"
    "buffer_load_dwordx4 a[128:131], v2, s[16:19], s74 offen offset:3072 sc1\n"
    "buffer_load_dwordx4 a[132:135], v10, s[16:19], s74 offen sc1\n"
    "buffer_load_dwordx4 a[136:139], v10, s[16:19], s74 offen offset:1024 sc1\n"
    "buffer_load_dwordx4 a[140:143], v10, s[16:19], s74 offen offset:2048 sc1\n"
    "buffer_load_dwordx4 a[144:147], v10, s[16:19], s74 offen offset:3072 sc1\n"
    "buffer_load_dwordx4 a[164:167], v9, s[16:19], s74 offen sc1\n"
    "buffer_load_dwordx4 a[168:171], v9, s[16:19], s74 offen offset:1024 sc1\n"
    "buffer_load_dwordx4 a[172:175], v9, s[16:19], s74 offen offset:2048 sc1\n"
    "buffer_load_dwordx4 a[176:179], v9, s[16:19], s74 offen offset:3072 sc1\n"
    "s_waitcnt vmcnt(0)\n"
    "v_accvgpr_read_b32 v24, a116\n"
    "v_accvgpr_read_b32 v25, a120\n"
    "v_accvgpr_read_b32 v26, a124\n"
    "v_max3_u32 v24, v24, v25, v26\n"
    "v_accvgpr_read_b32 v25, a128\n"
    "v_accvgpr_read_b32 v26, a132\n"
    "v_max3_u32 v24, v24, v25, v26\n"
    "v_accvgpr_read_b32 v25, a136\n"
    "v_accvgpr_read_b32 v26, a140\n"
    "v_max3_u32 v24, v24, v25, v26\n"
    "v_accvgpr_read_b32 v25, a144\n"
    "v_accvgpr_read_b32 v26, a164\n"
    "v_max3_u32 v24, v24, v25, v26\n"
    "v_accvgpr_read_b32 v25, a168\n"
    "v_accvgpr_read_b32 v26, a172\n"
    "v_max3_u32 v24, v24, v25, v26\n"
    "v_accvgpr_read_b32 v25, a176\n"
    "v_max_u32 v24, v24, v25\n"
    "v_cmp_eq_u32 vcc, -1, v24\n"
    "s_nop 1\n"
    "s_cbranch_vccnz .Lr13_%=\n"
    "s_branch .Ld13_%=\n"
    ".Lr14_%=:\n"
    "s_cmp_eq_u32 s42, 0\n"
    "s_cbranch_scc1 .Ld14_%=\n"
    "s_sub_u32 s42, s42, 1\n"
    "buffer_load_dwordx4 a[180:183], v2, s[16:19], s75 offen sc1\n"
    "buffer_load_dwordx4 a[184:187], v2, s[16:19], s75 offen offset:1024 sc1\n"
    "buffer_load_dwordx4 a[188:191], v2, s[16:19], s75 offen offset:2048 sc1\n"
    "buffer_load_dwordx4 a[192:195], v2, s[16:19], s75 offen offset:3072 sc1\n"
    "buffer_load_dwordx4 a[196:199], v10, s[16:19], s75 offen sc1\n"
    "buffer_load_dwordx4 a[200:203], v10, s[16:19], s75 offen offset:1024 sc1\n"
    "buffer_load_dwordx4 a[204:207], v10, s[16:19], s75 offen offset:2048 sc1\n"
    "buffer_load_dwordx4 a[208:211], v10, s[16:19], s75 offen offset:3072 sc1\n"
    "buffer_load_dwordx4 a[68:71], v9, s[16:19], s75 offen sc1\n"
    "buffer_load_dwordx4 a[72:75], v9, s[16:19], s75 offen offset:1024 sc1\n"
    "buffer_load_dwordx4 a[76:79], v9, s[16:19], s75 offen offset:2048 sc1\n"
    "buffer_load_dwordx4 a[80:83], v9, s[16:19], s75 offen offset:3072 sc1\n"
    "s_waitcnt vmcnt(0)\n"
    "v_accvgpr_read_b32 v24, a180\n"
    "v_accvgpr_read_b32 v25, a184\n"
    "v_accvgpr_read_b32 v26, a188\n"
    "v_max3_u32 v24, v24, v25, v26\n"
    "v_accvgpr_read_b32 v25, a192\n"
    "v_accvgpr_read_b32 v26, a196\n"
    "v_max3_u32 v24, v24, v25, v26\n"
    "v_accvgpr_read_b32 v25, a200\n"
    "v_accvgpr_read_b32 v26, a204\n"
    "v_max3_u32 v24, v24, v25, v26\n"
    "v_accvgpr_read_b32 v25, a208\n"
    "v_accvgpr_read_b32 v26, a68\n"
    "v_max3_u32 v24, v24, v25, v26\n"
    "v_accvgpr_read_b32 v25, a72\n"
    "v_accvgpr_read_b32 v26, a76\n"
    "v_max3_u32 v24, v24, v25, v26\n"
    "v_accvgpr_read_b32 v25, a80\n"
    "v_max_u32 v24, v24, v25\n"
    "v_cmp_eq_u32 vcc, -1, v24\n"
    "s_nop 1\n"
    "s_cbranch_vccnz .Lr14_%=\n"
    "s_branch .Ld14_%=\n"
    ".Lr15_%=:\n"
    "s_cmp_eq_u32 s42, 0\n"
    "s_cbranch_scc1 .Ld15_%=\n"
    "s_sub_u32 s42, s42, 1\n"
    "buffer_load_dwordx4 a[164:167], v2, s[16:19], s72 offen sc1\n"
    "buffer_load_dwordx4 a[168:171], v2, s[16:19], s72 offen offset:1024 sc1\n"
    "buffer_load_dwordx4 a[172:175], v2, s[16:19], s72 offen offset:2048 sc1\n"
    "buffer_load_dwordx4 a[176:179], v2, s[16:19], s72 offen offset:3072 sc1\n"
    "buffer_load_dwordx4 a[180:183], v10, s[16:19], s72 offen sc1\n"
    "buffer_load_dwordx4 a[184:187], v10, s[16:19], s72 offen offset:1024 sc1\n"
    "buffer_load_dwordx4 a[188:191], v10, s[16:19], s72 offen offset:2048 sc1\n"
    "buffer_load_dwordx4 a[192:195], v10, s[16:19], s72 offen offset:3072 sc1\n"
    "buffer_load_dwordx4 a[196:199], v9, s[16:19], s72 offen sc1\n"
    "buffer_load_dwordx4 a[200:203], v9, s[16:19], s72 offen offset:1024 sc1\n"
    "buffer_load_dwordx4 a[204:207], v9, s[16:19], s72 offen offset:2048 sc1\n"
    "buffer_load_dwordx4 a[208:211], v9, s[16:19], s72 offen offset:3072 sc1\n"
    "s_waitcnt vmcnt(0)\n"
    "v_accvgpr_read_b32 v24, a164\n"
    "v_accvgpr_read_b32 v25, a168\n"
    "v_accvgpr_read_b32 v26, a172\n"
    "v_max3_u32 v24, v24, v25, v26\n"
    "v_accvgpr_read_b32 v25, a176\n"
    "v_accvgpr_read_b32 v26, a180\n"
    "v_max3_u32 v24, v24, v25, v26\n"
    "v_accvgpr_read_b32 v25, a184\n"
    "v_accvgpr_read_b32 v26, a188\n"
    "v_max3_u32 v24, v24, v25, v26\n"
    "v_accvgpr_read_b32 v25, a192\n"
    "v_accvgpr_read_b32 v26, a196\n"
    "v_max3_u32 v24, v24, v25, v26\n"
    "v_accvgpr_read_b32 v25, a200\n"
    "v_accvgpr_read_b32 v26, a204\n"
    "v_max3_u32 v24, v24, v25, v26\n"
    "v_accvgpr_read_b32 v25, a208\n"
    "v_max_u32 v24, v24, v25\n"
    "v_cmp_eq_u32 vcc, -1, v24\n"
    "s_nop 1\n"
    "s_cbranch_vccnz .Lr15_%=\n"
    "s_branch .Ld15_%=\n"
    ".Lend_%=:\n"
    :
    : "{s[0:1]}"(__builtin_amdgcn_kernarg_segment_ptr()), "{s2}"(blockIdx.x), "{v0}"(threadIdx.x),
      "{s3}"((unsigned)(size_t)(__attribute__((address_space(3))) char*)lds)
    : "memory", "vcc", "scc", "s4", "s5", "s6", "s7", "s8", "s9", "s10", "s11", "s12", "s13", "s14", "s15", "s16", "s17", "s18", "s19", "s20",
      "s21", "s22", "s23", "s24", "s25", "s26", "s27", "s28", "s29", "s30", "s31", "s33", "s34", "s35", "s36", "s37", "s38", "s39", "s40", "s41",
      "s42", "s43", "s44", "s45", "s46", "s47", "s48", "s49", "s50", "s51", "s52", "s53", "s54", "s55", "s56", "s57", "s58", "s59", "s60", "s61",
      "s62", "s63", "s64", "s65", "s66", "s67", "s68", "s69", "s70", "s71", "s72", "s73", "s74", "s75", "s76", "s77", "s78", "s79", "s80", "s81",
      "s82", "s83", "s84", "s85", "s86", "s87", "s88", "s89", "s90", "s91", "s92", "s93", "s94", "s95", "s96", "s97", "s98", "s99", "v1", "v2",
      "v3", "v4", "v5", "v6", "v7", "v8", "v9", "v10", "v11", "v12", "v13", "v14", "v15", "v16", "v17", "v18", "v19", "v20", "v21", "v22",
      "v23", "v24", "v25", "v26", "v27", "v28", "v29", "v30", "v31", "v32", "v33", "v34", "v35", "v36", "v37", "v38", "v39", "v40", "v41", "v42",
      "v43", "v44", "v45", "v46", "v47", "v48", "v49", "v50", "v51", "v52", "v53", "v54", "v55", "v56", "v57", "v58", "v59", "v60", "v61", "v62",
      "v63", "v64", "v65", "v66", "v67", "v68", "v69", "v70", "v71", "v72", "v73", "v74", "v75", "v76", "v77", "v78", "v79", "v80", "v81", "v82",
      "v83", "v84", "v85", "v86", "v87", "v88", "v89", "v90", "v91", "v92", "v93", "v94", "v95", "v96", "v97", "v98", "v99", "v100", "v101", "v102",
      "v103", "v104", "v105", "v106", "v107", "v108", "v109", "v110", "v111", "v112", "v113", "v114", "v115", "v116", "v117", "v118", "v119", "v120", "v121", "v122",
      "v123", "v124", "v125", "v126", "v127", "v128", "v129", "v130", "v131", "v132", "v133", "v134", "v135", "v136", "v137", "v138", "v139", "v140", "v141", "v142",
      "v143", "v144", "v145", "v146", "v147", "v148", "v149", "v150", "v151", "v152", "v153", "v154", "v155", "v156", "v157", "v158", "v159", "v160", "v161", "v162",
      "v163", "v164", "v165", "v166", "v167", "v168", "v169", "v170", "v171", "v172", "v173", "v174", "v175", "v176", "v177", "v178", "v179", "v180", "v181", "v182",
      "v183", "v184", "v185", "v186", "v187", "v188", "v189", "v190", "v191", "v192", "v193", "v194", "v195", "v196", "v197", "v198", "v199", "v200", "v201", "v202",
      "v203", "v204", "v205", "v206", "v207", "v208", "v209", "v210", "v211", "v212", "v213", "v214", "v215", "v216", "v217", "v218", "v219", "v220", "v221", "v222",
      "v223", "v224", "v225", "v226", "v227", "v228", "v229", "v230", "v231", "v232", "v233", "v234", "v235", "v236", "v237", "v238", "v239", "v240", "v241", "v242",
      "v243", "v244", "v245", "v246", "v247", "v248", "v249", "v250", "v251", "v252", "v253", "v254", "v255", "a0", "a1", "a2", "a3", "a4", "a5", "a6",
      "a7", "a8", "a9", "a10", "a11", "a12", "a13", "a14", "a15", "a16", "a17", "a18", "a19", "a20", "a21", "a22", "a23", "a24", "a25", "a26",
      "a27", "a28", "a29", "a30", "a31", "a32", "a33", "a34", "a35", "a36", "a37", "a38", "a39", "a40", "a41", "a42", "a43", "a44", "a45", "a46",
      "a47", "a48", "a49", "a50", "a51", "a52", "a53", "a54", "a55", "a56", "a57", "a58", "a59", "a60", "a61", "a62", "a63", "a64", "a65", "a66",
      "a67", "a68", "a69", "a70", "a71", "a72", "a73", "a74", "a75", "a76", "a77", "a78", "a79", "a80", "a81", "a82", "a83", "a84", "a85", "a86",
      "a87", "a88", "a89", "a90", "a91", "a92", "a93", "a94", "a95", "a96", "a97", "a98", "a99", "a100", "a101", "a102", "a103", "a104", "a105", "a106",
      "a107", "a108", "a109", "a110", "a111", "a112", "a113", "a114", "a115", "a116", "a117", "a118", "a119", "a120", "a121", "a122", "a123", "a124", "a125", "a126",
      "a127", "a128", "a129", "a130", "a131", "a132", "a133", "a134", "a135", "a136", "a137", "a138", "a139", "a140", "a141", "a142", "a143", "a144", "a145", "a146",
      "a147", "a148", "a149", "a150", "a151", "a152", "a153", "a154", "a155", "a156", "a157", "a158", "a159", "a160", "a161", "a162", "a163", "a164", "a165", "a166",
      "a167", "a168", "a169", "a170", "a171", "a172", "a173", "a174", "a175", "a176", "a177", "a178", "a179", "a180", "a181", "a182", "a183", "a184", "a185", "a186",
      "a187", "a188", "a189", "a190", "a191", "a192", "a193", "a194", "a195", "a196", "a197", "a198", "a199", "a200", "a201", "a202", "a203", "a204", "a205", "a206",
      "a207", "a208", "a209", "a210", "a211", "a212", "a213", "a214", "a215", "a216", "a217", "a218", "a219", "a220", "a221", "a222", "a223", "a224", "a225", "a226",
      "a227", "a228", "a229", "a230", "a231", "a232", "a233", "a234", "a235", "a236", "a237", "a238", "a239", "a240", "a241", "a242", "a243", "a244", "a245", "a246",
      "a247", "a248", "a249", "a250", "a251", "a252", "a253", "a254", "a255");
}
extern "C" void kernel_launch(void* const* d_in, const int* in_sizes, int n_in, void* d_out, int out_size, void* d_ws, size_t ws_size, hipStream_t stream) {
  const unsigned k = 0x20000u;
  Args a = {{d_in[0], 2048u * 1280u, k}, {d_in[7], 640u * 7680u, k}, {d_in[6], 1280u, k}, {(char*)d_ws + 0, 0x1400000u, k}, {d_in[4], 640u * 3072u, k},
            {d_out, 2048u * 1280u, k}, {d_in[1], 640u * 3840u, k}, {d_in[2], 640u * 1280u, k}, {d_in[5], 1536u * 1280u, k}};
  fused_block<<<256, 256, 0, stream>>>(a);
}
```
